```python
import torch

def _softmax(x, axis=-1):
    return torch.softmax(x, dim=axis)
def _gelu(x):
    return torch.nn.functional.gelu(x, approximate="tanh")
def _silu(x):
    return torch.nn.functional.silu(x)
def _sigmoid(x):
    return torch.sigmoid(x)
def _layernorm(x, axis=-1, eps=1e-5):
    m = x.mean(dim=axis, keepdim=True)
    v = x.var(dim=axis, keepdim=True, unbiased=False)
    return (x - m) / torch.sqrt(v + eps)
def _scatter_add(tgt, idx, src, axis=0):
    return tgt.clone().index_add_(axis, idx, src)
def _segment_sum(src, seg, n):
    out = torch.zeros((n,) + src.shape[1:], dtype=src.dtype)
    return out.index_add_(0, seg, src)
def _pad(x, pw):
    flat = tuple(v for pair in reversed(pw) for v in pair)
    return torch.nn.functional.pad(x, flat)

kernel_name = 'gated_mix__sliding_local_dilated__moe_arch_mod__prompt'

def setup_inputs():

    _w = (lambda a: (lambda t: t.to(torch.bfloat16) if t.is_floating_point() else t)(torch.as_tensor(a)))
    def _A_setup():
        B, S, D = 1, 2048, 640
        return {
            "x": _w((lambda *s: torch.randn(*s, dtype=torch.bfloat16) * 0.05)(B, S, D)),
            "wqkv": _w((lambda *s: torch.randn(*s, dtype=torch.bfloat16) * 0.8)(D, 3 * D)),
            "wo": _w((lambda *s: torch.randn(*s, dtype=torch.bfloat16) * 0.05)(D, D)),
        }
    _A_in = _A_setup()

    def _B_setup():
        B, S, D, F, C = 1, 2048, 640, 1728, 2
        return {
            "x": _w((lambda *s: torch.randn(*s, dtype=torch.bfloat16) * 0.05)(B, S, D)),
            "w_r": _w((lambda *s: torch.randn(*s, dtype=torch.bfloat16) * 1.6)(D)),
            "w_gate": _w((lambda *s: torch.randn(*s, dtype=torch.bfloat16) * 0.4)(D, F)),
            "w_up": _w((lambda *s: torch.randn(*s, dtype=torch.bfloat16) * 0.4)(D, F)),
            "w_down": _w((lambda *s: torch.randn(*s, dtype=torch.bfloat16) * 0.4)(F, D)),
        }
    _B_in = _B_setup()

    return {
        "A_x": _A_in["x"],
        "A_wqkv": _A_in["wqkv"],
        "A_wo": _A_in["wo"],
        "B_x": _B_in["x"],
        "B_w_r": _B_in["w_r"],
        "B_w_gate": _B_in["w_gate"],
        "B_w_up": _B_in["w_up"],
        "B_w_down": _B_in["w_down"],
        "gate_w": _w((lambda *s: torch.randn(*s, dtype=torch.bfloat16) * 0.05)(640) * 16.0),
    }

def reference(A_x, A_wqkv, A_wo, B_x, B_w_r, B_w_gate, B_w_up, B_w_down, gate_w):
    def _A_ref(x, wqkv, wo):
        B, S, D = x.shape
        H = 32; Dh = D // H
        W, dil = 8, 2
        qkv = (lambda x, *d: x.permute(*d))((x @ wqkv).reshape(B, S, 3, H, Dh), 2, 0, 3, 1, 4)
        q, k, v = qkv[0], qkv[1], qkv[2]
        scale = Dh ** -0.5
        pos = torch.arange(S)
        off = torch.arange(W) * dil
        idx = pos[:, None] - off[None, :]
        mask = idx >= 0
        idx_c = torch.where(mask, idx, 0).reshape(-1)
        kg = k[:, :, idx_c, :].reshape(B, H, S, W, Dh)
        vg = v[:, :, idx_c, :].reshape(B, H, S, W, Dh)
        logits = torch.einsum("bhsd,bhswd->bhsw", q, kg) * scale
        logits = torch.where(mask[None, None, :, :], logits, -1e9)
        attn = _softmax(logits, axis=-1)
        o = torch.einsum("bhsw,bhswd->bhsd", attn, vg)
        return (lambda x, *d: x.permute(*d))(o, 0, 2, 1, 3).reshape(B, S, D) @ wo

    def _B_ref(x, w_r, w_gate, w_up, w_down):
        B, S, D = x.shape
        C = 2
        r = x @ w_r
        idx = torch.argsort(-r, axis=-1)[:, :C]
        idx_e = (lambda x, n, axis: torch.repeat_interleave(x, n, dim=axis))(idx.reshape(B, C, 1), D, axis=2)
        x_sel = (lambda x, i, axis: torch.take_along_dim(x, i, dim=axis))(x, idx_e, axis=1)
        g = _silu(x_sel @ w_gate)
        u = x_sel @ w_up
        y = (g * u) @ w_down
        r_sel = _sigmoid((lambda x, i, axis: torch.take_along_dim(x, i, dim=axis))(r, idx, axis=1))
        y = y * r_sel.reshape(B, C, 1)
        b_off = (torch.arange(B) * S).reshape(B, 1)
        flat_idx = (idx + b_off).reshape(B * C)
        zero = (x * 0.0).reshape(B * S, D)
        upd = _scatter_add(zero, flat_idx, y.reshape(B * C, D), axis=0)
        return x + upd.reshape(B, S, D)

    x = A_x
    g = _sigmoid((x * gate_w).sum(-1, keepdims=True))
    a = _A_ref(x, A_wqkv, A_wo)
    b = _B_ref(x, B_w_r, B_w_gate, B_w_up, B_w_down)
    return g * a + (1 - g) * b
```

```cpp
#include <hip/hip_runtime.h>
#include <stdint.h>

typedef unsigned short u16;
typedef unsigned long long u64;
typedef __attribute__((ext_vector_type(8))) __bf16 bf16x8;
typedef __attribute__((ext_vector_type(2))) __bf16 bf16x2;
typedef __attribute__((ext_vector_type(4))) short s16x4;
typedef __attribute__((ext_vector_type(4))) float f32x4;
typedef __attribute__((ext_vector_type(2))) float f32x2;
typedef __attribute__((ext_vector_type(4))) unsigned u32x4;
typedef __attribute__((ext_vector_type(2))) unsigned u32x2;
typedef __attribute__((ext_vector_type(10))) unsigned u32x10;
#define GAS __attribute__((address_space(1)))
#define LAS __attribute__((address_space(3)))
typedef GAS unsigned gu32;

#define S_LEN 2048
#define DM 640
#define NQKV 1920
#define FF 1728

#define WS_OF 0
#define WS_WOF 2621440
#define WS_E (WS_WOF + 819200)
#define WS_F2 (WS_E + 256)
#define WS_F (WS_F2 + 32768)
#define WS_K (WS_F + 28672)
#define WS_R (WS_K + 36864)
#define WS_A (WS_R + 256)
#define WS_YP (WS_A + 4096)
#define TAG_STEP 0x9E3779B9u
#define SENT 0xFFFFFFFFu
#define SPIN_CAP (1u << 18)
#define GO_SLEEP 12
#define NPRE 3
#define PUB_IT 1
#define ETA_G 170
#define ETA_F 175
#define D1 350
#define SPOS 10
#define D2 70
#define REARGS 0
#define TMAP 0
#define WPOS 3
#define NPRE2 8
#define CTS 32
#define A1 1
#define A3 2
#define JNT ""
#define XNT ""
#define PNT " nt"
#define XPF 0
#define WB8 2
#define MKA 5
#define WPD 8
#define RLX_AGENT __ATOMIC_RELAXED, __HIP_MEMORY_SCOPE_AGENT

__device__ __forceinline__ float bflo(unsigned u) { return __uint_as_float(u << 16); }
__device__ __forceinline__ float bfhi(unsigned u) { return __uint_as_float(u & 0xFFFF0000u); }
__device__ __forceinline__ unsigned pk2(float a, float b) {
  f32x2 v = {a, b};
  return __builtin_bit_cast(unsigned, __builtin_convertvector(v, bf16x2));
}
__device__ __forceinline__ float rbf(float f) { return bflo(pk2(f, f)); }
__device__ __forceinline__ unsigned perm_lo(unsigned hi_src, unsigned lo_src) {
  return __builtin_amdgcn_perm(hi_src, lo_src, 0x05040100u);
}
__device__ __forceinline__ unsigned perm_hi(unsigned hi_src, unsigned lo_src) {
  return __builtin_amdgcn_perm(hi_src, lo_src, 0x07060302u);
}
__device__ __forceinline__ f32x4 mfma16(u32x4 a, u32x4 b, f32x4 c) {
  return __builtin_amdgcn_mfma_f32_16x16x32_bf16(__builtin_bit_cast(bf16x8, a), __builtin_bit_cast(bf16x8, b), c, 0, 0, 0);
}
__device__ __forceinline__ u32x2 tr_read(const char* p) {
  return __builtin_bit_cast(u32x2, __builtin_amdgcn_ds_read_tr16_b64_v4i16((LAS s16x4*)(p)));
}
#define BARRIER() asm volatile("s_barrier" ::: "memory")
#define WAITV(n) asm volatile("s_waitcnt vmcnt(" #n ")" ::: "memory")
#define WAITV_(n) asm volatile("s_waitcnt vmcnt(%0)" ::"i"(n) : "memory")
#define WAITL0() asm volatile("s_waitcnt lgkmcnt(0)" ::: "memory")

__device__ __forceinline__ void top2_mrg(unsigned& k1, unsigned& k2, unsigned b1, unsigned b2) {
  k2 = max(max(k2, b2), min(k1, b1));
  k1 = max(k1, b1);
}
template <int CTRL>
__device__ __forceinline__ void top2_dpp(unsigned& k1, unsigned& k2) {
  const unsigned b1 = (unsigned)__builtin_amdgcn_update_dpp(0, (int)k1, CTRL, 0xF, 0xF, true);
  const unsigned b2 = (unsigned)__builtin_amdgcn_update_dpp(0, (int)k2, CTRL, 0xF, 0xF, true);
  top2_mrg(k1, k2, b1, b2);
}
__device__ __forceinline__ void top2_row(unsigned& k1, unsigned& k2) {
  top2_dpp<0xB1>(k1, k2);
  top2_dpp<0x4E>(k1, k2);
  top2_dpp<0x141>(k1, k2);
  top2_dpp<0x140>(k1, k2);
}
__device__ __forceinline__ void top2_all(unsigned& k1, unsigned& k2) {
  top2_row(k1, k2);
  {
    auto a = __builtin_amdgcn_permlane16_swap(k1, k1, false, false);
    auto c = __builtin_amdgcn_permlane16_swap(k2, k2, false, false);
    k1 = a[0];
    k2 = c[0];
    top2_mrg(k1, k2, a[1], c[1]);
  }
  {
    auto a = __builtin_amdgcn_permlane32_swap(k1, k1, false, false);
    auto c = __builtin_amdgcn_permlane32_swap(k2, k2, false, false);
    k1 = a[0];
    k2 = c[0];
    top2_mrg(k1, k2, a[1], c[1]);
  }
}
__device__ __forceinline__ void touch_line(const char* p, unsigned lds_off) {
  asm volatile("s_mov_b32 m0, %1\n\ts_nop 0\n\tglobal_load_lds_dword %0, off" ::"v"(p), "s"(lds_off) : "memory");
}
__device__ __forceinline__ void dma16(unsigned voff, const void* sbase, unsigned lds_off) {
  asm volatile("s_mov_b32 m0, %2\n\ts_nop 0\n\tglobal_load_lds_dwordx4 %0, %1" ::"v"(voff), "s"(sbase), "s"(lds_off) : "memory");
}
__device__ __forceinline__ void dma16_sc1(unsigned voff, const void* sbase, unsigned lds_off) {
  asm volatile("s_mov_b32 m0, %2\n\ts_nop 0\n\tglobal_load_lds_dwordx4 %0, %1 sc1" ::"v"(voff), "s"(sbase), "s"(lds_off) : "memory");
}
__device__ __forceinline__ void bdma16(unsigned voff, u32x4 srd, unsigned soff, unsigned lds_off) {
  asm volatile("s_mov_b32 m0, %3\n\ts_nop 0\n\tbuffer_load_dwordx4 %0, %1, %2 offen lds" ::"v"(voff), "s"(srd), "s"(soff), "s"(lds_off) : "memory");
}
__device__ __forceinline__ u32x4 make_srd(const void* p, unsigned bytes) {
  return (u32x4){(unsigned)(uintptr_t)p, (unsigned)((uintptr_t)p >> 32) & 0xFFFFu, bytes, 0x00020000u};
}
__device__ __forceinline__ float key_val(unsigned key) {
  unsigned u = key >> 16;
  u ^= (u & 0x8000u) ? 0x8000u : 0xFFFFu;
  return bflo(u);
}
__device__ __forceinline__ float fmx(float a, float b) { return __builtin_elementwise_maximum(a, b); }
#define SB() __builtin_amdgcn_sched_barrier(0)
__device__ __forceinline__ f32x4 mfma16h(u32x2 a, u32x2 b, f32x4 c) {
  return __builtin_amdgcn_mfma_f32_16x16x16bf16_1k(__builtin_bit_cast(s16x4, a), __builtin_bit_cast(s16x4, b), c, 0, 0, 0);
}

#define SLOT_B 43008
#define L_WO SLOT_B
#define L_EARLY (3 * SLOT_B)
#define L_PIECE L_EARLY
#define L_CAND (L_VAR + 128)
#define L_TOUCH (L_EARLY + 12288)
#define L_JUNK (L_WO + 102400)
#define L_VAR (L_JUNK + 384)
#define L_CAND2 (L_VAR + 48)
#define L_GT (L_VAR + 256)
#define L_FS (L_GT + 1024)
#define L_XT (L_FS + 1024)
#define L_TOTAL (L_XT + 10240)
#define P_STRIDE 336
#define CODE_LINES 246

__device__ __forceinline__ void res_wait(char* ws, char* smem, int lane, unsigned tag, int which) {
  const __amdgpu_buffer_rsrc_t r = __builtin_amdgcn_make_buffer_rsrc((void*)(ws + WS_R), 0, 256, 0x00020000);
  u32x4 v;
#pragma unroll 1
  for (unsigned spins = 0; spins < SPIN_CAP; ++spins) {
    asm volatile("" ::: "memory");
    v = __builtin_amdgcn_raw_buffer_load_b128(r, which * 128, 0, 16);
    if (__all((v[1] == tag) & (v[3] == tag))) break;
    __builtin_amdgcn_s_sleep(2);
  }
  if (lane == 0 && which == 0) *(u32x2*)(smem + L_VAR + 32) = (u32x2){v[0], v[2]};
  if (lane == 0 && which == 1) *(u32x4*)(smem + L_CAND2) = v;
}
__device__ __forceinline__ float dot2(unsigned a, unsigned b, float c) {
  return __builtin_amdgcn_fdot2_f32_bf16(__builtin_bit_cast(bf16x2, a), __builtin_bit_cast(bf16x2, b), c, false);
}
template <int CTRL>
__device__ __forceinline__ float dpp_add(float v) {
  return v + __int_as_float(__builtin_amdgcn_update_dpp(0, __float_as_int(v), CTRL, 0xF, 0xF, true));
}
__device__ __forceinline__ float row_sum(float v) {
  v = dpp_add<0xB1>(v);
  v = dpp_add<0x4E>(v);
  v = dpp_add<0x141>(v);
  return dpp_add<0x140>(v);
}

template <int N> struct IC { static constexpr int value = N; };
#define PIN(v) asm volatile("" : "+v"(v))
__global__ __launch_bounds__(512, 2) void kf(const u16*, const u16*, const u16*, const u16*, const u16*, const u16*, const u16*, const u16*, char*, u16*) {
  __shared__ __attribute__((aligned(16))) char smem[L_TOTAL];
  typedef const __attribute__((address_space(4))) u64* karg_t;
  const karg_t kp0 = (karg_t)__builtin_amdgcn_kernarg_segment_ptr();
  u64 pc;
  asm volatile("s_load_dword s100, %1, 0x0\n\ts_load_dword s101, %1, 0x40\n\ts_getpc_b64 %0" : "=s"(pc) : "s"(kp0) : "s100", "s101");
  const u16 *x, *wqkv, *wo, *w_r, *gate_w, *w_gate, *w_up, *w_down;
  char* ws;
  u16* out;
  auto args = [&]() {
    SB();
    karg_t kp = kp0;
    asm volatile("" : "+s"(kp));
    x = (const u16*)kp[0]; wqkv = (const u16*)kp[1]; wo = (const u16*)kp[2]; w_r = (const u16*)kp[3]; gate_w = (const u16*)kp[4];
    w_gate = (const u16*)kp[5]; w_up = (const u16*)kp[6]; w_down = (const u16*)kp[7]; ws = (char*)kp[8]; out = (u16*)kp[9];
  };
  const int tid = threadIdx.x, b = blockIdx.x;
  {
    const int ln = ((b >> 3) & (CTS - 1)) + CTS * tid;
    if (ln < CODE_LINES)
      touch_line((const char*)((pc & ~255ull) + ln * 128), (unsigned)(uintptr_t)(LAS char*)smem + L_TOUCH);
  }
  unsigned tag;
  auto get_tag = [&]() {
    asm volatile("s_load_dword %0, %1, 0x0\n\ts_waitcnt lgkmcnt(0)" : "=s"(tag) : "s"(ws + WS_E) : "memory");
    tag += TAG_STEP;
  };
  const int lane = tid & 63;
  const int wave = __builtin_amdgcn_readfirstlane(tid >> 6);
  const int xcd = b & 7, jj = b >> 3;
  const int c15 = lane & 15, g4 = lane >> 4;
  const bool gemm_blk = jj < 28;
  const int q_ = (xcd & 3) * 28 + jj;
  const int rg = q_ >> 2, hg = (xcd >> 2) * 4 + (q_ & 3);
  const int par = rg >= 14, gi = rg - par * 14;
  const int tb = min(73 * gi, 944);
  const int tlo = gi == 0 ? 0 : 73 * gi + 7;
  const int lg = jj >> 2;
  const bool keep = !TMAP || (lg & 3) == 3;
  const int gc = keep ? gi : (xcd & 1) * 7 + (lg & 3) + (xcd & 4), tbc = min(73 * gc, 944);
  const int rt2 = gemm_blk ? par * 16 + gc : (xcd & 2) * 8 + 14 + (xcd & 1), nt2 = keep ? hg : (lg & 4) + (jj & 3);
  const unsigned lds0 = (unsigned)(uintptr_t)(LAS char*)smem;
  auto wo_dma = [&](auto n_, int i0, int st, int lo) {
    const u32x4 srd = make_srd(ws + WS_WOF + nt2 * 102400, 102400);
    int g = __builtin_amdgcn_readfirstlane((i0 + (TMAP ? ((lg & 3) * (25 - lo)) >> 2 : (lg * (25 - lo)) >> 3)) * 4096);
#pragma unroll
    for (int i = 0; i < decltype(n_)::value; ++i) {
      if (g >= 102400) g -= (25 - lo) * 4096;
      asm volatile("s_add_u32 m0, %3, %2\n\ts_nop 0\n\tbuffer_load_dwordx4 %0, %1, %2 offen sc1 lds\n\t"
                   "buffer_load_dwordx4 %0, %1, %2 offen offset:1024 sc1 lds\n\tbuffer_load_dwordx4 %0, %1, %2 offen offset:2048 sc1 lds\n\t"
                   "buffer_load_dwordx4 %0, %1, %2 offen offset:3072 sc1 lds" ::"v"(lane * 16), "s"(srd), "s"(g), "s"(lds0 + L_WO)
                   : "memory", "scc");
      g += st * 4096;
    }
  };

  int od[5];
  const int pa = tb + c15 - 64 * gi;
  const bool pv = tb + c15 >= tlo;
  auto prepO = [&]() {
    const int h = hg * 4 + (wave & 3);
    const int b0 = (par * 64 + 56 + (pa >> 4)) * 20480, b1 = (par * 64 + gi * 4) * 20480;
    od[0] = (pv && lane < 32) ? b0 + (2 * h + g4) * 256 + (pa & 15) * 16 : 0x7FFFFF00;
    od[1] = b1 + (lane >> 5) * 20480 + (2 * h + (g4 & 1)) * 256 + c15 * 16;
    od[2] = od[1] + 2 * 20480;
    od[3] = b1 + g4 * 20480 + 16384 + h * 128 + c15 * 8;
    od[4] = (pv && g4 == 3) ? b0 + 16384 + h * 128 + (pa & 15) * 8 : 0x7FFFFF00;
#pragma unroll
    for (int e = 0; e < 5; ++e) PIN(od[e]);
  };
  auto row_t = [&](int row) {
    const int a = (xcd & 1) * 64 + row;
    return gemm_blk ? tbc + 16 + row : a + (a < 16 ? 0 : (a - 7) / 9) * 64;
  };
  int xoff[2], prd[2], lrow[2], pwr, glo;
  u64 rel;
  auto chunk_off = [&](int row, int c8) { return ((2 * row_t(row) + (rt2 >> 4)) * DM + nt2 * 80 + c8 * 8) * 2; };
  auto prepB = [&]() {
#pragma unroll
    for (int e = 0; e < 2; ++e) {
      const int task = (e == 0 || tid < 128) ? tid + 512 * e : tid;
      lrow[e] = task / 10;
      const int c8 = task - lrow[e] * 10;
      xoff[e] = chunk_off(lrow[e], c8);
      prd[e] = lrow[e] * P_STRIDE + c8 * 32;
      PIN(lrow[e]); PIN(xoff[e]); PIN(prd[e]);
    }
    pwr = (wave * 16 + c15) * P_STRIDE + g4 * 16;
    PIN(pwr);
    glo = L_GT + lrow[0] * 16 + (((gemm_blk ? tbc : 0) + lrow[0]) & 1) * 8;
    PIN(glo);
    {
      const int a0 = (xcd & 1) * 64 + (wave & 3) * 16, gl = (xcd & 1) ? 6 + (lane >> 3) : min(lane >> 3, 6);
      rel = __builtin_amdgcn_ballot_w64(((lane >> 2) & 1) == (wave >> 2) && (gemm_blk ? lane < 8 : gl >= (a0 < 16 ? 0 : (a0 - 7) / 9) && gl <= (a0 + 8) / 9));
      asm volatile("" : "+s"(rel));
    }
  };
  auto lrow_of = [&](unsigned key) {
    const int i = 2047 - (int)(key & 0xFFFFu);
    const int t = i >> 1, g = max(t - 7, 0) / 73, rho = t - min(73 * g, 944);
    return (i & 1) == (rt2 >> 4) ? (rho >= 16 ? g * 64 + rho - 16 : 896 + t - 64 * g) - (rt2 & 15) * 64 : -1;
  };
  auto rows_job = [&]() {
    if (lane < 2) *(int*)(smem + L_VAR + 40 + lane * 4) = lrow_of(*(const unsigned*)(smem + L_VAR + 32 + lane * 4));
  };
  auto gate_off = [&](int row) {
    const int p2 = rt2 >> 4, tg = row_t(row);
    return (tg < 896 ? (p2 * 112 + (tg >> 3)) * 128 + (tg & 7) * 8 : (224 + p2 * 32 + ((tg - 896) >> 2)) * 128 + (tg & 3) * 8) + 64;
  };
  auto flag_off = [&]() {
    const int g = gemm_blk ? gc : (xcd & 1) ? 6 + (lane >> 3) : min(lane >> 3, 6);
    return (((rt2 >> 4) * 14 + g) * 8 + (lane & 7)) * 128 + 56;
  };
#define rfl __builtin_amdgcn_make_buffer_rsrc((void*)(ws + WS_F), 0, 28672, 0x00020000)
  auto gate_dma = [&](int go) { dma16_sc1(go & ~15, ws + WS_K, lds0 + L_GT); };
  auto gate_lds = [&](int row) { return *(const u32x2*)(smem + L_GT + row * 16 + (((gemm_blk ? tbc : 0) + row) & 1) * 8); };
  auto now = []() { return (unsigned)__builtin_amdgcn_s_memrealtime(); };
  auto flag_wait = [&](int foff) {
#pragma unroll 1
    for (unsigned spins = 0; spins < SPIN_CAP; ++spins) {
      asm volatile("" ::: "memory");
      const u32x4 v = __builtin_amdgcn_raw_buffer_load_b128(rfl, foff, 0, 16);
      if ((v[1] == tag) & (v[3] == tag) & (foff != 0x7FFFFF00)) {
        *(u32x4*)(smem + L_FS + lane * 16) = v;
        foff = 0x7FFFFF00;
      }
      if (__all(foff == 0x7FFFFF00)) break;
      __builtin_amdgcn_s_sleep(8);
    }
  };
#define ro __builtin_amdgcn_make_buffer_rsrc((void*)(ws + WS_OF), 0, S_LEN * DM * 2, 0x00020000)

  if (__builtin_expect(gemm_blk, 1)) {
    if (__builtin_expect(wave >= 4, 1)) {
      const int hw = wave - 4;
      const int rot = ((jj >> 2) % 5) * 2 + (xcd & 1);
      int voffA = (lane >> 3) * (DM * 4) + (((lane & 7) ^ (((hw & 1) * 4 + g4) & 7)) * 16);
      int sA[3];
#pragma unroll
      for (int v = 0; v < 3; ++v) {
        sA[v] = ((tb + 8 * (hw + 4 * v)) * 2 + par) * (DM * 2);
      }
      const int hi = lane >> 5;
      int pair = ((lane & 31) >> 1) ^ (hi | ((hw & 1) << 1) | ((hw >> 1) << 2));
      const int sec = (pair >= 5) + (pair >= 10);
      int voffW = pair == 15 ? 0x7FFFFF00 : hi * (NQKV * 2) + sec * (1280 - 160) + hg * 160 + pair * 32 + ((lane & 1) ^ (sec == 2)) * 16;
      const int n0 = (hw & 1) + 4 * (hw >> 1);
      PIN(voffA); PIN(voffW);
      args();
      const u32x4 rsx = make_srd(x, S_LEN * DM * 2), rsw = make_srd(wqkv, DM * NQKV * 2);
      auto issue = [&](int i, int buf) {
        int kc = rot + (i < 5 ? 2 * i : 2 * i - 9);
        if (kc >= 10) kc -= 10;
        const unsigned dst = lds0 + buf * SLOT_B;
#pragma unroll
        for (int v = 0; v < 3; ++v)
          if (v < 2 || hw < 2) bdma16(voffA, rsx, sA[v] + kc * 128, dst + (hw + 4 * v) * 1024);
        const int so = (kc * 64 + 2 * n0) * (NQKV * 2);
#pragma unroll
        for (int u = 0; u < 8; ++u) {
          const int dn = 2 * (u & 1) + 8 * (u >> 1);
          bdma16(voffW, rsw, so + dn * 2 * (NQKV * 2), dst + 10240 + (n0 + dn) * 1024);
        }
      };
      const int qp = rg * 8 + hg;
      issue(0, 0);
      SB();
      int md = (par * 64 + gi * 4 + 2 * (hw & 1) + (lane >> 5)) * 20480 + 16384 + hg * 512 + (lane & 31) * 16;
      PIN(md);
      int so_ = hw == 0 ? flag_off() : gate_off(lane);
      PIN(so_);
      prepB();
      __builtin_amdgcn_sched_barrier(0);
      issue(1, 1);
      get_tag();
      WAITV(10);
      BARRIER();
      issue(2, 2);
      WAITV(10);
      BARRIER();
      {
        int buf = 0;
#pragma unroll 1
        for (int i = 3; i < 9; ++i) {
          const bool ex = i == PUB_IT + 3 && hw == 2;
          if (ex && lane == 0) __hip_atomic_store((gu32*)(ws + WS_F2 + b * 128), tag, RLX_AGENT);
          issue(i, buf);
          if (ex) WAITV(11); else WAITV(10);
          buf = buf == 2 ? 0 : buf + 1;
          BARRIER();
        }
      }
      if (hw == 3) {
        if (lane < 2) dma16_sc1(lane * 128, ws + WS_R, lds0 + L_CAND);
        if (lane < 4) *(unsigned*)(smem + L_VAR + lane * 4) = 0u;
      }
      issue(9, 0);
      auto mark = [&](int e0, int e1) {
        const u32x4 sent4 = {SENT, SENT, SENT, SENT};
        const int mo_ = (par * 64 + gi * 4 + (hw & 1) * 2) * 20480 + hg * 2048;
#pragma unroll
        for (int e = e0; e < e1; ++e) {
          if (e == 0) __builtin_amdgcn_raw_buffer_store_b128(sent4, ro, md, 0, 16);
          else __builtin_amdgcn_raw_buffer_store_b128(sent4, ro, lane * 16 + ((e - 1) & 1) * 1024, mo_ + ((e - 1) >> 1) * 20480, 16);
        }
        SB();
      };
      auto words = [&]() {
        const u32x4 rv = *(const u32x4*)(smem + L_CAND + (lane & 1) * 16);
        if (__all((rv[1] == tag) & (rv[3] == tag))) {
          if (tid == 256) *(u32x2*)(smem + L_VAR + 32) = (u32x2){rv[0], rv[2]};
        } else {
          res_wait(ws, smem, lane, tag, 0);
          res_wait(ws, smem, lane, tag, 1);
        }
      };
      if (hw >= 2) {
        mark(0, MKA);
        WAITV_(10 + MKA);
        BARRIER();
        mark(MKA, 5);
        WAITV(5);
        BARRIER();
        const unsigned t0_ = now();
        WAITV(0);
        __builtin_amdgcn_raw_buffer_store_b128((u32x4){t0_, tag, t0_, tag}, rfl, lane < 4 ? qp * 128 + (hw & 1) * 64 + lane * 16 : 0x7FFFFF00, 0, 16);
        if (hw == 2) rows_job();
      } else {
        WAITV(10);
        BARRIER();
        words();
        wo_dma(IC<WB8>{}, hw, 2, 25);
        WAITV_(4 * WB8);
        WAITL0();
        BARRIER();
        if (hw == 1) gate_dma(so_);
        wo_dma(IC<SPOS>{}, 2 * WB8 + hw, 2, 2 * WB8);
        if (hw == 0 && lane < 8) dma16_sc1(so_, ws + WS_F, lds0 + L_FS);
        wo_dma(IC<12 - WB8 - SPOS>{}, 2 * (WB8 + SPOS) + hw, 2, 2 * WB8);
        if (hw == 0) wo_dma(IC<1>{}, 24, 2, 2 * WB8);
        WAITV(0);
      }
      WAITL0();
      BARRIER();
    } else {
      const bool wjob = wave < 2 || (wave == 2 && b < 64);
      const int tj0 = wave < 2 ? gi * 64 + hg * 8 + wave * 4 : 896 + (b & 31) * 4, pj = wave < 2 ? par : b >> 5;
      u32x4 xs[5], wv[4];
      int jo0 = lane * 16, jo1 = g4 * 2560 + c15 * 16, jo2 = c15 * 16;
      PIN(jo0); PIN(jo1); PIN(jo2);
      args();
      if (wjob) {
        const u16* xr = x + (2 * tj0 + pj) * DM;
        asm volatile("global_load_dwordx4 %0, %9, %12" JNT "\n\tglobal_load_dwordx4 %1, %9, %12 offset:2560" JNT "\n\t"
                     "global_load_dwordx4 %2, %9, %13" JNT "\n\tglobal_load_dwordx4 %3, %9, %13 offset:2560" JNT "\n\t"
                     "global_load_dwordx4 %4, %10, %12 offset:1024" JNT "\n\tglobal_load_dwordx4 %5, %9, %14\n\t"
                     "global_load_dwordx4 %6, %11, %14 offset:1024\n\tglobal_load_dwordx4 %7, %9, %15\n\tglobal_load_dwordx4 %8, %11, %15 offset:1024"
                     : "=&v"(xs[0]), "=&v"(xs[1]), "=&v"(xs[2]), "=&v"(xs[3]), "=&v"(xs[4]), "=&v"(wv[0]), "=&v"(wv[1]), "=&v"(wv[2]), "=&v"(wv[3])
                     : "v"(jo0), "v"(jo1), "v"(jo2), "s"(xr), "s"(xr + 4 * DM), "s"(w_r), "s"(gate_w)
                     : "memory");
      }
      f32x4 acc[5][4];
#pragma unroll
      for (int m = 0; m < 5; ++m)
#pragma unroll
        for (int f = 0; f < 4; ++f) acc[m][f] = (f32x4){0.f, 0.f, 0.f, 0.f};
      const int wks = b % 20, wT = (b / 20) * 4 + wave;
      if (b < 200) {
        const int kh = wks >= 10, s = wks - kh * 10, pr = lane >> 3, g = wave, hi = pr >> 2;
        const int row = (s < 8 ? (2 * (kh * 8 + s) + (g >> 1)) * 20 + 4 * (g & 1) + 8 * hi : (kh * 16 + (s - 8) * 8 + hi * 4 + g) * 20 + 16) + (pr & 3);
        asm volatile("s_mov_b32 m0, %2\n\ts_nop 0\n\tglobal_load_lds_dwordx4 %0, %1" PNT ::"v"((row * DM + (b / 20) * 64 + (lane & 7) * 8) * 2), "s"(wo), "s"(lds0 + L_PIECE + wave * 1024) : "memory");
      }
      const __amdgpu_buffer_rsrc_t rk = __builtin_amdgcn_make_buffer_rsrc((void*)(ws + WS_K), 0, 36864, 0x00020000);
      const int ko = c15 != 0 ? 0x7FFFFF00 : wave < 2 ? (rg * 8 + hg) * 128 + (wave * 4 + g4) * 8 : (224 + b) * 128 + g4 * 8;
      auto rows4 = [&](float (&p)[5]) {
        const auto a = __builtin_amdgcn_permlane32_swap(__float_as_uint(p[0]), __float_as_uint(p[2]), false, false);
        const auto c = __builtin_amdgcn_permlane32_swap(__float_as_uint(p[1]), __float_as_uint(p[3]), false, false);
        const auto e = __builtin_amdgcn_permlane16_swap(__float_as_uint(__uint_as_float(a[0]) + __uint_as_float(a[1])),
                                                        __float_as_uint(__uint_as_float(c[0]) + __uint_as_float(c[1])), false, false);
        return row_sum(__uint_as_float(e[0]) + __uint_as_float(e[1]) + p[4]);
      };
      get_tag();
      if (wjob) {
        if (b < 200) WAITV_(3 + XPF); else WAITV_(2 + XPF);
#pragma unroll
        for (int i = 0; i < 5; ++i) PIN(xs[i]);
        PIN(wv[0]); PIN(wv[1]);
        float p[5];
#pragma unroll
        for (int i = 0; i < 5; ++i) {
          p[i] = 0.f;
#pragma unroll
          for (int q = 0; q < 4; ++q) p[i] = dot2(xs[i][q], wv[i == 4][q], p[i]);
        }
        const float r_ = rows4(p);
        unsigned u = pk2(r_, r_) & 0xFFFFu;
        u ^= (u & 0x8000u) ? 0xFFFFu : 0x8000u;
        const unsigned key = (u << 16) | (unsigned)(2047 - (2 * (tj0 + g4) + pj));
        __builtin_amdgcn_raw_buffer_store_b64((u32x2){key, tag}, rk, ko, 0, 16);
        SB();
      }
      if (wjob) {
        WAITV(1);
        PIN(wv[2]); PIN(wv[3]);
        float p[5];
#pragma unroll
        for (int i = 0; i < 5; ++i) {
          p[i] = 0.f;
#pragma unroll
          for (int q = 0; q < 4; ++q) {
            const unsigned gw = wv[2 + (i == 4)][q];
            p[i] = dot2(pk2(bflo(xs[i][q]) * bflo(gw), bfhi(xs[i][q]) * bfhi(gw)), 0x3F803F80u, p[i]);
          }
        }
        const float g_ = rbf(rows4(p));
        __builtin_amdgcn_raw_buffer_store_b64((u32x2){__float_as_uint(rbf(1.0f / (1.0f + __expf(-g_)))), tag}, rk, c15 != 0 ? ko : ko + 64, 0, 16);
        SB();
      }
      prepO();
      const int p4 = lane & 3;
      f32x4 limA, limB;
      const int e0 = c15 - 4 * g4;
#pragma unroll
      for (int i = 0; i < 4; ++i) {
        limA[i] = e0 - i <= -9 ? 0.f : -__builtin_inff();
        limB[i] = (e0 - i >= 0 && e0 - i <= 7) ? 0.f : -__builtin_inff();
      }
      unsigned mA01 = (e0 <= -9 ? 0xFFFFu : 0u) | (e0 - 1 <= -9 ? 0xFFFF0000u : 0u);
      unsigned mA23 = (e0 - 2 <= -9 ? 0xFFFFu : 0u) | (e0 - 3 <= -9 ? 0xFFFF0000u : 0u);
      PIN(limA); PIN(limB); PIN(mA01); PIN(mA23);
      const unsigned mA[2] = {mA01, mA23};
      u32x4 ones = {0x3F803F80u, 0x3F803F80u, 0x3F803F80u, 0x3F803F80u};
      PIN(ones);
      u32x2 sel[4], kz[5];
#pragma unroll
      for (int t = 0; t < 4; ++t) {
        const bool on = g4 == 2 && (((c15 >> 2) - t + 1) & 3) < 2;
        sel[t] = (u32x2){on && p4 < 2 ? 0x3F80u << (16 * p4) : 0u, on && p4 >= 2 ? 0x3F80u << (16 * (p4 - 2)) : 0u};
        PIN(sel[t]);
      }
#pragma unroll
      for (int t = 0; t < 5; ++t) {
        kz[t] = (u32x2){0u, 0u};
        PIN(kz[t]);
      }
      const int swz = (c15 >> 1) & 7;
      const int aoff0 = c15 * 128 + ((g4 ^ swz) * 16);
      const int aoff1 = c15 * 128 + (((4 + g4) ^ swz) * 16);
      int woff[4];
      {
        const int q4 = c15 >> 2, sw = q4 | ((g4 & 1) << 2);
#pragma unroll
        for (int t = 0; t < 4; ++t) {
          const int sc = t < 3 ? t : p4;
          const int by = wave * 40 + (t < 3 ? p4 : 4) * 8;
          woff[t] = 10240 + (8 * g4 + q4) * 512 + (((sc == 3 ? 15 : sc * 5 + (by >> 5)) ^ sw) * 32) + ((by & 31) ^ (sc == 2 ? 16 : sc == 3 ? 8 : 0));
          PIN(woff[t]);
        }
      }
      int wo3[2][4], ao3[2][2];
#pragma unroll
      for (int q = 0; q < 2; ++q) {
#pragma unroll
        for (int j = 0; j < 4; ++j) {
          wo3[q][j] = woff[j] + (q + 1) * SLOT_B;
          PIN(wo3[q][j]);
        }
        ao3[q][0] = aoff0 + (q + 1) * SLOT_B;
        ao3[q][1] = aoff1 + (q + 1) * SLOT_B;
        PIN(ao3[q][0]); PIN(ao3[q][1]);
      }
#define GM(m, f, wf, af) acc[m][f] = (f) == 2 ? mfma16(af[m], wf[f], acc[m][f]) : mfma16(wf[f], af[m], acc[m][f])
      u32x4 af0[5], wf0[4], af1[5], wf1[4];
      auto rd1 = [&](u32x4(&an)[5], u32x4(&wn)[4], int q, int h, int r) {
        const int ao = q ? ao3[q - 1][h] : h ? aoff1 : aoff0;
        if (r == 2) {
          an[0] = *(const u32x4*)(smem + ao);
        } else if (r >= 9) {
          an[r - 8] = *(const u32x4*)(smem + ao + (r - 8) * 2048);
        } else {
          const int k = r < 2 ? r : r - 1;
          const u32x2 t = tr_read(smem + (q ? wo3[q - 1][k >> 1] : woff[k >> 1]) + h * 16384 + (k & 1) * 2048);
          wn[k >> 1][(k & 1) * 2] = t[0];
          wn[k >> 1][(k & 1) * 2 + 1] = t[1];
        }
      };
      auto half = [&](const u32x4(&a)[5], const u32x4(&w)[4], u32x4(&an)[5], u32x4(&wn)[4], int q, int h) {
#pragma unroll
        for (int i = 0; i < 20; ++i) {
          GM(i >> 2, i & 3, w, a);
          SB();
          if (i < 13) rd1(an, wn, q, h, i);
          SB();
        }
      };
      if (b < 200) {
        if (!wjob) WAITV(0);
        const __amdgpu_buffer_rsrc_t r = __builtin_amdgcn_make_buffer_rsrc((void*)(ws + WS_WOF), 0, 819200, 0x00020000);
        const char* tp = smem + L_PIECE + wave * 1024 + (c15 >> 2) * 128 + g4 * 32 + (lane & 3) * 8;
        const u32x2 lo = tr_read(tp), hi = tr_read(tp + 512);
        const int T = (b / 20) * 4 + g4;
        __builtin_amdgcn_raw_buffer_store_b128((u32x4){lo[0], lo[1], hi[0], hi[1]}, r, (((T / 5) * 20 + wks) * 5 + T % 5) * 1024 + (wave * 16 + c15) * 16, 0, 16);
      }
      WAITL0();
      BARRIER();
#pragma unroll
      for (int r = 0; r < 13; ++r) rd1(af0, wf0, 0, 0, r);
      SB();
#pragma unroll
      for (int i = 0; i < 9; ++i) {
        half(af0, wf0, af1, wf1, i % 3, 1);
        if (i == PUB_IT) WAITV(0);
        if (i == 6) prepB();
        SB();
        WAITL0();
        BARRIER();
        if (i < 8) half(af1, wf1, af0, wf0, (i + 1) % 3, 0);
      }
      u32x4 wf2[4], af2[5];
      u32x4 qf[5], kf[5];
      u32x10 vb, tb_;
      u32x4 pf[5], os[3];
      u32x2 pp[5], ob[6], obT, obU;
      f32x4 l[5], sA[5], oA[5], sm[5], tx[5], oT[3];
      f32x2 ar[5][2], nn0[2], nn1[2], nn2[2], nn3[2], nn4[2], nnT[2], nnU[2];
      float mx[6], inv[5], it, iu;
      constexpr u64 mo = 0xFFFF0000FFFF0000ull, mh = 0xFFFFFFFF00000000ull;
      auto selm = [](u64 m_, float a, float c) { return __builtin_amdgcn_inverse_ballot_w64(m_) ? a : c; };
      const float cs = 0.22360679774997896f * 1.4426950408889634f;
      const f32x2 c2 = {cs, cs};
      const f32x4 zero4 = {0.f, 0.f, 0.f, 0.f};
      auto cpy = [](float v) { PIN(v); return v; };
      auto cpyu = [](unsigned v) { PIN(v); return v; };
      auto mulo = [](float v, float c) { PIN(v); return v * c; };
#define XBAR() BARRIER()
      GM(0, 3, wf1, af1); SB();
      af0[0] = *(const u32x4*)(smem + aoff0 + 0); SB();
      { const u32x2 t_ = tr_read(smem + woff[3] + 0); wf0[3][0] = t_[0]; wf0[3][1] = t_[1]; } SB();
      GM(0, 1, wf1, af1); SB();
      { const u32x2 t_ = tr_read(smem + woff[3] + 2048); wf0[3][2] = t_[0]; wf0[3][3] = t_[1]; } SB();
      { const u32x2 t_ = tr_read(smem + woff[1] + 0); wf0[1][0] = t_[0]; wf0[1][1] = t_[1]; } SB();
      GM(0, 0, wf1, af1); SB();
      { const u32x2 t_ = tr_read(smem + woff[1] + 2048); wf0[1][2] = t_[0]; wf0[1][3] = t_[1]; } SB();
      { const u32x2 t_ = tr_read(smem + woff[0] + 0); wf0[0][0] = t_[0]; wf0[0][1] = t_[1]; } SB();
      GM(0, 2, wf1, af1); SB();
      { const u32x2 t_ = tr_read(smem + woff[0] + 2048); wf0[0][2] = t_[0]; wf0[0][3] = t_[1]; } SB();
      af2[0] = *(const u32x4*)(smem + aoff1 + 0); SB();
      GM(1, 3, wf1, af1); SB();
      { const u32x2 t_ = tr_read(smem + woff[2] + 0); wf0[2][0] = t_[0]; wf0[2][1] = t_[1]; } SB();
      { const u32x2 t_ = tr_read(smem + woff[3] + 16384); wf2[3][0] = t_[0]; wf2[3][1] = t_[1]; } SB();
      GM(1, 1, wf1, af1); SB();
      { const u32x2 t_ = tr_read(smem + woff[2] + 2048); wf0[2][2] = t_[0]; wf0[2][3] = t_[1]; } SB();
      { const u32x2 t_ = tr_read(smem + woff[3] + 18432); wf2[3][2] = t_[0]; wf2[3][3] = t_[1]; } SB();
      GM(1, 0, wf1, af1); SB();
      { const u32x2 t_ = tr_read(smem + woff[1] + 16384); wf2[1][0] = t_[0]; wf2[1][1] = t_[1]; } SB();
      { const u32x2 t_ = tr_read(smem + woff[1] + 18432); wf2[1][2] = t_[0]; wf2[1][3] = t_[1]; } SB();
      GM(1, 2, wf1, af1); SB();
      { const u32x2 t_ = tr_read(smem + woff[0] + 16384); wf2[0][0] = t_[0]; wf2[0][1] = t_[1]; } SB();
      { const u32x2 t_ = tr_read(smem + woff[0] + 18432); wf2[0][2] = t_[0]; wf2[0][3] = t_[1]; } SB();
      GM(2, 3, wf1, af1); SB();
      af0[1] = *(const u32x4*)(smem + aoff0 + 2048); SB();
      { const u32x2 t_ = tr_read(smem + woff[2] + 16384); wf2[2][0] = t_[0]; wf2[2][1] = t_[1]; } SB();
      GM(2, 1, wf1, af1); SB();
      af2[1] = *(const u32x4*)(smem + aoff1 + 2048); SB();
      { const u32x2 t_ = tr_read(smem + woff[2] + 18432); wf2[2][2] = t_[0]; wf2[2][3] = t_[1]; } SB();
      GM(2, 0, wf1, af1); SB();
      af0[2] = *(const u32x4*)(smem + aoff0 + 4096); SB();
      af2[2] = *(const u32x4*)(smem + aoff1 + 4096); SB();
      GM(2, 2, wf1, af1); SB();
      af0[3] = *(const u32x4*)(smem + aoff0 + 6144); SB();
      af2[3] = *(const u32x4*)(smem + aoff1 + 6144); SB();
      GM(0, 3, wf0, af0); SB();
      af0[4] = *(const u32x4*)(smem + aoff0 + 8192); SB();
      af2[4] = *(const u32x4*)(smem + aoff1 + 8192); SB();
      GM(0, 1, wf0, af0); SB();
      GM(0, 0, wf0, af0); SB();
      GM(0, 2, wf0, af0); SB();
      GM(0, 3, wf2, af2); SB();
      GM(0, 1, wf2, af2); SB();
      GM(0, 0, wf2, af2); SB();
      GM(0, 2, wf2, af2); SB();
      GM(1, 3, wf0, af0); SB();
      GM(1, 1, wf0, af0); SB();
      GM(1, 0, wf0, af0); SB();
      GM(1, 2, wf0, af0); SB();
      qf[0][2] = pk2(acc[0][3][0], acc[0][3][1]); SB();
      GM(1, 3, wf2, af2); SB();
      qf[0][3] = pk2(acc[0][3][2], acc[0][3][3]); SB();
      { const auto r_ = __builtin_amdgcn_permlane16_swap(qf[0][2], kz[0][0], false, false); qf[0][2] = r_[0]; kf[0][2] = r_[1]; } SB();
      GM(1, 1, wf2, af2); SB();
      kf[0][0] = pk2(acc[0][1][0], acc[0][1][1]); SB();
      { const auto r_ = __builtin_amdgcn_permlane16_swap(qf[0][3], kz[0][1], false, false); qf[0][3] = r_[0]; kf[0][3] = r_[1]; } SB();
      GM(1, 0, wf2, af2); SB();
      kf[0][1] = pk2(acc[0][1][2], acc[0][1][3]); SB();
      qf[0][0] = pk2(acc[0][0][0], acc[0][0][1]); SB();
      tx[0] = mfma16h((u32x2){qf[0][2], qf[0][3]}, sel[0], zero4); SB();
      qf[1][2] = pk2(acc[1][3][0], acc[1][3][1]); SB();
      qf[1][3] = pk2(acc[1][3][2], acc[1][3][3]); SB();
      GM(1, 2, wf2, af2); SB();
      { const auto r_ = __builtin_amdgcn_permlane16_swap(qf[1][2], kz[1][0], false, false); qf[1][2] = r_[0]; kf[1][2] = r_[1]; } SB();
      kf[1][0] = pk2(acc[1][1][0], acc[1][1][1]); SB();
      GM(2, 3, wf0, af0); SB();
      qf[1][0] = pk2(acc[1][0][0], acc[1][0][1]); SB();
      { const auto r_ = __builtin_amdgcn_permlane16_swap(qf[1][3], kz[1][1], false, false); qf[1][3] = r_[0]; kf[1][3] = r_[1]; } SB();
      GM(2, 1, wf0, af0); SB();
      kf[1][1] = pk2(acc[1][1][2], acc[1][1][3]); SB();
      qf[1][1] = pk2(acc[1][0][2], acc[1][0][3]); SB();
      tx[1] = mfma16h((u32x2){qf[1][2], qf[1][3]}, sel[1], zero4); SB();
      qf[0][1] = pk2(acc[0][0][2], acc[0][0][3]); SB();
      tb_[0] = pk2(tx[0][0], tx[0][1]); SB();
      l[1] = mfma16(kf[1], qf[1], limB); SB();
      tb_[1] = pk2(tx[0][2], tx[0][3]); SB();
      vb[0] = pk2(acc[0][2][0], acc[0][2][1]); SB();
      sA[1] = mfma16(kf[0], qf[1], limA); SB();
      vb[1] = pk2(acc[0][2][2], acc[0][2][3]); SB();
      vb[2] = pk2(acc[1][2][0], acc[1][2][1]); SB();
      l[0] = mfma16(kf[0], qf[0], limB); SB();
      vb[3] = pk2(acc[1][2][2], acc[1][2][3]); SB();
      tb_[2] = pk2(tx[1][0], tx[1][1]); SB();
      GM(2, 0, wf0, af0); SB();
      tb_[3] = pk2(tx[1][2], tx[1][3]); SB();
      GM(2, 2, wf0, af0); SB();
      l[1][0] = fmx(l[1][0], sA[1][0]); SB();
      l[1][1] = fmx(l[1][1], sA[1][1]); SB();
      GM(2, 3, wf2, af2); SB();
      l[1][2] = fmx(l[1][2], sA[1][2]); SB();
      mx[0] = fmx(fmx(l[0][0], l[0][1]), l[0][2]); SB();
      GM(2, 1, wf2, af2); SB();
      l[1][3] = fmx(l[1][3], sA[1][3]); SB();
      mx[1] = fmx(fmx(l[1][0], l[1][1]), l[1][2]); SB();
      GM(2, 0, wf2, af2); SB();
      mx[0] = fmx(mx[0], l[0][3]); SB();
      mx[1] = fmx(mx[1], l[1][3]); SB();
      GM(2, 2, wf2, af2); SB();
      qf[2][2] = pk2(acc[2][3][0], acc[2][3][1]); SB();
      qf[2][3] = pk2(acc[2][3][2], acc[2][3][3]); SB();
      GM(3, 3, wf1, af1); SB();
      { const auto r_ = __builtin_amdgcn_permlane16_swap(qf[2][2], kz[2][0], false, false); qf[2][2] = r_[0]; kf[2][2] = r_[1]; } SB();
      kf[2][0] = pk2(acc[2][1][0], acc[2][1][1]); SB();
      GM(3, 1, wf1, af1); SB();
      qf[2][0] = pk2(acc[2][0][0], acc[2][0][1]); SB();
      { const auto r_ = __builtin_amdgcn_permlane16_swap(qf[2][3], kz[2][1], false, false); qf[2][3] = r_[0]; kf[2][3] = r_[1]; } SB();
      GM(3, 0, wf1, af1); SB();
      kf[2][1] = pk2(acc[2][1][2], acc[2][1][3]); SB();
      qf[2][1] = pk2(acc[2][0][2], acc[2][0][3]); SB();
      tx[2] = mfma16h((u32x2){qf[2][2], qf[2][3]}, sel[2], zero4); SB();
      { const auto r_ = __builtin_amdgcn_permlane32_swap(__float_as_uint(mx[0]), __float_as_uint(mx[1]), false, false); mx[0] = __uint_as_float(r_[0]); mx[1] = __uint_as_float(r_[1]); } SB();
      mx[0] = fmx(mx[0], mx[1]); SB();
      l[2] = mfma16(kf[2], qf[2], limB); SB();
      mx[1] = cpy(mx[0]); SB();
      vb[4] = pk2(acc[2][2][0], acc[2][2][1]); SB();
      sA[2] = mfma16(kf[1], qf[2], limA); SB();
      { const auto r_ = __builtin_amdgcn_permlane16_swap(__float_as_uint(mx[0]), __float_as_uint(mx[1]), false, false); mx[0] = __uint_as_float(r_[0]); mx[1] = __uint_as_float(r_[1]); } SB();
      mx[0] = fmx(mx[0], mx[1]); SB();
      GM(3, 2, wf1, af1); SB();
      mx[1] = mx[0] * -cs; SB();
      mx[0] = mulo(mx[0], -cs); SB();
      GM(3, 3, wf0, af0); SB();
      vb[5] = pk2(acc[2][2][2], acc[2][2][3]); SB();
      { const auto r_ = __builtin_amdgcn_permlane32_swap(__float_as_uint(mx[0]), __float_as_uint(mx[1]), false, false); mx[0] = __uint_as_float(r_[0]); mx[1] = __uint_as_float(r_[1]); } SB();
      GM(3, 1, wf0, af0); SB();
      l[2][0] = fmx(l[2][0], sA[2][0]); SB();
      l[2][1] = fmx(l[2][1], sA[2][1]); SB();
      GM(3, 0, wf0, af0); SB();
      l[2][2] = fmx(l[2][2], sA[2][2]); SB();
      l[2][3] = fmx(l[2][3], sA[2][3]); SB();
      GM(3, 2, wf0, af0); SB();
      mx[2] = fmx(fmx(l[2][0], l[2][1]), l[2][2]); SB();
      mx[2] = fmx(mx[2], l[2][3]); SB();
      GM(3, 3, wf2, af2); SB();
      ar[1][0] = __builtin_elementwise_fma((f32x2){l[1][0], l[1][1]}, c2, (f32x2){mx[1], mx[1]}); SB();
      ar[1][1] = __builtin_elementwise_fma((f32x2){l[1][2], l[1][3]}, c2, (f32x2){mx[1], mx[1]}); SB();
      GM(3, 1, wf2, af2); SB();
      ar[0][0] = __builtin_elementwise_fma((f32x2){l[0][0], l[0][1]}, c2, (f32x2){mx[0], mx[0]}); SB();
      ar[0][1] = __builtin_elementwise_fma((f32x2){l[0][2], l[0][3]}, c2, (f32x2){mx[0], mx[0]}); SB();
      GM(3, 0, wf2, af2); SB();
      ar[1][0][0] = __builtin_amdgcn_exp2f(ar[1][0][0]); SB();
      GM(3, 2, wf2, af2); SB();
      qf[3][2] = pk2(acc[3][3][0], acc[3][3][1]); SB();
      qf[3][3] = pk2(acc[3][3][2], acc[3][3][3]); SB();
      GM(4, 3, wf1, af1); SB();
      { const auto r_ = __builtin_amdgcn_permlane16_swap(qf[3][2], kz[3][0], false, false); qf[3][2] = r_[0]; kf[3][2] = r_[1]; } SB();
      kf[3][0] = pk2(acc[3][1][0], acc[3][1][1]); SB();
      GM(4, 1, wf1, af1); SB();
      qf[3][0] = pk2(acc[3][0][0], acc[3][0][1]); SB();
      { const auto r_ = __builtin_amdgcn_permlane16_swap(qf[3][3], kz[3][1], false, false); qf[3][3] = r_[0]; kf[3][3] = r_[1]; } SB();
      GM(4, 0, wf1, af1); SB();
      kf[3][1] = pk2(acc[3][1][2], acc[3][1][3]); SB();
      qf[3][1] = pk2(acc[3][0][2], acc[3][0][3]); SB();
      tx[3] = mfma16h((u32x2){qf[3][2], qf[3][3]}, sel[3], zero4); SB();
      ar[1][0][1] = __builtin_amdgcn_exp2f(ar[1][0][1]); SB();
      l[3] = mfma16(kf[3], qf[3], limB); SB();
      ar[1][1][0] = __builtin_amdgcn_exp2f(ar[1][1][0]); SB();
      sA[3] = mfma16(kf[2], qf[3], limA); SB();
      ar[1][1][1] = __builtin_amdgcn_exp2f(ar[1][1][1]); SB();
      GM(4, 2, wf1, af1); SB();
      ar[0][0][0] = __builtin_amdgcn_exp2f(ar[0][0][0]); SB();
      GM(4, 3, wf0, af0); SB();
      ar[0][0][1] = __builtin_amdgcn_exp2f(ar[0][0][1]); SB();
      GM(4, 1, wf0, af0); SB();
      ar[0][1][0] = __builtin_amdgcn_exp2f(ar[0][1][0]); SB();
      GM(4, 0, wf0, af0); SB();
      l[3][0] = fmx(l[3][0], sA[3][0]); SB();
      l[3][1] = fmx(l[3][1], sA[3][1]); SB();
      GM(4, 2, wf0, af0); SB();
      l[3][2] = fmx(l[3][2], sA[3][2]); SB();
      l[3][3] = fmx(l[3][3], sA[3][3]); SB();
      GM(4, 3, wf2, af2); SB();
      mx[3] = fmx(fmx(l[3][0], l[3][1]), l[3][2]); SB();
      mx[3] = fmx(mx[3], l[3][3]); SB();
      GM(4, 1, wf2, af2); SB();
      ar[0][1][1] = __builtin_amdgcn_exp2f(ar[0][1][1]); SB();
      GM(4, 0, wf2, af2); SB();
      { const auto r_ = __builtin_amdgcn_permlane32_swap(__float_as_uint(mx[2]), __float_as_uint(mx[3]), false, false); mx[2] = __uint_as_float(r_[0]); mx[3] = __uint_as_float(r_[1]); } SB();
      mx[2] = fmx(mx[2], mx[3]); SB();
      GM(4, 2, wf2, af2); SB();
      qf[4][2] = pk2(acc[4][3][0], acc[4][3][1]); SB();
      qf[4][3] = pk2(acc[4][3][2], acc[4][3][3]); SB();
      mx[3] = cpy(mx[2]); SB();
      { const auto r_ = __builtin_amdgcn_permlane16_swap(qf[4][2], kz[4][0], false, false); qf[4][2] = r_[0]; kf[4][2] = r_[1]; } SB();
      kf[4][0] = pk2(acc[4][1][0], acc[4][1][1]); SB();
      qf[4][0] = pk2(acc[4][0][0], acc[4][0][1]); SB();
      { const auto r_ = __builtin_amdgcn_permlane16_swap(qf[4][3], kz[4][1], false, false); qf[4][3] = r_[0]; kf[4][3] = r_[1]; } SB();
      kf[4][1] = pk2(acc[4][1][2], acc[4][1][3]); SB();
      qf[4][1] = pk2(acc[4][0][2], acc[4][0][3]); SB();
      tx[4] = mfma16h((u32x2){qf[4][2], qf[4][3]}, sel[0], zero4); SB();
      { const auto r_ = __builtin_amdgcn_permlane16_swap(__float_as_uint(mx[2]), __float_as_uint(mx[3]), false, false); mx[2] = __uint_as_float(r_[0]); mx[3] = __uint_as_float(r_[1]); } SB();
      mx[2] = fmx(mx[2], mx[3]); SB();
      l[4] = mfma16(kf[4], qf[4], limB); SB();
      mx[3] = mx[2] * -cs; SB();
      mx[2] = mulo(mx[2], -cs); SB();
      sA[4] = mfma16(kf[3], qf[4], limA); SB();
      pp[1][0] = pk2(ar[1][0][0], ar[1][0][1]); SB();
      { const auto r_ = __builtin_amdgcn_permlane32_swap(__float_as_uint(mx[2]), __float_as_uint(mx[3]), false, false); mx[2] = __uint_as_float(r_[0]); mx[3] = __uint_as_float(r_[1]); } SB();
      ar[2][0] = __builtin_elementwise_fma((f32x2){l[2][0], l[2][1]}, c2, (f32x2){mx[2], mx[2]}); SB();
      ar[2][1] = __builtin_elementwise_fma((f32x2){l[2][2], l[2][3]}, c2, (f32x2){mx[2], mx[2]}); SB();
      ar[2][0][0] = __builtin_amdgcn_exp2f(ar[2][0][0]); SB();
      ar[2][0][1] = __builtin_amdgcn_exp2f(ar[2][0][1]); SB();
      ar[2][1][0] = __builtin_amdgcn_exp2f(ar[2][1][0]); SB();
      ar[2][1][1] = __builtin_amdgcn_exp2f(ar[2][1][1]); SB();
      l[4][0] = fmx(l[4][0], sA[4][0]); SB();
      l[4][1] = fmx(l[4][1], sA[4][1]); SB();
      l[4][2] = fmx(l[4][2], sA[4][2]); SB();
      l[4][3] = fmx(l[4][3], sA[4][3]); SB();
      mx[4] = fmx(fmx(l[4][0], l[4][1]), l[4][2]); SB();
      mx[4] = fmx(mx[4], l[4][3]); SB();
      mx[5] = cpy(mx[4]); SB();
      pp[1][1] = pk2(ar[1][1][0], ar[1][1][1]); SB();
      pp[2][0] = pk2(ar[2][0][0], ar[2][0][1]); SB();
      { const auto r_ = __builtin_amdgcn_permlane32_swap(__float_as_uint(mx[4]), __float_as_uint(mx[5]), false, false); mx[4] = __uint_as_float(r_[0]); mx[5] = __uint_as_float(r_[1]); } SB();
      mx[4] = fmx(mx[4], mx[5]); SB();
      mx[5] = cpy(mx[4]); SB();
      pp[2][1] = pk2(ar[2][1][0], ar[2][1][1]); SB();
      pp[0][0] = pk2(ar[0][0][0], ar[0][0][1]); SB();
      { const auto r_ = __builtin_amdgcn_permlane16_swap(__float_as_uint(mx[4]), __float_as_uint(mx[5]), false, false); mx[4] = __uint_as_float(r_[0]); mx[5] = __uint_as_float(r_[1]); } SB();
      pp[0][1] = pk2(ar[0][1][0], ar[0][1][1]); SB();
      mx[4] = fmx(mx[4], mx[5]); SB();
      pf[1][0] = pp[1][0] & mA[0]; SB();
      sm[0] = mfma16h((u32x2){ones[0], ones[1]}, pp[0], zero4); SB();
      pf[1][2] = pp[1][0] & ~mA[0]; SB();
      pf[1][1] = pp[1][1] & mA[1]; SB();
      oA[0] = mfma16h((u32x2){vb[0], vb[1]}, pp[0], zero4); SB();
      pf[1][3] = pp[1][1] & ~mA[1]; SB();
      pf[2][0] = pp[2][0] & mA[0]; SB();
      oT[2] = mfma16h((u32x2){tb_[0], tb_[1]}, pp[0], zero4); SB();
      pf[2][2] = pp[2][0] & ~mA[0]; SB();
      pf[2][1] = pp[2][1] & mA[1]; SB();
      sm[1] = mfma16(ones, pf[1], zero4); SB();
      pf[2][3] = pp[2][1] & ~mA[1]; SB();
      mx[4] = mx[4] * -cs; SB();
      PIN(tb_); oT[0] = mfma16(__builtin_shufflevector(tb_, tb_, 0, 1, 2, 3), pf[1], zero4); SB();
      ar[3][0] = __builtin_elementwise_fma((f32x2){l[3][0], l[3][1]}, c2, (f32x2){mx[3], mx[3]}); SB();
      ar[3][1] = __builtin_elementwise_fma((f32x2){l[3][2], l[3][3]}, c2, (f32x2){mx[3], mx[3]}); SB();
      sm[2] = mfma16(ones, pf[2], zero4); SB();
      ar[4][0] = __builtin_elementwise_fma((f32x2){l[4][0], l[4][1]}, c2, (f32x2){mx[4], mx[4]}); SB();
      ar[4][1] = __builtin_elementwise_fma((f32x2){l[4][2], l[4][3]}, c2, (f32x2){mx[4], mx[4]}); SB();
      PIN(vb); oA[1] = mfma16(__builtin_shufflevector(vb, vb, 0, 1, 2, 3), pf[1], zero4); SB();
      ar[3][0][0] = __builtin_amdgcn_exp2f(ar[3][0][0]); SB();
      PIN(vb); oA[2] = mfma16(__builtin_shufflevector(vb, vb, 2, 3, 4, 5), pf[2], zero4); SB();
      ar[3][0][1] = __builtin_amdgcn_exp2f(ar[3][0][1]); SB();
      ar[3][1][0] = __builtin_amdgcn_exp2f(ar[3][1][0]); SB();
      ar[3][1][1] = __builtin_amdgcn_exp2f(ar[3][1][1]); SB();
      ar[4][0][0] = __builtin_amdgcn_exp2f(ar[4][0][0]); SB();
      ar[4][0][1] = __builtin_amdgcn_exp2f(ar[4][0][1]); SB();
      ar[4][1][0] = __builtin_amdgcn_exp2f(ar[4][1][0]); SB();
      ar[4][1][1] = __builtin_amdgcn_exp2f(ar[4][1][1]); SB();
      pp[3][0] = pk2(ar[3][0][0], ar[3][0][1]); SB();
      pp[3][1] = pk2(ar[3][1][0], ar[3][1][1]); SB();
      pp[4][0] = pk2(ar[4][0][0], ar[4][0][1]); SB();
      pp[4][1] = pk2(ar[4][1][0], ar[4][1][1]); SB();
      inv[0] = __builtin_amdgcn_rcpf(sm[0][0]); PIN(sm[0]); SB();
      inv[1] = __builtin_amdgcn_rcpf(sm[1][0]); PIN(sm[1]); SB();
      inv[2] = __builtin_amdgcn_rcpf(sm[2][0]); PIN(sm[2]); SB();
      pf[3][0] = pp[3][0] & mA[0]; SB();
      pf[3][2] = pp[3][0] & ~mA[0]; SB();
      pf[3][1] = pp[3][1] & mA[1]; SB();
      pf[3][3] = pp[3][1] & ~mA[1]; SB();
      pf[4][0] = pp[4][0] & mA[0]; SB();
      pf[4][2] = pp[4][0] & ~mA[0]; SB();
      sm[3] = mfma16(ones, pf[3], zero4); SB();
      pf[4][1] = pp[4][1] & mA[1]; SB();
      pf[4][3] = pp[4][1] & ~mA[1]; SB();
      tb_[4] = pk2(tx[2][0], tx[2][1]); SB();
      tb_[5] = pk2(tx[2][2], tx[2][3]); SB();
      sm[4] = mfma16(ones, pf[4], zero4); SB();
      nn0[0] = (f32x2){oA[0][0], oA[0][1]} * (f32x2){inv[0], inv[0]}; SB();
      nn0[1] = (f32x2){oA[0][2], oA[0][3]} * (f32x2){inv[0], inv[0]}; SB();
      PIN(tb_); oT[1] = mfma16(__builtin_shufflevector(tb_, tb_, 2, 3, 4, 5), pf[2], zero4); SB();
      ob[0][0] = pk2(nn0[0][0], nn0[0][1]); SB();
      ob[0][1] = pk2(nn0[1][0], nn0[1][1]); SB();
      nn1[0] = (f32x2){oA[1][0], oA[1][1]} * (f32x2){inv[1], inv[1]}; SB();
      nn1[1] = (f32x2){oA[1][2], oA[1][3]} * (f32x2){inv[1], inv[1]}; SB();
      nn2[0] = (f32x2){oA[2][0], oA[2][1]} * (f32x2){inv[2], inv[2]}; SB();
      nn2[1] = (f32x2){oA[2][2], oA[2][3]} * (f32x2){inv[2], inv[2]}; SB();
      vb[6] = pk2(acc[3][2][0], acc[3][2][1]); SB();
      vb[7] = pk2(acc[3][2][2], acc[3][2][3]); SB();
      vb[8] = pk2(acc[4][2][0], acc[4][2][1]); SB();
      vb[9] = pk2(acc[4][2][2], acc[4][2][3]); SB();
      PIN(vb); oA[3] = mfma16(__builtin_shufflevector(vb, vb, 4, 5, 6, 7), pf[3], zero4); SB();
      tb_[6] = pk2(tx[3][0], tx[3][1]); SB();
      tb_[7] = pk2(tx[3][2], tx[3][3]); SB();
      PIN(vb); oA[4] = mfma16(__builtin_shufflevector(vb, vb, 6, 7, 8, 9), pf[4], zero4); SB();
      tb_[8] = pk2(tx[4][0], tx[4][1]); SB();
      tb_[9] = pk2(tx[4][2], tx[4][3]); SB();
      PIN(tb_); oT[0] = mfma16(__builtin_shufflevector(tb_, tb_, 4, 5, 6, 7), pf[3], oT[0]); SB();
      ob[1][0] = pk2(nn1[0][0], nn1[0][1]); SB();
      ob[1][1] = pk2(nn1[1][0], nn1[1][1]); SB();
      PIN(tb_); oT[1] = mfma16(__builtin_shufflevector(tb_, tb_, 6, 7, 8, 9), pf[4], oT[1]); SB();
      ob[2][0] = pk2(nn2[0][0], nn2[0][1]); SB();
      ob[2][1] = pk2(nn2[1][0], nn2[1][1]); SB();
      ob[5][0] = cpyu(ob[0][0]); SB();
      ob[5][1] = cpyu(ob[0][1]); SB();
      nnU[0] = (f32x2){oT[2][0], oT[2][1]} * (f32x2){inv[0], inv[0]}; SB();
      nnU[1] = (f32x2){oT[2][2], oT[2][3]} * (f32x2){inv[0], inv[0]}; SB();
      obU[0] = pk2(nnU[0][0], nnU[0][1]); SB();
      obU[1] = pk2(nnU[1][0], nnU[1][1]); SB();
      { const auto r_ = __builtin_amdgcn_permlane32_swap(ob[0][0], ob[5][0], false, false); os[0][0] = r_[0]; os[0][2] = r_[1]; } SB();
      { const auto r_ = __builtin_amdgcn_permlane32_swap(ob[0][1], ob[5][1], false, false); os[0][1] = r_[0]; os[0][3] = r_[1]; } SB();
      { const auto r_ = __builtin_amdgcn_permlane32_swap(ob[1][0], ob[2][0], false, false); os[1][0] = r_[0]; os[1][2] = r_[1]; } SB();
      { const auto r_ = __builtin_amdgcn_permlane32_swap(ob[1][1], ob[2][1], false, false); os[1][1] = r_[0]; os[1][3] = r_[1]; } SB();
      inv[3] = __builtin_amdgcn_rcpf(sm[3][0]); PIN(sm[3]); SB();
      inv[4] = __builtin_amdgcn_rcpf(sm[4][0]); PIN(sm[4]); SB();
      it = selm(mo, inv[2], inv[1]); SB();
      iu = selm(mo, inv[4], inv[3]); SB();
      nn3[0] = (f32x2){oA[3][0], oA[3][1]} * (f32x2){inv[3], inv[3]}; SB();
      nn3[1] = (f32x2){oA[3][2], oA[3][3]} * (f32x2){inv[3], inv[3]}; SB();
      nn4[0] = (f32x2){oA[4][0], oA[4][1]} * (f32x2){inv[4], inv[4]}; SB();
      nn4[1] = (f32x2){oA[4][2], oA[4][3]} * (f32x2){inv[4], inv[4]}; SB();
      it = selm(mh, iu, it); SB();
      oT[0][0] = selm(mo, oT[1][0], oT[0][0]); SB();
      oT[0][1] = selm(mo, oT[1][1], oT[0][1]); SB();
      oT[0][2] = selm(mo, oT[1][2], oT[0][2]); SB();
      oT[0][3] = selm(mo, oT[1][3], oT[0][3]); SB();
      ob[3][0] = pk2(nn3[0][0], nn3[0][1]); SB();
      ob[3][1] = pk2(nn3[1][0], nn3[1][1]); SB();
      ob[4][0] = pk2(nn4[0][0], nn4[0][1]); SB();
      ob[4][1] = pk2(nn4[1][0], nn4[1][1]); SB();
      nnT[0] = (f32x2){oT[0][0], oT[0][1]} * (f32x2){it, it}; SB();
      nnT[1] = (f32x2){oT[0][2], oT[0][3]} * (f32x2){it, it}; SB();
      obT[0] = pk2(nnT[0][0], nnT[0][1]); SB();
      obT[1] = pk2(nnT[1][0], nnT[1][1]); SB();
      XBAR(); SB();
      { const auto r_ = __builtin_amdgcn_permlane32_swap(ob[3][0], ob[4][0], false, false); os[2][0] = r_[0]; os[2][2] = r_[1]; } SB();
      { const auto r_ = __builtin_amdgcn_permlane32_swap(ob[3][1], ob[4][1], false, false); os[2][1] = r_[0]; os[2][3] = r_[1]; } SB();
      __builtin_amdgcn_raw_buffer_store_b128(os[0], ro, od[0], 0, 16); SB();
      __builtin_amdgcn_raw_buffer_store_b128(os[1], ro, od[1], 0, 16); SB();
      __builtin_amdgcn_raw_buffer_store_b128(os[2], ro, od[2], 0, 16); SB();
      __builtin_amdgcn_raw_buffer_store_b64(obT, ro, od[3], 0, 16); SB();
      __builtin_amdgcn_raw_buffer_store_b64(obU, ro, od[4], 0, 16); SB();
    }
  } else {
    const int f = xcd * 4 + (jj - 28);
    args();
    float* red = (float*)smem;
    u16* hs = (u16*)(smem + 32768);
    const bool do1 = f < 27;
    const bool lead = f == 31;
    if (__builtin_expect(lead, 0)) {
      get_tag();
      const __amdgpu_buffer_rsrc_t rk = __builtin_amdgcn_make_buffer_rsrc((void*)(ws + WS_K), 0, 36864, 0x00020000);
      u32x2 kv[2];
      int ko[2];
      ko[0] = (tid >> 2) * 128 + (tid & 3) * 16;
      ko[1] = tid < 384 ? (128 + (tid >> 2)) * 128 + (tid & 3) * 16 : (224 + ((tid - 384) >> 1)) * 128 + (tid & 1) * 16;
      kv[0] = kv[1] = (u32x2){0u, 0u};
#pragma unroll 1
      for (unsigned spins = 0; spins < SPIN_CAP; ++spins) {
        asm volatile("" ::: "memory");
        u32x4 t_[2];
#pragma unroll
        for (int q = 0; q < 2; ++q) t_[q] = __builtin_amdgcn_raw_buffer_load_b128(rk, ko[q], 0, 16);
        bool ok = true;
#pragma unroll
        for (int q = 0; q < 2; ++q)
          if (ko[q] != 0x7FFFFF00) {
            if ((t_[q][1] == tag) & (t_[q][3] == tag)) {
              kv[q] = (u32x2){t_[q][0], t_[q][2]};
              ko[q] = 0x7FFFFF00;
            } else {
              ok = false;
            }
          }
        if (__all(ok)) break;
        __builtin_amdgcn_s_sleep(4);
      }
      unsigned k1 = max(kv[0][0], kv[0][1]), k2 = min(kv[0][0], kv[0][1]);
      top2_mrg(k1, k2, max(kv[1][0], kv[1][1]), min(kv[1][0], kv[1][1]));
      top2_all(k1, k2);
      unsigned* lk = (unsigned*)(smem + L_VAR + 64);
      if (lane == 0) *(u32x2*)(lk + wave * 2) = (u32x2){k1, k2};
      WAITL0();
      BARRIER();
      k1 = 0u;
      k2 = 0u;
#pragma unroll
      for (int q = 0; q < 4; ++q) {
        const u32x4 a_ = *(const u32x4*)(lk + q * 4);
        top2_mrg(k1, k2, a_[0], a_[1]);
        top2_mrg(k1, k2, a_[2], a_[3]);
      }
      if (tid == 0) {
        const __amdgpu_buffer_rsrc_t rr = __builtin_amdgcn_make_buffer_rsrc((void*)(ws + WS_R), 0, 128, 0x00020000);
        __builtin_amdgcn_raw_buffer_store_b128((u32x4){k1, tag, k2, tag}, rr, 0, 0, 16);
        *(u32x2*)(smem + L_VAR + 32) = (u32x2){k1, k2};
      }
    }
    if (__builtin_expect(f == 30, 0)) {
      get_tag();
      if (wave < 4) {
        const __amdgpu_buffer_rsrc_t rf = __builtin_amdgcn_make_buffer_rsrc((void*)(ws + WS_F2), 0, 32768, 0x00020000);
        int fo = tid < 227 ? tid * 128 : 0x7FFFFF00;
#pragma unroll 1
        for (unsigned spins = 0; spins < SPIN_CAP; ++spins) {
          asm volatile("" ::: "memory");
          if (__builtin_amdgcn_raw_buffer_load_b32(rf, fo, 0, 16) == tag) fo = 0x7FFFFF00;
          if (__all(fo == 0x7FFFFF00)) break;
          __builtin_amdgcn_s_sleep(4);
        }
      }
      BARRIER();
      if (tid == 0) {
        const __amdgpu_buffer_rsrc_t rr = __builtin_amdgcn_make_buffer_rsrc((void*)(ws + WS_R), 0, 256, 0x00020000);
        const unsigned t_ = now();
        __builtin_amdgcn_raw_buffer_store_b128((u32x4){t_, tag, t_, tag}, rr, 128, 0, 16);
        *(u32x4*)(smem + L_CAND2) = (u32x4){t_, tag, t_, tag};
      }
    }
    if (__builtin_expect(f >= 27 && f < 30, 0)) {
      get_tag();
#pragma unroll 1
      for (int c = wave * 3 + f - 27; c < 320; c += 24)
        __builtin_amdgcn_raw_buffer_store_b128((u32x4){SENT, SENT, SENT, SENT}, ro, lane * 16, (c >= 160 ? 112 * 20480 : 56 * 20480) + c * 1024, 16);
      WAITV(0);
      BARRIER();
      if (tid == 0) __hip_atomic_store((gu32*)(ws + WS_F2 + (197 + f) * 128), tag, RLX_AGENT);
    }
    const int g0 = wave < 4 ? wave * 3 : 4 + wave * 2, ng = wave < 4 ? 3 : 2;
    u32x4 r1[3][8], wd[2][8];
    int vo2 = (wave - 3) * 256 + c15 * 16 + g4 * 8 * (DM * 2);
    PIN(vo2);
    if (do1) {
      const char* pa = (const char*)(c15 < 8 ? w_gate : w_up) + f * 128 + (c15 & 7) * 16 + (g0 * 32 + g4 * 8) * (FF * 2);
      const GAS char* pp[12];
#pragma unroll
      for (int j = 0; j < 12; ++j) {
        pp[j] = (const GAS char*)pa + ((j >> 2) * 32 + (j & 3) * 2) * (FF * 2) + FF;
        PIN(pp[j]);
      }
      SB();
#pragma unroll
      for (int j = 0; j < 24; ++j)
        if (j < 16 || wave < 4) r1[j >> 3][j & 7] = __builtin_nontemporal_load((const GAS u32x4*)(pp[j >> 1] + ((j & 1) ? FF : -FF)));
    }
    SB();
    BARRIER();
    if (do1 && wave >= 3) {
      const __amdgpu_buffer_rsrc_t r = __builtin_amdgcn_make_buffer_rsrc((void*)w_down, 0, DM * FF * 2, 0x00020000);
#pragma unroll
      for (int j = 0; j < 16; ++j) wd[j >> 3][j & 7] = __builtin_amdgcn_raw_buffer_load_b128(r, vo2, (f * 64 + (j >> 3) * 32 + (j & 7)) * (DM * 2), 2);
    }
    SB();
    if (f < 30) get_tag();
    prepB();
    auto transp = [&](u32x4(&w)[8]) {
      u32x4 t[8];
#pragma unroll
      for (int d = 0; d < 4; ++d)
#pragma unroll
        for (int p = 0; p < 4; ++p) {
          t[2 * d][p] = perm_lo(w[2 * p + 1][d], w[2 * p][d]);
          t[2 * d + 1][p] = perm_hi(w[2 * p + 1][d], w[2 * p][d]);
        }
#pragma unroll
      for (int j = 0; j < 8; ++j) {
        w[j] = t[j];
        PIN(w[j]);
      }
    };
    if (do1) {
      transp(r1[0]);
      transp(r1[1]);
      if (wave < 4) transp(r1[2]);
      if (wave >= 3) {
        transp(wd[0]);
        transp(wd[1]);
      }
    }
    __builtin_amdgcn_s_waitcnt(0x0F70);
    if (wave == 0 && !lead) {
      res_wait(ws, smem, lane, tag, 0);
      if (do1) {
        WAITL0();
        const u32x2 kq = *(const u32x2*)(smem + L_VAR + 32);
#pragma unroll
        for (int t = 0; t < 2; ++t) {
          const u16* xr = x + (2047 - (int)(__builtin_amdgcn_readfirstlane(kq[t]) & 2047u)) * DM;
          dma16(lane * 16, xr, lds0 + 36864 + t * 1296);
          if (lane < 16) dma16(lane * 16, xr + 512, lds0 + 36864 + t * 1296 + 1024);
        }
        if (lane == 0) dma16_sc1(0, ws + WS_R + 128, lds0 + L_CAND2);
        WAITV(0);
      }
    }
    if (wave == 1 && f != 30 && !do1) res_wait(ws, smem, lane, tag, 1);
    WAITL0();
    BARRIER();
    if (tid == 128) __hip_atomic_store((gu32*)(ws + WS_A + f * 128), tag, RLX_AGENT);
    bool seen = false, early = false;
    auto wo_a = [&]() {
      wo_dma(IC<1>{}, wave, 8, 25);
      if (wave < 5) wo_dma(IC<1>{}, 8 + wave, 8, 25);
    };
    const __amdgpu_buffer_rsrc_t ryp = __builtin_amdgcn_make_buffer_rsrc((void*)(ws + WS_YP), 0, 27 * 10240, 0x00020000);
    auto pick = [&](const f32x4(&a)[8]) {
      f32x4 v;
#pragma unroll
      for (int q = 0; q < 4; ++q) {
        const float lo = (g4 & 1) ? a[2 + (q >> 1)][q & 1] : a[q >> 1][q & 1], hi = (g4 & 1) ? a[6 + (q >> 1)][q & 1] : a[4 + (q >> 1)][q & 1];
        v[q] = (g4 & 2) ? hi : lo;
      }
      return v;
    };
    if (do1) {
      u32x4 xf[3];
#pragma unroll
      for (int k = 0; k < 3; ++k) xf[k] = *(const u32x4*)(smem + 36864 + (c15 & 1) * 1296 + ((g0 + (k < ng ? k : 0)) * 32 + g4 * 8) * 2);
      {
        const u32x4 rv = *(const u32x4*)(smem + L_CAND2);
        seen = __all((rv[1] == tag) & (rv[3] == tag));
        early = WPOS < 3 && seen;
      }
      if (WPOS == 2 && early) {
        wo_a();
        if (wave >= 2) wo_dma(IC<2>{}, 11 + wave, 6, 25);
      }
      f32x4 acc[8];
#pragma unroll
      for (int ff = 0; ff < 8; ++ff) acc[ff] = (f32x4){0.f, 0.f, 0.f, 0.f};
#pragma unroll
      for (int k = 0; k < 3; ++k)
        if (k < ng) {
#pragma unroll
          for (int j = 0; j < 8; ++j) acc[j] = mfma16(xf[k], r1[k][j], acc[j]);
        }
      SB();
      if (WPOS == 0 && early) wo_a();
      SB();
      *(f32x4*)(red + wave * 256 + c15 * 16 + g4 * 4) = pick(acc);
      WAITL0();
      if (WPOS == 1 && early) wo_a();
      BARRIER();
      if (wave < 2) {
        float gs = 0.f, us = 0.f;
#pragma unroll
        for (int w = 0; w < 8; ++w) {
          gs += red[w * 256 + tid];
          us += red[w * 256 + 128 + tid];
        }
        const float gv = rbf(gs), uv = rbf(us);
        const float hv = rbf(gv * __builtin_amdgcn_rcpf(1.0f + __expf(-gv))) * uv;
        hs[(tid & 1) * 64 + (tid >> 1)] = (u16)pk2(hv, hv);
      } else if (WPOS < 2 && early) {
        wo_dma(IC<2>{}, 11 + wave, 6, 25);
      }
      WAITL0();
      BARRIER();
      if (!seen && wave == 2) res_wait(ws, smem, lane, tag, 1);
      if (wave >= 3) {
        const u32x4 h0 = *(const u32x4*)(hs + (c15 & 1) * 64 + g4 * 8), h1 = *(const u32x4*)(hs + (c15 & 1) * 64 + 32 + g4 * 8);
#pragma unroll
        for (int j = 0; j < 8; ++j) acc[j] = mfma16(h0, wd[0][j], (f32x4){0.f, 0.f, 0.f, 0.f});
#pragma unroll
        for (int j = 0; j < 8; ++j) acc[j] = mfma16(h1, wd[1][j], acc[j]);
        *(f32x4*)(red + 2048 + wave * 256 + c15 * 16 + g4 * 4) = pick(acc);
        const f32x4 y = *(const f32x4*)(red + 2048 + wave * 256 + lane * 4);
#pragma unroll
        for (int t = 0; t < 2; ++t)
          __builtin_amdgcn_raw_buffer_store_b128((u32x4){__float_as_uint(y[t]), tag, __float_as_uint(y[2 + t]), tag}, ryp,
                                                 ((f * 2 + t) * 640 + (wave - 3) * 128 + lane * 2) * 8, 0, 16);
      }
      WAITL0();
      BARRIER();
    }
    if (!early) {
      wo_a();
      if (wave >= 2) wo_dma(IC<2>{}, 11 + wave, 6, 25);
    }
    if (wave == 1) gate_dma(gate_off(lane));
    if (tid < 4) *(unsigned*)(smem + L_VAR + tid * 4) = 0u;
    if (wave == 2 || b == 247) {
      const unsigned tp = __builtin_amdgcn_readfirstlane(*(const unsigned*)(smem + L_CAND2)) + D1;
#pragma unroll 1
      for (unsigned spins = 0; spins < 128 && (int)(now() - tp) < 0; ++spins) __builtin_amdgcn_s_sleep(2);
    }
    if (wave == 2) {
      const unsigned t_ = now() - D2;
      const u32x4 v = __builtin_amdgcn_raw_buffer_load_b128(rfl, flag_off(), 0, 16);
      *(u32x4*)(smem + L_FS + lane * 16) = (u32x4){(v[1] == tag ? v[0] : t_) + (ETA_F - ETA_G), tag, 0u, tag};
    }
    if (b == 247 && wave == 3) {
      const __amdgpu_buffer_rsrc_t ra = __builtin_amdgcn_make_buffer_rsrc((void*)(ws + WS_A), 0, 4096, 0x00020000);
#pragma unroll 1
      for (unsigned spins = 0; spins < SPIN_CAP; ++spins) {
        asm volatile("" ::: "memory");
        if (__all(__builtin_amdgcn_raw_buffer_load_b32(ra, (lane & 31) * 128, 0, 16) == tag)) break;
        __builtin_amdgcn_s_sleep(8);
      }
      if (lane == 0) __hip_atomic_store((gu32*)(ws + WS_E), tag, RLX_AGENT);
    }
    if (wave == 0) rows_job();
    if (early && wave >= 3) WAITV(2); else WAITV(0);
    WAITL0();
    BARRIER();
  }

  {
    const int m = wave & 3, kh = wave >> 2;
    const __amdgpu_buffer_rsrc_t rout = __builtin_amdgcn_make_buffer_rsrc((void*)out, 0, S_LEN * DM * 2, 0x00020000);
    const int oo = (rt2 * 4 + m) * 20480 + kh * 8192, ot = (rt2 * 4 + m) * 20480 + 16384 + kh * 2048;
    const int l16 = lane * 16;
    u32x4 fs = *(const u32x4*)(smem + L_FS + l16);
    const u32x4 kk = *(const u32x4*)(smem + L_VAR + 32);
    const u32x2 g0_ = *(const u32x2*)(smem + glo);
    if (wave == 7) {
      if (__builtin_expect((~__builtin_amdgcn_ballot_w64((fs[1] == tag) & (fs[3] == tag)) & 0xFFull) != 0, 0)) {
        flag_wait(lane >= 8 ? 0x7FFFFF00 : flag_off());
        WAITL0();
        if (lane == 0) *(volatile LAS unsigned*)(LAS char*)(smem + L_VAR + 8) = 1u;
      }
    }
    dma16(xoff[0], x, lds0 + L_XT + wave * 1024);
    if (wave < 2) dma16(xoff[1], x, lds0 + L_XT + 8192 + wave * 1024);
    SB();
    const char* wb = smem + L_WO + kh * 51200 + l16;
    u32x4 wq[WPD][5];
    auto wrd = [&](int k) {
#pragma unroll
      for (int f = 0; f < 5; ++f) wq[k % WPD][f] = *(const u32x4*)(wb + (k * 5 + f) * 1024);
    };
#pragma unroll
    for (int k = 0; k < NPRE; ++k) wrd(k);
    if (wave >= 4) {
#pragma unroll
      for (int k = NPRE; k < NPRE2; ++k) wrd(k);
    }
    float gt[2];
    auto gate = [&](int e, u32x2 v) {
      if (__builtin_expect(!__all(v[1] == tag), 0)) {
        const __amdgpu_buffer_rsrc_t rg_ = __builtin_amdgcn_make_buffer_rsrc((void*)(ws + WS_K), 0, 36864, 0x00020000);
#pragma unroll 1
        for (unsigned spins = 0; spins < SPIN_CAP; ++spins) {
          asm volatile("" ::: "memory");
          v = __builtin_amdgcn_raw_buffer_load_b64(rg_, gate_off(lrow[e]), 0, 16);
          if (__all(v[1] == tag)) break;
        }
      }
      gt[e] = __uint_as_float(v[0]);
    };
    gate(0, g0_);
    SB();
    const int l0 = __builtin_amdgcn_readfirstlane(kk[2]), l1 = __builtin_amdgcn_readfirstlane(kk[3]);
    unsigned rmin = min((unsigned)l0, (unsigned)l1), ry = kh ? max((unsigned)l0, (unsigned)l1) : rmin;
    asm volatile("" : "+s"(rmin), "+s"(ry));
#define routed (rmin < 64u)
#define yjob (ry < 64u)
    {
      const u64 inv = ~__builtin_amdgcn_ballot_w64((fs[1] == tag) & (fs[3] == tag));
      if (__builtin_expect((inv & rel) != 0, 0)) {
#pragma unroll 1
        for (unsigned spins = 0; spins < (SPIN_CAP << 6); ++spins) {
          if (__builtin_amdgcn_readfirstlane(*(volatile LAS unsigned*)(LAS char*)(smem + L_VAR + 8))) break;
          __builtin_amdgcn_s_sleep(1);
        }
        fs[0] = *(volatile LAS unsigned*)(LAS char*)(smem + L_FS + l16);
      }
      const unsigned tg = fs[0] + ETA_G;
      unsigned spins = 128;
#pragma unroll 1
      while ((__builtin_amdgcn_ballot_w64((int)(now() - tg) < 0) & rel) != 0 && --spins) {}
    }
    u32x4 yv[7];
    auto y_issue = [&](bool again) {
      char* wy = ws;
      int ly = lane, l0y = l0;
      asm volatile("" : "+s"(wy), "+v"(ly), "+s"(l0y));
      const u32x4 srd = make_srd(wy + WS_YP, 27 * 10240);
      const int yo = (ly >> 4) * 71680 + (((kh || (unsigned)l0y >= 64u) ? 640 : 0) + nt2 * 80 + m * 20 + ((ly & 15) < 10 ? ly & 15 : 0) * 2) * 8, y6 = ly >= 48 ? 0x7FFFFF00 : yo;
#pragma unroll
      for (int j = 0; j < 7; ++j) {
        if (again) asm volatile("buffer_load_dwordx4 %0, %1, %2, %3 offen sc1\n\ts_waitcnt vmcnt(0)" : "=v"(yv[j]) : "v"(j < 6 ? yo : y6), "s"(srd), "s"(j * 10240) : "memory");
        else asm volatile("buffer_load_dwordx4 %0, %1, %2, %3 offen sc1" : "=v"(yv[j]) : "v"(j < 6 ? yo : y6), "s"(srd), "s"(j * 10240) : "memory");
      }
    };
    auto y_done = [&]() {
      WAITV(0);
      int l0y = l0;
      asm volatile("" : "+s"(l0y));
      const int ytok = kh || (unsigned)l0y >= 64u;
      unsigned d = 0u;
      float y[2] = {0.f, 0.f};
#pragma unroll
      for (int j = 0; j < 7; ++j) {
        PIN(yv[j]);
        const unsigned e = (yv[j][1] ^ tag) | (yv[j][3] ^ tag);
        d |= (j == 6 && g4 == 3) ? 0u : e;
        y[0] += __uint_as_float(yv[j][0]);
        y[1] += __uint_as_float(yv[j][2]);
      }
      if (!__all(d == 0)) return false;
      const auto a = __builtin_amdgcn_permlane32_swap(__float_as_uint(y[0]), __float_as_uint(y[1]), false, false);
      const float h = __uint_as_float(a[0]) + __uint_as_float(a[1]);
      const auto c = __builtin_amdgcn_permlane16_swap(__float_as_uint(h), __float_as_uint(h), false, false);
      const float rsel = rbf(__builtin_amdgcn_rcpf(1.0f + __expf(-key_val(ytok ? kk[1] : kk[0]))));
      const float r = rbf(__uint_as_float(c[0]) + __uint_as_float(c[1])) * rsel;
      if (c15 < 10) *(u16*)(smem + L_JUNK + ytok * 160 + m * 40 + c15 * 4 + (lane >> 5) * 2) = (u16)pk2(r, r);
      return true;
    };
    u32x4 of[8];
    u32x2 ofl[2], ofh[2];
    const u32x4 sro = make_srd(ws + WS_OF, S_LEN * DM * 2);
    int l8 = lane * 8;
    asm volatile("buffer_load_dwordx4 %0, %8, %9, %10 offen sc1\n\tbuffer_load_dwordx4 %1, %8, %9, %10 offen offset:1024 sc1\n\t"
                 "buffer_load_dwordx4 %2, %8, %9, %10 offen offset:2048 sc1\n\tbuffer_load_dwordx4 %3, %8, %9, %10 offen offset:3072 sc1\n\t"
                 "buffer_load_dwordx4 %4, %8, %9, %11 offen sc1\n\tbuffer_load_dwordx4 %5, %8, %9, %11 offen offset:1024 sc1\n\t"
                 "buffer_load_dwordx4 %6, %8, %9, %11 offen offset:2048 sc1\n\tbuffer_load_dwordx4 %7, %8, %9, %11 offen offset:3072 sc1"
                 : "=&v"(of[0]), "=&v"(of[1]), "=&v"(of[2]), "=&v"(of[3]), "=&v"(of[4]), "=&v"(of[5]), "=&v"(of[6]), "=&v"(of[7])
                 : "v"(l16), "s"(sro), "s"(oo), "s"(oo + 4096)
                 : "memory");
    asm volatile("buffer_load_dwordx2 %0, %4, %5, %6 offen sc1\n\tbuffer_load_dwordx2 %1, %4, %5, %6 offen offset:512 sc1\n\t"
                 "buffer_load_dwordx2 %2, %4, %5, %6 offen offset:1024 sc1\n\tbuffer_load_dwordx2 %3, %4, %5, %6 offen offset:1536 sc1"
                 : "=&v"(ofl[0]), "=&v"(ofh[0]), "=&v"(ofl[1]), "=&v"(ofh[1])
                 : "v"(l8), "s"(sro), "s"(ot)
                 : "memory");
    if (wave < 4) {
#pragma unroll
      for (int k = NPRE; k < NPRE2; ++k) wrd(k);
    }
#pragma unroll
    for (int k = NPRE2; k < WPD; ++k) wrd(k);
    auto o_again = [&](int k) {
      if (k < 8)
        asm volatile("buffer_load_dwordx4 %0, %1, %2, %3 offen sc1" : "+v"(of[k]) : "v"(l16), "s"(sro), "s"(oo + k * 1024) : "memory");
      else
        asm volatile("buffer_load_dwordx2 %0, %2, %3, %4 offen sc1\n\tbuffer_load_dwordx2 %1, %2, %3, %4 offen offset:512 sc1"
                     : "+v"(ofl[k - 8]), "+v"(ofh[k - 8])
                     : "v"(l8), "s"(sro), "s"(ot + (k - 8) * 1024)
                     : "memory");
    };
    auto o_pin = [&](int k) {
      if (k < 8) PIN(of[k]);
      else asm volatile("" : "+v"(ofl[k - 8]), "+v"(ofh[k - 8]));
    };
    auto o_get = [&](int k) { return k < 8 ? of[k] : (u32x4){ofl[k - 8][0], ofl[k - 8][1], ofh[k - 8][0], ofh[k - 8][1]}; };
    auto o_bad = [&](int k) {
      const u32x4 v = o_get(k);
      return __any(max(max(v[0], v[1]), max(v[2], v[3])) == SENT);
    };
    f32x2 cb[2][4];
    auto base = [&](int e, u32x4 bv) {
      const float omg = 1.0f - gt[e];
#pragma unroll
      for (int q = 0; q < 4; ++q) cb[e][q] = (f32x2){bflo(bv[q]), bfhi(bv[q])} * (f32x2){omg, omg};
    };
    u32x4 x0_;
    f32x4 ac2[5];
#pragma unroll
    for (int f = 0; f < 5; ++f) ac2[f] = (f32x4){0.f, 0.f, 0.f, 0.f};
    __builtin_amdgcn_sched_barrier(0);
    constexpr int GS[5] = {0, 2, 5, 8, 10};
    auto vleft = [](int k) { return k < 8 ? 11 - k : k == 8 ? 2 : 0; };
    WAITV(10);
    o_pin(0);
    o_pin(1);
    bool bad = o_bad(0) | o_bad(1);
#pragma unroll
    for (int g = 0; g < 4; ++g) {
      if (__builtin_expect(bad, 0)) {
        WAITV(0);
#pragma unroll 1
        for (unsigned spins = 0; spins < SPIN_CAP; ++spins) {
          bool any = false;
#pragma unroll
          for (int k2 = GS[g]; k2 < 10; ++k2) {
            o_pin(k2);
            if (o_bad(k2)) {
              o_again(k2);
              if (k2 < GS[g + 1]) any = true;
            }
          }
          WAITV(0);
          if (!any) break;
        }
      }
      if (g == 2 && __builtin_expect(yjob, 0)) y_issue(false);
      unsigned mxv = 0;
      const int nm = (GS[g + 1] - GS[g]) * 5, nn = g < 3 ? GS[g + 2] - GS[g + 1] : 0;
#pragma unroll
      for (int i = 0; i < nm; ++i) {
        const int k = GS[g] + i / 5, f = i % 5;
        ac2[f] = mfma16(wq[k % WPD][f], o_get(k), ac2[f]);
        if (k + WPD < 10) wq[k % WPD][f] = *(const u32x4*)(wb + ((k + WPD) * 5 + f) * 1024);
        SB();
        const int j = i - (nm - 2 - 2 * nn);
        if (j >= 0 && j < 2 * nn) {
          const int kn = GS[g + 1] + j / 2;
          if (!(j & 1)) {
            WAITV_(vleft(kn));
            o_pin(kn);
          }
          const u32x4 nv = o_get(kn);
          mxv = max(max(mxv, nv[(j & 1) * 2]), nv[(j & 1) * 2 + 1]);
        }
        if (nn && j == 2 * nn) bad = __any(mxv == SENT);
        if (k == 2 && f == 0) x0_ = *(const u32x4*)(smem + L_XT + tid * 16);
        if (g == 3 && i < 8 && (i & 1)) {
          const float omg = 1.0f - gt[0];
          cb[0][i >> 1] = (f32x2){bflo(x0_[i >> 1]), bfhi(x0_[i >> 1])} * (f32x2){omg, omg};
          PIN(cb[0][i >> 1]);
        }
        SB();
      }
    }
#pragma unroll
    for (int f = 0; f < 5; ++f) *(f32x4*)(smem + pwr + f * 64) = ac2[f];
    if (__builtin_expect(yjob, 0)) {
#pragma unroll 1
      for (unsigned spins = 0; spins < SPIN_CAP; ++spins) {
        if (y_done()) break;
        y_issue(true);
      }
    }
    if (wave < 2) {
      gate(1, gate_lds(lrow[1]));
      base(1, *(const u32x4*)(smem + L_XT + 8192 + tid * 16));
#pragma unroll
      for (int q = 0; q < 4; ++q) PIN(cb[1][q]);
    }
    WAITL0();
    BARRIER();
#pragma unroll
    for (int e = 0; e < 2; ++e) {
      if (e == 1 && wave >= 2) break;
      const char* pp = smem + prd[e];
      const f32x4 a0 = *(const f32x4*)pp + *(const f32x4*)(pp + 64 * P_STRIDE);
      const f32x4 a1 = *(const f32x4*)(pp + 16) + *(const f32x4*)(pp + 64 * P_STRIDE + 16);
      if (__builtin_expect(routed, 0)) {
        if (lrow[e] == l0 || lrow[e] == l1) {
          const int c8 = (prd[e] - lrow[e] * P_STRIDE) >> 5;
          const u32x4 ys = *(const u32x4*)(smem + L_JUNK + (lrow[e] == l0 ? 0 : 160) + c8 * 16);
          u32x4 bv = *(const u32x4*)(smem + L_XT + e * 8192 + tid * 16);
#pragma unroll
          for (int q = 0; q < 4; ++q) bv[q] = pk2(bflo(bv[q]) + bflo(ys[q]), bfhi(bv[q]) + bfhi(ys[q]));
          base(e, bv);
        }
      }
      u32x4 v;
#pragma unroll
      for (int q = 0; q < 4; ++q) {
        const f32x2 a = q < 2 ? (f32x2){a0[2 * q], a0[2 * q + 1]} : (f32x2){a1[2 * q - 4], a1[2 * q - 3]};
        const f32x2 r = __builtin_elementwise_fma(a, (f32x2){gt[e], gt[e]}, cb[e][q]);
        v[q] = pk2(r[0], r[1]);
      }
      __builtin_amdgcn_raw_buffer_store_b128(v, rout, xoff[e], 0, 0);
    }
  }
}

extern "C" void kernel_launch(void* const* d_in, const int* in_sizes, int n_in, void* d_out, int out_size, void* d_ws,
                              size_t ws_size, hipStream_t stream) {
  kf<<<256, 512, 0, stream>>>((const u16*)d_in[0], (const u16*)d_in[1], (const u16*)d_in[2], (const u16*)d_in[4],
                              (const u16*)d_in[8], (const u16*)d_in[5], (const u16*)d_in[6], (const u16*)d_in[7],
                              (char*)d_ws, (u16*)d_out);
}
```

```cpp
#include <hip/hip_runtime.h>
#include <stdint.h>

typedef unsigned short u16;
typedef unsigned long long u64;
typedef __attribute__((ext_vector_type(8))) __bf16 bf16x8;
typedef __attribute__((ext_vector_type(2))) __bf16 bf16x2;
typedef __attribute__((ext_vector_type(4))) short s16x4;
typedef __attribute__((ext_vector_type(4))) float f32x4;
typedef __attribute__((ext_vector_type(2))) float f32x2;
typedef __attribute__((ext_vector_type(4))) unsigned u32x4;
typedef __attribute__((ext_vector_type(2))) unsigned u32x2;
typedef __attribute__((ext_vector_type(10))) unsigned u32x10;
#define GAS __attribute__((address_space(1)))
#define LAS __attribute__((address_space(3)))
typedef GAS unsigned gu32;

#define S_LEN 2048
#define DM 640
#define NQKV 1920
#define FF 1728

#define WS_OF 0
#define WS_WOF 2621440
#define WS_E (WS_WOF + 819200)
#define WS_F2 (WS_E + 256)
#define WS_F (WS_F2 + 32768)
#define WS_K (WS_F + 28672)
#define WS_R (WS_K + 36864)
#define WS_A (WS_R + 256)
#define WS_YP (WS_A + 4096)
#define TAG_STEP 0x9E3779B9u
#define SENT 0xFFFFFFFFu
#define SPIN_CAP (1u << 18)
#define GO_SLEEP 12
#define NPRE 3
#define PUB_IT 1
#define ETA_G 25
#define ETA_F 30
#define D1 350
#define SPOS 10
#define D2 75
#define REARGS 0
#define TMAP 0
#define PRD 10
#define FST 0
#define YT 0
#define NW1 16
#define PRIO 0
#define OT 0
#define OPOL " sc1"
#define YPOS 1
#define XB 160
#define FMAP 1
#define KSW 0
#define FDL 0
#define TCY 2750
#define OPOL2 " sc1"
#define OTI 3
#define OTA 0
#define WPOS 3
#define NPRE2 WPD
#define CTS 32
#define A1 1
#define A3 2
#define JNT ""
#define XNT ""
#define PNT " nt"
#define XPF 0
#define WB8 2
#define MKA 5
#define WPD 7
#define RLX_AGENT __ATOMIC_RELAXED, __HIP_MEMORY_SCOPE_AGENT

__device__ __forceinline__ float bflo(unsigned u) { return __uint_as_float(u << 16); }
__device__ __forceinline__ float bfhi(unsigned u) { return __uint_as_float(u & 0xFFFF0000u); }
__device__ __forceinline__ unsigned pk2(float a, float b) {
  f32x2 v = {a, b};
  return __builtin_bit_cast(unsigned, __builtin_convertvector(v, bf16x2));
}
__device__ __forceinline__ float rbf(float f) { return bflo(pk2(f, f)); }
__device__ __forceinline__ unsigned perm_lo(unsigned hi_src, unsigned lo_src) {
  return __builtin_amdgcn_perm(hi_src, lo_src, 0x05040100u);
}
__device__ __forceinline__ unsigned perm_hi(unsigned hi_src, unsigned lo_src) {
  return __builtin_amdgcn_perm(hi_src, lo_src, 0x07060302u);
}
__device__ __forceinline__ f32x4 mfma16(u32x4 a, u32x4 b, f32x4 c) {
  return __builtin_amdgcn_mfma_f32_16x16x32_bf16(__builtin_bit_cast(bf16x8, a), __builtin_bit_cast(bf16x8, b), c, 0, 0, 0);
}
__device__ __forceinline__ u32x2 tr_read(const char* p) {
  return __builtin_bit_cast(u32x2, __builtin_amdgcn_ds_read_tr16_b64_v4i16((LAS s16x4*)(p)));
}
#define BARRIER() asm volatile("s_barrier" ::: "memory")
#define WAITV(n) asm volatile("s_waitcnt vmcnt(" #n ")" ::: "memory")
#define WAITV_(n) asm volatile("s_waitcnt vmcnt(%0)" ::"i"(n) : "memory")
#define WAITL0() asm volatile("s_waitcnt lgkmcnt(0)" ::: "memory")

__device__ __forceinline__ void top2_mrg(unsigned& k1, unsigned& k2, unsigned b1, unsigned b2) {
  k2 = max(max(k2, b2), min(k1, b1));
  k1 = max(k1, b1);
}
template <int CTRL>
__device__ __forceinline__ void top2_dpp(unsigned& k1, unsigned& k2) {
  const unsigned b1 = (unsigned)__builtin_amdgcn_update_dpp(0, (int)k1, CTRL, 0xF, 0xF, true);
  const unsigned b2 = (unsigned)__builtin_amdgcn_update_dpp(0, (int)k2, CTRL, 0xF, 0xF, true);
  top2_mrg(k1, k2, b1, b2);
}
__device__ __forceinline__ void top2_row(unsigned& k1, unsigned& k2) {
  top2_dpp<0xB1>(k1, k2);
  top2_dpp<0x4E>(k1, k2);
  top2_dpp<0x141>(k1, k2);
  top2_dpp<0x140>(k1, k2);
}
__device__ __forceinline__ void top2_all(unsigned& k1, unsigned& k2) {
  top2_row(k1, k2);
  {
    auto a = __builtin_amdgcn_permlane16_swap(k1, k1, false, false);
    auto c = __builtin_amdgcn_permlane16_swap(k2, k2, false, false);
    k1 = a[0];
    k2 = c[0];
    top2_mrg(k1, k2, a[1], c[1]);
  }
  {
    auto a = __builtin_amdgcn_permlane32_swap(k1, k1, false, false);
    auto c = __builtin_amdgcn_permlane32_swap(k2, k2, false, false);
    k1 = a[0];
    k2 = c[0];
    top2_mrg(k1, k2, a[1], c[1]);
  }
}
__device__ __forceinline__ void touch_line(const char* p, unsigned lds_off) {
  asm volatile("s_mov_b32 m0, %1\n\ts_nop 0\n\tglobal_load_lds_dword %0, off" ::"v"(p), "s"(lds_off) : "memory");
}
__device__ __forceinline__ void dma16(unsigned voff, const void* sbase, unsigned lds_off) {
  asm volatile("s_mov_b32 m0, %2\n\ts_nop 0\n\tglobal_load_lds_dwordx4 %0, %1" ::"v"(voff), "s"(sbase), "s"(lds_off) : "memory");
}
__device__ __forceinline__ void dma16_sc1(unsigned voff, const void* sbase, unsigned lds_off) {
  asm volatile("s_mov_b32 m0, %2\n\ts_nop 0\n\tglobal_load_lds_dwordx4 %0, %1 sc1" ::"v"(voff), "s"(sbase), "s"(lds_off) : "memory");
}
__device__ __forceinline__ void bdma16(unsigned voff, u32x4 srd, unsigned soff, unsigned lds_off) {
  asm volatile("s_mov_b32 m0, %3\n\ts_nop 0\n\tbuffer_load_dwordx4 %0, %1, %2 offen lds" ::"v"(voff), "s"(srd), "s"(soff), "s"(lds_off) : "memory");
}
__device__ __forceinline__ u32x4 make_srd(const void* p, unsigned bytes) {
  return (u32x4){(unsigned)(uintptr_t)p, (unsigned)((uintptr_t)p >> 32) & 0xFFFFu, bytes, 0x00020000u};
}
__device__ __forceinline__ float key_val(unsigned key) {
  unsigned u = key >> 16;
  u ^= (u & 0x8000u) ? 0x8000u : 0xFFFFu;
  return bflo(u);
}
__device__ __forceinline__ float fmx(float a, float b) { return __builtin_elementwise_maximum(a, b); }
#define SB() __builtin_amdgcn_sched_barrier(0)
__device__ __forceinline__ f32x4 mfma16h(u32x2 a, u32x2 b, f32x4 c) {
  return __builtin_amdgcn_mfma_f32_16x16x16bf16_1k(__builtin_bit_cast(s16x4, a), __builtin_bit_cast(s16x4, b), c, 0, 0, 0);
}

#define SLOT_B 43008
#define L_WO SLOT_B
#define L_EARLY (3 * SLOT_B)
#define L_PIECE L_EARLY
#define L_CAND (L_VAR + 128)
#define L_TOUCH (L_EARLY + 12288)
#define L_JUNK (L_WO + 102400)
#define L_VAR (L_JUNK + 384)
#define L_CAND2 (L_VAR + 48)
#define L_GT (L_VAR + 256)
#define L_FS (L_GT + 1024)
#define L_XT (L_FS + 1024)
#define L_TOTAL (L_XT + 10240)
#define CODE_LINES 251

__device__ __forceinline__ void res_wait(char* ws, char* smem, int lane, unsigned tag, int which) {
  const __amdgpu_buffer_rsrc_t r = __builtin_amdgcn_make_buffer_rsrc((void*)(ws + WS_R), 0, 256, 0x00020000);
  u32x4 v;
#pragma unroll 1
  for (unsigned spins = 0; spins < SPIN_CAP; ++spins) {
    asm volatile("" ::: "memory");
    v = __builtin_amdgcn_raw_buffer_load_b128(r, which * 128, 0, 16);
    if (__all((v[1] == tag) & (v[3] == tag))) break;
    __builtin_amdgcn_s_sleep(2);
  }
  if (lane == 0 && which == 0) *(u32x2*)(smem + L_VAR + 32) = (u32x2){v[0], v[2]};
  if (lane == 0 && which == 1) *(u32x4*)(smem + L_CAND2) = v;
}
__device__ __forceinline__ float dot2(unsigned a, unsigned b, float c) {
  return __builtin_amdgcn_fdot2_f32_bf16(__builtin_bit_cast(bf16x2, a), __builtin_bit_cast(bf16x2, b), c, false);
}
template <int CTRL>
__device__ __forceinline__ float dpp_add(float v) {
  return v + __int_as_float(__builtin_amdgcn_update_dpp(0, __float_as_int(v), CTRL, 0xF, 0xF, true));
}
__device__ __forceinline__ float row_sum(float v) {
  v = dpp_add<0xB1>(v);
  v = dpp_add<0x4E>(v);
  v = dpp_add<0x141>(v);
  return dpp_add<0x140>(v);
}

template <int N> struct IC { static constexpr int value = N; };
#define PIN(v) asm volatile("" : "+v"(v))
__global__ __launch_bounds__(512, 2) void kf(const u16*, const u16*, const u16*, const u16*, const u16*, const u16*, const u16*, const u16*, char*, u16*) {
  __shared__ __attribute__((aligned(16))) char smem[L_TOTAL];
  typedef const __attribute__((address_space(4))) u64* karg_t;
  const karg_t kp0 = (karg_t)__builtin_amdgcn_kernarg_segment_ptr();
  u64 pc;
  asm volatile("s_load_dword s100, %1, 0x0\n\ts_load_dword s101, %1, 0x40\n\ts_getpc_b64 %0" : "=s"(pc) : "s"(kp0) : "s100", "s101");
  const u16 *x, *wqkv, *wo, *w_r, *gate_w, *w_gate, *w_up, *w_down;
  char* ws;
  u16* out;
  auto args = [&]() {
    SB();
    karg_t kp = kp0;
    asm volatile("" : "+s"(kp));
    x = (const u16*)kp[0]; wqkv = (const u16*)kp[1]; wo = (const u16*)kp[2]; w_r = (const u16*)kp[3]; gate_w = (const u16*)kp[4];
    w_gate = (const u16*)kp[5]; w_up = (const u16*)kp[6]; w_down = (const u16*)kp[7]; ws = (char*)kp[8]; out = (u16*)kp[9];
  };
  const int tid = threadIdx.x, b = blockIdx.x;
  {
    const int ln = ((b >> 3) & (CTS - 1)) + CTS * tid;
    if (ln < CODE_LINES)
      touch_line((const char*)((pc & ~255ull) + ln * 128), (unsigned)(uintptr_t)(LAS char*)smem + L_TOUCH);
  }
  unsigned tag;
  auto get_tag = [&]() {
    asm volatile("s_load_dword %0, %1, 0x0\n\ts_waitcnt lgkmcnt(0)" : "=s"(tag) : "s"(ws + WS_E) : "memory");
    tag += TAG_STEP;
  };
  const int lane = tid & 63;
  const int wave = __builtin_amdgcn_readfirstlane(tid >> 6);
  const int xcd = b & 7, jj = b >> 3;
  const int c15 = lane & 15, g4 = lane >> 4;
  const bool gemm_blk = jj < 28;
  const int q_ = (xcd & 3) * 28 + jj;
  const int rg = q_ >> 2, hg = (xcd >> 2) * 4 + (q_ & 3);
  const int par = rg >= 14, gi = rg - par * 14;
  const int tb = min(73 * gi, 944);
  const int tlo = gi == 0 ? 0 : 73 * gi + 7;
  const int lg = jj >> 2;
  const bool keep = !TMAP || (lg & 3) == 3;
  const int gc = keep ? gi : (xcd & 1) * 7 + (lg & 3) + (xcd & 4), tbc = min(73 * gc, 944);
  const int rt2 = gemm_blk ? par * 16 + gc : (xcd & 2) * 8 + 14 + (xcd & 1), nt2 = keep ? hg : (lg & 4) + (jj & 3);
  const unsigned lds0 = (unsigned)(uintptr_t)(LAS char*)smem;
  auto wo_dma = [&](auto n_, int i0, int st, int lo) {
    const u32x4 srd = make_srd(ws + WS_WOF + nt2 * 102400, 102400);
    int g = __builtin_amdgcn_readfirstlane((i0 + (TMAP ? ((lg & 3) * (25 - lo)) >> 2 : (lg * (25 - lo)) >> 3)) * 4096);
#pragma unroll
    for (int i = 0; i < decltype(n_)::value; ++i) {
      if (g >= 102400) g -= (25 - lo) * 4096;
      asm volatile("s_add_u32 m0, %3, %2\n\ts_nop 0\n\tbuffer_load_dwordx4 %0, %1, %2 offen sc1 lds\n\t"
                   "buffer_load_dwordx4 %0, %1, %2 offen offset:1024 sc1 lds\n\tbuffer_load_dwordx4 %0, %1, %2 offen offset:2048 sc1 lds\n\t"
                   "buffer_load_dwordx4 %0, %1, %2 offen offset:3072 sc1 lds" ::"v"(lane * 16), "s"(srd), "s"(g), "s"(lds0 + L_WO)
                   : "memory", "scc");
      g += st * 4096;
    }
  };

  int od[5];
  const int pa = tb + c15 - 64 * gi;
  const bool pv = tb + c15 >= tlo;
  auto prepO = [&]() {
    const int h = hg * 4 + (wave & 3);
    const int b0 = (par * 64 + 56 + (pa >> 4)) * 20480, b1 = (par * 64 + gi * 4) * 20480;
    od[0] = (pv && lane < 32) ? b0 + (2 * h + g4) * 256 + (pa & 15) * 16 : 0x7FFFFF00;
    od[1] = b1 + (lane >> 5) * 20480 + (2 * h + (g4 & 1)) * 256 + c15 * 16;
    od[2] = od[1] + 2 * 20480;
    od[3] = b1 + g4 * 20480 + 16384 + h * 128 + c15 * 8;
    od[4] = (pv && g4 == 3) ? b0 + 16384 + h * 128 + (pa & 15) * 8 : 0x7FFFFF00;
#pragma unroll
    for (int e = 0; e < 5; ++e) PIN(od[e]);
  };
  auto row_t = [&](int row) {
    const int a = (xcd & 1) * 64 + row;
    return gemm_blk ? tbc + 16 + row : a + (a < 16 ? 0 : (a - 7) / 9) * 64;
  };
  int adr[3], glo;
  u64 rel;
  auto tile_off = [&](int row) { return ((2 * row_t(row) + (rt2 >> 4)) * DM + nt2 * 80) * 2; };
  auto prepB = [&]() {
    const int row = (wave & 3) * 16 + c15;
    if ((wave >> 2) == FST) {
#pragma unroll
      for (int e = 0; e < 3; ++e) {
        const int c = min(e * 64 + lane, 159), r = c / 10;
        adr[e] = tile_off((wave & 3) * 16 + r) + (c - r * 10) * 16;
      }
    } else {
      adr[2] = tile_off(row);
      adr[0] = adr[2] + (nt2 & 1) * 32 + (g4 & 1) * 32 + (g4 >> 1) * 16;
      adr[1] = adr[2] + ((nt2 & 1) ? 0 : 128) + g4 * 8;
    }
    PIN(adr[0]); PIN(adr[1]); PIN(adr[2]);
    glo = L_GT + row * 16 + (((gemm_blk ? tbc : 0) + row) & 1) * 8;
    PIN(glo);
    {
      const int a0 = (xcd & 1) * 64 + (wave & 3) * 16, gl = (xcd & 1) ? 6 + (lane >> 3) : min(lane >> 3, 6);
      rel = __builtin_amdgcn_ballot_w64(((lane >> 2) & 1) == ((wave >> 2) ^ (jj & KSW)) && (gemm_blk ? lane < 8 : gl >= (a0 < 16 ? 0 : (a0 - 7) / 9) && gl <= (a0 + 8) / 9));
      asm volatile("" : "+s"(rel));
    }
  };
  auto lrow_of = [&](unsigned key) {
    const int i = 2047 - (int)(key & 0xFFFFu);
    const int t = i >> 1, g = max(t - 7, 0) / 73, rho = t - min(73 * g, 944);
    return (i & 1) == (rt2 >> 4) ? (rho >= 16 ? g * 64 + rho - 16 : 896 + t - 64 * g) - (rt2 & 15) * 64 : -1;
  };
  auto rows_job = [&]() {
    if (lane < 2) {
      const unsigned key = *(const unsigned*)(smem + L_VAR + 32 + lane * 4);
      *(int*)(smem + L_VAR + 40 + lane * 4) = lrow_of(key);
      *(float*)(smem + L_JUNK + 256 + lane * 4) = rbf(__builtin_amdgcn_rcpf(1.0f + __expf(-key_val(key))));
    }
  };
  auto gate_off = [&](int row) {
    const int p2 = rt2 >> 4, tg = row_t(row);
    return (tg < 896 ? (p2 * 112 + (tg >> 3)) * 128 + (tg & 7) * 8 : (224 + p2 * 32 + ((tg - 896) >> 2)) * 128 + (tg & 3) * 8) + 64;
  };
  auto flag_off = [&]() {
    const int g = gemm_blk ? gc : (xcd & 1) ? 6 + (lane >> 3) : min(lane >> 3, 6);
    return (((rt2 >> 4) * 14 + g) * 8 + (lane & 7)) * 128 + 56;
  };
#define rfl __builtin_amdgcn_make_buffer_rsrc((void*)(ws + WS_F), 0, 28672, 0x00020000)
  auto gate_dma = [&](int go) { dma16_sc1(go & ~15, ws + WS_K, lds0 + L_GT); };
  auto now = []() { return (unsigned)__builtin_amdgcn_s_memrealtime(); };
  auto flag_wait = [&](int foff) {
#pragma unroll 1
    for (unsigned spins = 0; spins < SPIN_CAP; ++spins) {
      asm volatile("" ::: "memory");
      const u32x4 v = __builtin_amdgcn_raw_buffer_load_b128(rfl, foff, 0, 16);
      if ((v[1] == tag) & (v[3] == tag) & (foff != 0x7FFFFF00)) {
        *(u32x4*)(smem + L_FS + lane * 16) = v;
        foff = 0x7FFFFF00;
      }
      if (__all(foff == 0x7FFFFF00)) break;
      __builtin_amdgcn_s_sleep(8);
    }
  };
#define ro __builtin_amdgcn_make_buffer_rsrc((void*)(ws + WS_OF), 0, S_LEN * DM * 2, 0x00020000)

  if (__builtin_expect(gemm_blk, 1)) {
    if (__builtin_expect(wave >= 4, 1)) {
      const int hw = wave - 4;
      const int rot = ((jj >> 2) % 5) * 2 + (xcd & 1);
      int voffA = (lane >> 3) * (DM * 4) + (((lane & 7) ^ (((hw & 1) * 4 + g4) & 7)) * 16);
      int sA[3];
#pragma unroll
      for (int v = 0; v < 3; ++v) {
        sA[v] = ((tb + 8 * (hw + 4 * v)) * 2 + par) * (DM * 2);
      }
      const int hi = lane >> 5;
      int pair = ((lane & 31) >> 1) ^ (hi | ((hw & 1) << 1) | ((hw >> 1) << 2));
      const int sec = (pair >= 5) + (pair >= 10);
      int voffW = pair == 15 ? 0x7FFFFF00 : hi * (NQKV * 2) + sec * (1280 - 160) + hg * 160 + pair * 32 + ((lane & 1) ^ (sec == 2)) * 16;
      const int n0 = (hw & 1) + 4 * (hw >> 1);
      PIN(voffA); PIN(voffW);
      args();
      const u32x4 rsx = make_srd(x, S_LEN * DM * 2), rsw = make_srd(wqkv, DM * NQKV * 2);
      auto issue = [&](int i, int buf) {
        int kc = rot + (i < 5 ? 2 * i : 2 * i - 9);
        if (kc >= 10) kc -= 10;
        const unsigned dst = lds0 + buf * SLOT_B;
#pragma unroll
        for (int v = 0; v < 3; ++v)
          if (v < 2 || hw < 2) bdma16(voffA, rsx, sA[v] + kc * 128, dst + (hw + 4 * v) * 1024);
        const int so = (kc * 64 + 2 * n0) * (NQKV * 2);
#pragma unroll
        for (int u = 0; u < 8; ++u) {
          const int dn = 2 * (u & 1) + 8 * (u >> 1);
          bdma16(voffW, rsw, so + dn * 2 * (NQKV * 2), dst + 10240 + (n0 + dn) * 1024);
        }
      };
      const int qp = rg * 8 + hg;
      issue(0, 0);
      SB();
      int md = (par * 64 + gi * 4 + 2 * (hw & 1) + (lane >> 5)) * 20480 + 16384 + hg * 512 + (lane & 31) * 16;
      PIN(md);
      int so_ = hw == 0 ? flag_off() : gate_off(lane);
      PIN(so_);
      prepB();
      __builtin_amdgcn_sched_barrier(0);
      issue(1, 1);
      get_tag();
      WAITV(10);
      BARRIER();
      issue(2, 2);
      WAITV(10);
      BARRIER();
      const unsigned r0_ = now(), c0_ = (unsigned)__builtin_amdgcn_s_memtime();
      {
        int buf = 0;
#pragma unroll 1
        for (int i = 3; i < 9; ++i) {
          const bool ex = i == PUB_IT + 3 && hw == 2;
          if (ex && lane == 0) __hip_atomic_store((gu32*)(ws + WS_F2 + b * 128), tag, RLX_AGENT);
          issue(i, buf);
          if (OTA && i >= 3 && i < 7)
            asm volatile("s_load_dword s100, %0, 0x0\n\ts_load_dword s100, %0, 0xa00\n\ts_load_dword s100, %0, 0x1400\n\ts_load_dword s100, %0, 0x1e00\n\t"
                         "s_load_dword s100, %0, 0x9c\n\ts_load_dword s100, %0, 0xa9c\n\ts_load_dword s100, %0, 0x149c\n\ts_load_dword s100, %0, 0x1e9c\n\t"
                         "s_load_dword s100, %0, 0x50\n\ts_load_dword s100, %0, 0xa50\n\ts_load_dword s100, %0, 0x1450\n\ts_load_dword s100, %0, 0x1e50" ::"s"(
                             (const char*)out + tile_off(hw * 16 + (i - 3) * 4))
                         : "s100");
          if (!OTA && i == OTI)
            asm volatile("s_load_dword s100, %0, 0x0\n\ts_load_dword s100, %0, 0xa00\n\ts_load_dword s100, %0, 0x1400\n\ts_load_dword s100, %0, 0x1e00\n\t"
                         "s_load_dword s100, %0, 0x2800\n\ts_load_dword s100, %0, 0x3200\n\ts_load_dword s100, %0, 0x3c00\n\ts_load_dword s100, %0, 0x4600" ::"s"(
                             (const char*)out + tile_off(hw * 8 + (nt2 & 1) * 32) + ((nt2 & 1) ? 0 : 128))
                         : "s100");
          if (ex) WAITV(11); else WAITV(10);
          buf = buf == 2 ? 0 : buf + 1;
          BARRIER();
        }
      }
      if (hw == 3) {
        if (lane < 2) dma16_sc1(lane * 128, ws + WS_R, lds0 + L_CAND);
        if (lane < 8) *(unsigned*)(smem + L_VAR + lane * 4) = 0u;
      }
      issue(9, 0);
      auto mark = [&](int e0, int e1) {
        const u32x4 sent4 = {SENT, SENT, SENT, SENT};
        const int mo_ = (par * 64 + gi * 4 + (hw & 1) * 2) * 20480 + hg * 2048;
#pragma unroll
        for (int e = e0; e < e1; ++e) {
          if (e == 0) __builtin_amdgcn_raw_buffer_store_b128(sent4, ro, md, 0, 16);
          else __builtin_amdgcn_raw_buffer_store_b128(sent4, ro, lane * 16 + ((e - 1) & 1) * 1024, mo_ + ((e - 1) >> 1) * 20480, 16);
        }
        SB();
      };
      auto words = [&]() {
        const u32x4 rv = *(const u32x4*)(smem + L_CAND + (lane & 1) * 16);
        if (__all((rv[1] == tag) & (rv[3] == tag))) {
          if (tid == 256) *(u32x2*)(smem + L_VAR + 32) = (u32x2){rv[0], rv[2]};
        } else {
          res_wait(ws, smem, lane, tag, 0);
          res_wait(ws, smem, lane, tag, 1);
        }
      };
      if (hw >= 2) {
        mark(0, MKA);
        WAITV_(10 + MKA);
        BARRIER();
        mark(MKA, 5);
        WAITV(5);
        BARRIER();
        unsigned t0_ = now();
        t0_ += (unsigned)((float)(t0_ - r0_) * (float)TCY * __builtin_amdgcn_rcpf((float)((unsigned)__builtin_amdgcn_s_memtime() - c0_)));
        WAITV(0);
        __builtin_amdgcn_raw_buffer_store_b128((u32x4){t0_, tag, t0_, tag}, rfl, lane < 4 ? qp * 128 + (hw & 1) * 64 + lane * 16 : 0x7FFFFF00, 0, 16);
        if (hw == 2) rows_job();
      } else {
        WAITV(10);
        BARRIER();
        words();
        wo_dma(IC<WB8>{}, hw, 2, 25);
        WAITV_(4 * WB8);
        WAITL0();
        BARRIER();
        if (hw == 1) gate_dma(so_);
        wo_dma(IC<SPOS>{}, 2 * WB8 + hw, 2, 2 * WB8);
        if (hw == 0 && lane < 8) dma16_sc1(so_, ws + WS_F, lds0 + L_FS);
        wo_dma(IC<12 - WB8 - SPOS>{}, 2 * (WB8 + SPOS) + hw, 2, 2 * WB8);
        if (hw == 0) wo_dma(IC<1>{}, 24, 2, 2 * WB8);
        WAITV(0);
      }
      WAITL0();
      BARRIER();
    } else {
      const int xb_ = b - XB;
      const bool wjob = wave < 2 || (wave == 2 && (unsigned)xb_ < 64u);
      const int tj0 = wave < 2 ? gi * 64 + hg * 8 + wave * 4 : 896 + (xb_ & 31) * 4, pj = wave < 2 ? par : xb_ >> 5;
      u32x4 xs[5], wv[4];
      int jo0 = lane * 16, jo1 = g4 * 2560 + c15 * 16, jo2 = c15 * 16;
      PIN(jo0); PIN(jo1); PIN(jo2);
      args();
      if (wjob) {
        const u16* xr = x + (2 * tj0 + pj) * DM;
        asm volatile("global_load_dwordx4 %0, %9, %12" JNT "\n\tglobal_load_dwordx4 %1, %9, %12 offset:2560" JNT "\n\t"
                     "global_load_dwordx4 %2, %9, %13" JNT "\n\tglobal_load_dwordx4 %3, %9, %13 offset:2560" JNT "\n\t"
                     "global_load_dwordx4 %4, %10, %12 offset:1024" JNT "\n\tglobal_load_dwordx4 %5, %9, %14\n\t"
                     "global_load_dwordx4 %6, %11, %14 offset:1024\n\tglobal_load_dwordx4 %7, %9, %15\n\tglobal_load_dwordx4 %8, %11, %15 offset:1024"
                     : "=&v"(xs[0]), "=&v"(xs[1]), "=&v"(xs[2]), "=&v"(xs[3]), "=&v"(xs[4]), "=&v"(wv[0]), "=&v"(wv[1]), "=&v"(wv[2]), "=&v"(wv[3])
                     : "v"(jo0), "v"(jo1), "v"(jo2), "s"(xr), "s"(xr + 4 * DM), "s"(w_r), "s"(gate_w)
                     : "memory");
      }
      f32x4 acc[5][4];
#pragma unroll
      for (int m = 0; m < 5; ++m)
#pragma unroll
        for (int f = 0; f < 4; ++f) acc[m][f] = (f32x4){0.f, 0.f, 0.f, 0.f};
      const int wks = b % 20, wT = (b / 20) * 4 + wave;
      if (b < 200) {
        const int kh = wks >= 10, s = wks - kh * 10, pr = lane >> 3, g = wave, hi = pr >> 2;
        const int row = (s < 8 ? (2 * (kh * 8 + s) + (g >> 1)) * 20 + 4 * (g & 1) + 8 * hi : (kh * 16 + (s - 8) * 8 + hi * 4 + g) * 20 + 16) + (pr & 3);
        asm volatile("s_mov_b32 m0, %2\n\ts_nop 0\n\tglobal_load_lds_dwordx4 %0, %1" PNT ::"v"((row * DM + (b / 20) * 64 + (lane & 7) * 8) * 2), "s"(wo), "s"(lds0 + L_PIECE + wave * 1024) : "memory");
      }
      const __amdgpu_buffer_rsrc_t rk = __builtin_amdgcn_make_buffer_rsrc((void*)(ws + WS_K), 0, 36864, 0x00020000);
      const int ko = c15 != 0 ? 0x7FFFFF00 : wave < 2 ? (rg * 8 + hg) * 128 + (wave * 4 + g4) * 8 : (224 + xb_) * 128 + g4 * 8;
      auto rows4 = [&](float (&p)[5]) {
        const auto a = __builtin_amdgcn_permlane32_swap(__float_as_uint(p[0]), __float_as_uint(p[2]), false, false);
        const auto c = __builtin_amdgcn_permlane32_swap(__float_as_uint(p[1]), __float_as_uint(p[3]), false, false);
        const auto e = __builtin_amdgcn_permlane16_swap(__float_as_uint(__uint_as_float(a[0]) + __uint_as_float(a[1])),
                                                        __float_as_uint(__uint_as_float(c[0]) + __uint_as_float(c[1])), false, false);
        return row_sum(__uint_as_float(e[0]) + __uint_as_float(e[1]) + p[4]);
      };
      get_tag();
      if (wjob) {
        if (b < 200) WAITV_(3 + XPF); else WAITV_(2 + XPF);
#pragma unroll
        for (int i = 0; i < 5; ++i) PIN(xs[i]);
        PIN(wv[0]); PIN(wv[1]);
        float p[5];
#pragma unroll
        for (int i = 0; i < 5; ++i) {
          p[i] = 0.f;
#pragma unroll
          for (int q = 0; q < 4; ++q) p[i] = dot2(xs[i][q], wv[i == 4][q], p[i]);
        }
        const float r_ = rows4(p);
        unsigned u = pk2(r_, r_) & 0xFFFFu;
        u ^= (u & 0x8000u) ? 0xFFFFu : 0x8000u;
        const unsigned key = (u << 16) | (unsigned)(2047 - (2 * (tj0 + g4) + pj));
        __builtin_amdgcn_raw_buffer_store_b64((u32x2){key, tag}, rk, ko, 0, 16);
        SB();
      }
      if (wjob) {
        WAITV(1);
        PIN(wv[2]); PIN(wv[3]);
        float p[5];
#pragma unroll
        for (int i = 0; i < 5; ++i) {
          p[i] = 0.f;
#pragma unroll
          for (int q = 0; q < 4; ++q) {
            const unsigned gw = wv[2 + (i == 4)][q];
            p[i] = dot2(pk2(bflo(xs[i][q]) * bflo(gw), bfhi(xs[i][q]) * bfhi(gw)), 0x3F803F80u, p[i]);
          }
        }
        const float g_ = rbf(rows4(p));
        __builtin_amdgcn_raw_buffer_store_b64((u32x2){__float_as_uint(rbf(1.0f / (1.0f + __expf(-g_)))), tag}, rk, c15 != 0 ? ko : ko + 64, 0, 16);
        SB();
      }
      prepO();
      const int p4 = lane & 3;
      f32x4 limA, limB;
      const int e0 = c15 - 4 * g4;
#pragma unroll
      for (int i = 0; i < 4; ++i) {
        limA[i] = e0 - i <= -9 ? 0.f : -__builtin_inff();
        limB[i] = (e0 - i >= 0 && e0 - i <= 7) ? 0.f : -__builtin_inff();
      }
      unsigned mA01 = (e0 <= -9 ? 0xFFFFu : 0u) | (e0 - 1 <= -9 ? 0xFFFF0000u : 0u);
      unsigned mA23 = (e0 - 2 <= -9 ? 0xFFFFu : 0u) | (e0 - 3 <= -9 ? 0xFFFF0000u : 0u);
      PIN(limA); PIN(limB); PIN(mA01); PIN(mA23);
      const unsigned mA[2] = {mA01, mA23};
      u32x4 ones = {0x3F803F80u, 0x3F803F80u, 0x3F803F80u, 0x3F803F80u};
      PIN(ones);
      u32x2 sel[4], kz[5];
#pragma unroll
      for (int t = 0; t < 4; ++t) {
        const bool on = g4 == 2 && (((c15 >> 2) - t + 1) & 3) < 2;
        sel[t] = (u32x2){on && p4 < 2 ? 0x3F80u << (16 * p4) : 0u, on && p4 >= 2 ? 0x3F80u << (16 * (p4 - 2)) : 0u};
        PIN(sel[t]);
      }
#pragma unroll
      for (int t = 0; t < 5; ++t) {
        kz[t] = (u32x2){0u, 0u};
        PIN(kz[t]);
      }
      const int swz = (c15 >> 1) & 7;
      const int aoff0 = c15 * 128 + ((g4 ^ swz) * 16);
      const int aoff1 = c15 * 128 + (((4 + g4) ^ swz) * 16);
      int woff[4];
      {
        const int q4 = c15 >> 2, sw = q4 | ((g4 & 1) << 2);
#pragma unroll
        for (int t = 0; t < 4; ++t) {
          const int sc = t < 3 ? t : p4;
          const int by = wave * 40 + (t < 3 ? p4 : 4) * 8;
          woff[t] = 10240 + (8 * g4 + q4) * 512 + (((sc == 3 ? 15 : sc * 5 + (by >> 5)) ^ sw) * 32) + ((by & 31) ^ (sc == 2 ? 16 : sc == 3 ? 8 : 0));
          PIN(woff[t]);
        }
      }
      int wo3[2][4], ao3[2][2];
#pragma unroll
      for (int q = 0; q < 2; ++q) {
#pragma unroll
        for (int j = 0; j < 4; ++j) {
          wo3[q][j] = woff[j] + (q + 1) * SLOT_B;
          PIN(wo3[q][j]);
        }
        ao3[q][0] = aoff0 + (q + 1) * SLOT_B;
        ao3[q][1] = aoff1 + (q + 1) * SLOT_B;
        PIN(ao3[q][0]); PIN(ao3[q][1]);
      }
#define GM(m, f, wf, af) acc[m][f] = (f) == 2 ? mfma16(af[m], wf[f], acc[m][f]) : mfma16(wf[f], af[m], acc[m][f])
      u32x4 af0[5], wf0[4], af1[5], wf1[4];
      auto rd1 = [&](u32x4(&an)[5], u32x4(&wn)[4], int q, int h, int r) {
        const int ao = q ? ao3[q - 1][h] : h ? aoff1 : aoff0;
        if (r == 2) {
          an[0] = *(const u32x4*)(smem + ao);
        } else if (r >= 9) {
          an[r - 8] = *(const u32x4*)(smem + ao + (r - 8) * 2048);
        } else {
          const int k = r < 2 ? r : r - 1;
          const u32x2 t = tr_read(smem + (q ? wo3[q - 1][k >> 1] : woff[k >> 1]) + h * 16384 + (k & 1) * 2048);
          wn[k >> 1][(k & 1) * 2] = t[0];
          wn[k >> 1][(k & 1) * 2 + 1] = t[1];
        }
      };
      auto half = [&](const u32x4(&a)[5], const u32x4(&w)[4], u32x4(&an)[5], u32x4(&wn)[4], int q, int h) {
#pragma unroll
        for (int i = 0; i < 20; ++i) {
          GM(i >> 2, i & 3, w, a);
          SB();
          if (i < 13) rd1(an, wn, q, h, i);
          SB();
        }
      };
      if (b < 200) {
        if (!wjob) WAITV(0);
        const __amdgpu_buffer_rsrc_t r = __builtin_amdgcn_make_buffer_rsrc((void*)(ws + WS_WOF), 0, 819200, 0x00020000);
        const char* tp = smem + L_PIECE + wave * 1024 + (c15 >> 2) * 128 + g4 * 32 + (lane & 3) * 8;
        const u32x2 lo = tr_read(tp), hi = tr_read(tp + 512);
        const int T = (b / 20) * 4 + g4;
        __builtin_amdgcn_raw_buffer_store_b128((u32x4){lo[0], lo[1], hi[0], hi[1]}, r, (((T / 5) * 20 + wks) * 5 + (T + 5 - ((T / 5) & 1)) % 5) * 1024 + (wave * 16 + c15) * 16, 0, 16);
      }
      WAITL0();
      BARRIER();
#pragma unroll
      for (int r = 0; r < 13; ++r) rd1(af0, wf0, 0, 0, r);
      SB();
#pragma unroll
      for (int i = 0; i < 9; ++i) {
        half(af0, wf0, af1, wf1, i % 3, 1);
        if (i == PUB_IT) WAITV(0);
        if (i == 6) prepB();
        SB();
        WAITL0();
        BARRIER();
        if (i < 8) half(af1, wf1, af0, wf0, (i + 1) % 3, 0);
      }
      u32x4 wf2[4], af2[5];
      u32x4 qf[5], kf[5];
      u32x10 vb, tb_;
      u32x4 pf[5], os[3];
      u32x2 pp[5], ob[6], obT, obU;
      f32x4 l[5], sA[5], oA[5], sm[5], tx[5], oT[3];
      f32x2 ar[5][2], nn0[2], nn1[2], nn2[2], nn3[2], nn4[2], nnT[2], nnU[2];
      float mx[6], inv[5], it, iu;
      constexpr u64 mo = 0xFFFF0000FFFF0000ull, mh = 0xFFFFFFFF00000000ull;
      auto selm = [](u64 m_, float a, float c) { return __builtin_amdgcn_inverse_ballot_w64(m_) ? a : c; };
      const float cs = 0.22360679774997896f * 1.4426950408889634f;
      const f32x2 c2 = {cs, cs};
      const f32x4 zero4 = {0.f, 0.f, 0.f, 0.f};
      auto cpy = [](float v) { PIN(v); return v; };
      auto cpyu = [](unsigned v) { PIN(v); return v; };
      auto mulo = [](float v, float c) { PIN(v); return v * c; };
#define XBAR() BARRIER()
      GM(0, 3, wf1, af1); SB();
      af0[0] = *(const u32x4*)(smem + aoff0 + 0); SB();
      { const u32x2 t_ = tr_read(smem + woff[3] + 0); wf0[3][0] = t_[0]; wf0[3][1] = t_[1]; } SB();
      GM(0, 1, wf1, af1); SB();
      { const u32x2 t_ = tr_read(smem + woff[3] + 2048); wf0[3][2] = t_[0]; wf0[3][3] = t_[1]; } SB();
      { const u32x2 t_ = tr_read(smem + woff[1] + 0); wf0[1][0] = t_[0]; wf0[1][1] = t_[1]; } SB();
      GM(0, 0, wf1, af1); SB();
      { const u32x2 t_ = tr_read(smem + woff[1] + 2048); wf0[1][2] = t_[0]; wf0[1][3] = t_[1]; } SB();
      { const u32x2 t_ = tr_read(smem + woff[0] + 0); wf0[0][0] = t_[0]; wf0[0][1] = t_[1]; } SB();
      GM(0, 2, wf1, af1); SB();
      { const u32x2 t_ = tr_read(smem + woff[0] + 2048); wf0[0][2] = t_[0]; wf0[0][3] = t_[1]; } SB();
      af2[0] = *(const u32x4*)(smem + aoff1 + 0); SB();
      GM(1, 3, wf1, af1); SB();
      { const u32x2 t_ = tr_read(smem + woff[2] + 0); wf0[2][0] = t_[0]; wf0[2][1] = t_[1]; } SB();
      { const u32x2 t_ = tr_read(smem + woff[3] + 16384); wf2[3][0] = t_[0]; wf2[3][1] = t_[1]; } SB();
      GM(1, 1, wf1, af1); SB();
      { const u32x2 t_ = tr_read(smem + woff[2] + 2048); wf0[2][2] = t_[0]; wf0[2][3] = t_[1]; } SB();
      { const u32x2 t_ = tr_read(smem + woff[3] + 18432); wf2[3][2] = t_[0]; wf2[3][3] = t_[1]; } SB();
      GM(1, 0, wf1, af1); SB();
      { const u32x2 t_ = tr_read(smem + woff[1] + 16384); wf2[1][0] = t_[0]; wf2[1][1] = t_[1]; } SB();
      { const u32x2 t_ = tr_read(smem + woff[1] + 18432); wf2[1][2] = t_[0]; wf2[1][3] = t_[1]; } SB();
      GM(1, 2, wf1, af1); SB();
      { const u32x2 t_ = tr_read(smem + woff[0] + 16384); wf2[0][0] = t_[0]; wf2[0][1] = t_[1]; } SB();
      { const u32x2 t_ = tr_read(smem + woff[0] + 18432); wf2[0][2] = t_[0]; wf2[0][3] = t_[1]; } SB();
      GM(2, 3, wf1, af1); SB();
      af0[1] = *(const u32x4*)(smem + aoff0 + 2048); SB();
      { const u32x2 t_ = tr_read(smem + woff[2] + 16384); wf2[2][0] = t_[0]; wf2[2][1] = t_[1]; } SB();
      GM(2, 1, wf1, af1); SB();
      af2[1] = *(const u32x4*)(smem + aoff1 + 2048); SB();
      { const u32x2 t_ = tr_read(smem + woff[2] + 18432); wf2[2][2] = t_[0]; wf2[2][3] = t_[1]; } SB();
      GM(2, 0, wf1, af1); SB();
      af0[2] = *(const u32x4*)(smem + aoff0 + 4096); SB();
      af2[2] = *(const u32x4*)(smem + aoff1 + 4096); SB();
      GM(2, 2, wf1, af1); SB();
      af0[3] = *(const u32x4*)(smem + aoff0 + 6144); SB();
      af2[3] = *(const u32x4*)(smem + aoff1 + 6144); SB();
      GM(0, 3, wf0, af0); SB();
      af0[4] = *(const u32x4*)(smem + aoff0 + 8192); SB();
      af2[4] = *(const u32x4*)(smem + aoff1 + 8192); SB();
      GM(0, 1, wf0, af0); SB();
      GM(0, 0, wf0, af0); SB();
      GM(0, 2, wf0, af0); SB();
      GM(0, 3, wf2, af2); SB();
      GM(0, 1, wf2, af2); SB();
      GM(0, 0, wf2, af2); SB();
      GM(0, 2, wf2, af2); SB();
      GM(1, 3, wf0, af0); SB();
      GM(1, 1, wf0, af0); SB();
      GM(1, 0, wf0, af0); SB();
      GM(1, 2, wf0, af0); SB();
      qf[0][2] = pk2(acc[0][3][0], acc[0][3][1]); SB();
      GM(1, 3, wf2, af2); SB();
      qf[0][3] = pk2(acc[0][3][2], acc[0][3][3]); SB();
      { const auto r_ = __builtin_amdgcn_permlane16_swap(qf[0][2], kz[0][0], false, false); qf[0][2] = r_[0]; kf[0][2] = r_[1]; } SB();
      GM(1, 1, wf2, af2); SB();
      kf[0][0] = pk2(acc[0][1][0], acc[0][1][1]); SB();
      { const auto r_ = __builtin_amdgcn_permlane16_swap(qf[0][3], kz[0][1], false, false); qf[0][3] = r_[0]; kf[0][3] = r_[1]; } SB();
      GM(1, 0, wf2, af2); SB();
      kf[0][1] = pk2(acc[0][1][2], acc[0][1][3]); SB();
      qf[0][0] = pk2(acc[0][0][0], acc[0][0][1]); SB();
      tx[0] = mfma16h((u32x2){qf[0][2], qf[0][3]}, sel[0], zero4); SB();
      qf[1][2] = pk2(acc[1][3][0], acc[1][3][1]); SB();
      qf[1][3] = pk2(acc[1][3][2], acc[1][3][3]); SB();
      GM(1, 2, wf2, af2); SB();
      { const auto r_ = __builtin_amdgcn_permlane16_swap(qf[1][2], kz[1][0], false, false); qf[1][2] = r_[0]; kf[1][2] = r_[1]; } SB();
      kf[1][0] = pk2(acc[1][1][0], acc[1][1][1]); SB();
      GM(2, 3, wf0, af0); SB();
      qf[1][0] = pk2(acc[1][0][0], acc[1][0][1]); SB();
      { const auto r_ = __builtin_amdgcn_permlane16_swap(qf[1][3], kz[1][1], false, false); qf[1][3] = r_[0]; kf[1][3] = r_[1]; } SB();
      GM(2, 1, wf0, af0); SB();
      kf[1][1] = pk2(acc[1][1][2], acc[1][1][3]); SB();
      qf[1][1] = pk2(acc[1][0][2], acc[1][0][3]); SB();
      tx[1] = mfma16h((u32x2){qf[1][2], qf[1][3]}, sel[1], zero4); SB();
      qf[0][1] = pk2(acc[0][0][2], acc[0][0][3]); SB();
      tb_[0] = pk2(tx[0][0], tx[0][1]); SB();
      l[1] = mfma16(kf[1], qf[1], limB); SB();
      tb_[1] = pk2(tx[0][2], tx[0][3]); SB();
      vb[0] = pk2(acc[0][2][0], acc[0][2][1]); SB();
      sA[1] = mfma16(kf[0], qf[1], limA); SB();
      vb[1] = pk2(acc[0][2][2], acc[0][2][3]); SB();
      vb[2] = pk2(acc[1][2][0], acc[1][2][1]); SB();
      l[0] = mfma16(kf[0], qf[0], limB); SB();
      vb[3] = pk2(acc[1][2][2], acc[1][2][3]); SB();
      tb_[2] = pk2(tx[1][0], tx[1][1]); SB();
      GM(2, 0, wf0, af0); SB();
      tb_[3] = pk2(tx[1][2], tx[1][3]); SB();
      GM(2, 2, wf0, af0); SB();
      l[1][0] = fmx(l[1][0], sA[1][0]); SB();
      l[1][1] = fmx(l[1][1], sA[1][1]); SB();
      GM(2, 3, wf2, af2); SB();
      l[1][2] = fmx(l[1][2], sA[1][2]); SB();
      mx[0] = fmx(fmx(l[0][0], l[0][1]), l[0][2]); SB();
      GM(2, 1, wf2, af2); SB();
      l[1][3] = fmx(l[1][3], sA[1][3]); SB();
      mx[1] = fmx(fmx(l[1][0], l[1][1]), l[1][2]); SB();
      GM(2, 0, wf2, af2); SB();
      mx[0] = fmx(mx[0], l[0][3]); SB();
      mx[1] = fmx(mx[1], l[1][3]); SB();
      GM(2, 2, wf2, af2); SB();
      qf[2][2] = pk2(acc[2][3][0], acc[2][3][1]); SB();
      qf[2][3] = pk2(acc[2][3][2], acc[2][3][3]); SB();
      GM(3, 3, wf1, af1); SB();
      { const auto r_ = __builtin_amdgcn_permlane16_swap(qf[2][2], kz[2][0], false, false); qf[2][2] = r_[0]; kf[2][2] = r_[1]; } SB();
      kf[2][0] = pk2(acc[2][1][0], acc[2][1][1]); SB();
      GM(3, 1, wf1, af1); SB();
      qf[2][0] = pk2(acc[2][0][0], acc[2][0][1]); SB();
      { const auto r_ = __builtin_amdgcn_permlane16_swap(qf[2][3], kz[2][1], false, false); qf[2][3] = r_[0]; kf[2][3] = r_[1]; } SB();
      GM(3, 0, wf1, af1); SB();
      kf[2][1] = pk2(acc[2][1][2], acc[2][1][3]); SB();
      qf[2][1] = pk2(acc[2][0][2], acc[2][0][3]); SB();
      tx[2] = mfma16h((u32x2){qf[2][2], qf[2][3]}, sel[2], zero4); SB();
      { const auto r_ = __builtin_amdgcn_permlane32_swap(__float_as_uint(mx[0]), __float_as_uint(mx[1]), false, false); mx[0] = __uint_as_float(r_[0]); mx[1] = __uint_as_float(r_[1]); } SB();
      mx[0] = fmx(mx[0], mx[1]); SB();
      l[2] = mfma16(kf[2], qf[2], limB); SB();
      mx[1] = cpy(mx[0]); SB();
      vb[4] = pk2(acc[2][2][0], acc[2][2][1]); SB();
      sA[2] = mfma16(kf[1], qf[2], limA); SB();
      { const auto r_ = __builtin_amdgcn_permlane16_swap(__float_as_uint(mx[0]), __float_as_uint(mx[1]), false, false); mx[0] = __uint_as_float(r_[0]); mx[1] = __uint_as_float(r_[1]); } SB();
      mx[0] = fmx(mx[0], mx[1]); SB();
      GM(3, 2, wf1, af1); SB();
      mx[1] = mx[0] * -cs; SB();
      mx[0] = mulo(mx[0], -cs); SB();
      GM(3, 3, wf0, af0); SB();
      vb[5] = pk2(acc[2][2][2], acc[2][2][3]); SB();
      { const auto r_ = __builtin_amdgcn_permlane32_swap(__float_as_uint(mx[0]), __float_as_uint(mx[1]), false, false); mx[0] = __uint_as_float(r_[0]); mx[1] = __uint_as_float(r_[1]); } SB();
      GM(3, 1, wf0, af0); SB();
      l[2][0] = fmx(l[2][0], sA[2][0]); SB();
      l[2][1] = fmx(l[2][1], sA[2][1]); SB();
      GM(3, 0, wf0, af0); SB();
      l[2][2] = fmx(l[2][2], sA[2][2]); SB();
      l[2][3] = fmx(l[2][3], sA[2][3]); SB();
      GM(3, 2, wf0, af0); SB();
      mx[2] = fmx(fmx(l[2][0], l[2][1]), l[2][2]); SB();
      mx[2] = fmx(mx[2], l[2][3]); SB();
      GM(3, 3, wf2, af2); SB();
      ar[1][0] = __builtin_elementwise_fma((f32x2){l[1][0], l[1][1]}, c2, (f32x2){mx[1], mx[1]}); SB();
      ar[1][1] = __builtin_elementwise_fma((f32x2){l[1][2], l[1][3]}, c2, (f32x2){mx[1], mx[1]}); SB();
      GM(3, 1, wf2, af2); SB();
      ar[0][0] = __builtin_elementwise_fma((f32x2){l[0][0], l[0][1]}, c2, (f32x2){mx[0], mx[0]}); SB();
      ar[0][1] = __builtin_elementwise_fma((f32x2){l[0][2], l[0][3]}, c2, (f32x2){mx[0], mx[0]}); SB();
      GM(3, 0, wf2, af2); SB();
      ar[1][0][0] = __builtin_amdgcn_exp2f(ar[1][0][0]); SB();
      GM(3, 2, wf2, af2); SB();
      qf[3][2] = pk2(acc[3][3][0], acc[3][3][1]); SB();
      qf[3][3] = pk2(acc[3][3][2], acc[3][3][3]); SB();
      GM(4, 3, wf1, af1); SB();
      { const auto r_ = __builtin_amdgcn_permlane16_swap(qf[3][2], kz[3][0], false, false); qf[3][2] = r_[0]; kf[3][2] = r_[1]; } SB();
      kf[3][0] = pk2(acc[3][1][0], acc[3][1][1]); SB();
      GM(4, 1, wf1, af1); SB();
      qf[3][0] = pk2(acc[3][0][0], acc[3][0][1]); SB();
      { const auto r_ = __builtin_amdgcn_permlane16_swap(qf[3][3], kz[3][1], false, false); qf[3][3] = r_[0]; kf[3][3] = r_[1]; } SB();
      GM(4, 0, wf1, af1); SB();
      kf[3][1] = pk2(acc[3][1][2], acc[3][1][3]); SB();
      qf[3][1] = pk2(acc[3][0][2], acc[3][0][3]); SB();
      tx[3] = mfma16h((u32x2){qf[3][2], qf[3][3]}, sel[3], zero4); SB();
      ar[1][0][1] = __builtin_amdgcn_exp2f(ar[1][0][1]); SB();
      l[3] = mfma16(kf[3], qf[3], limB); SB();
      ar[1][1][0] = __builtin_amdgcn_exp2f(ar[1][1][0]); SB();
      sA[3] = mfma16(kf[2], qf[3], limA); SB();
      ar[1][1][1] = __builtin_amdgcn_exp2f(ar[1][1][1]); SB();
      GM(4, 2, wf1, af1); SB();
      ar[0][0][0] = __builtin_amdgcn_exp2f(ar[0][0][0]); SB();
      GM(4, 3, wf0, af0); SB();
      ar[0][0][1] = __builtin_amdgcn_exp2f(ar[0][0][1]); SB();
      GM(4, 1, wf0, af0); SB();
      ar[0][1][0] = __builtin_amdgcn_exp2f(ar[0][1][0]); SB();
      GM(4, 0, wf0, af0); SB();
      l[3][0] = fmx(l[3][0], sA[3][0]); SB();
      l[3][1] = fmx(l[3][1], sA[3][1]); SB();
      GM(4, 2, wf0, af0); SB();
      l[3][2] = fmx(l[3][2], sA[3][2]); SB();
      l[3][3] = fmx(l[3][3], sA[3][3]); SB();
      GM(4, 3, wf2, af2); SB();
      mx[3] = fmx(fmx(l[3][0], l[3][1]), l[3][2]); SB();
      mx[3] = fmx(mx[3], l[3][3]); SB();
      GM(4, 1, wf2, af2); SB();
      ar[0][1][1] = __builtin_amdgcn_exp2f(ar[0][1][1]); SB();
      GM(4, 0, wf2, af2); SB();
      { const auto r_ = __builtin_amdgcn_permlane32_swap(__float_as_uint(mx[2]), __float_as_uint(mx[3]), false, false); mx[2] = __uint_as_float(r_[0]); mx[3] = __uint_as_float(r_[1]); } SB();
      mx[2] = fmx(mx[2], mx[3]); SB();
      GM(4, 2, wf2, af2); SB();
      qf[4][2] = pk2(acc[4][3][0], acc[4][3][1]); SB();
      qf[4][3] = pk2(acc[4][3][2], acc[4][3][3]); SB();
      mx[3] = cpy(mx[2]); SB();
      { const auto r_ = __builtin_amdgcn_permlane16_swap(qf[4][2], kz[4][0], false, false); qf[4][2] = r_[0]; kf[4][2] = r_[1]; } SB();
      kf[4][0] = pk2(acc[4][1][0], acc[4][1][1]); SB();
      qf[4][0] = pk2(acc[4][0][0], acc[4][0][1]); SB();
      { const auto r_ = __builtin_amdgcn_permlane16_swap(qf[4][3], kz[4][1], false, false); qf[4][3] = r_[0]; kf[4][3] = r_[1]; } SB();
      kf[4][1] = pk2(acc[4][1][2], acc[4][1][3]); SB();
      qf[4][1] = pk2(acc[4][0][2], acc[4][0][3]); SB();
      tx[4] = mfma16h((u32x2){qf[4][2], qf[4][3]}, sel[0], zero4); SB();
      { const auto r_ = __builtin_amdgcn_permlane16_swap(__float_as_uint(mx[2]), __float_as_uint(mx[3]), false, false); mx[2] = __uint_as_float(r_[0]); mx[3] = __uint_as_float(r_[1]); } SB();
      mx[2] = fmx(mx[2], mx[3]); SB();
      l[4] = mfma16(kf[4], qf[4], limB); SB();
      mx[3] = mx[2] * -cs; SB();
      mx[2] = mulo(mx[2], -cs); SB();
      sA[4] = mfma16(kf[3], qf[4], limA); SB();
      pp[1][0] = pk2(ar[1][0][0], ar[1][0][1]); SB();
      { const auto r_ = __builtin_amdgcn_permlane32_swap(__float_as_uint(mx[2]), __float_as_uint(mx[3]), false, false); mx[2] = __uint_as_float(r_[0]); mx[3] = __uint_as_float(r_[1]); } SB();
      ar[2][0] = __builtin_elementwise_fma((f32x2){l[2][0], l[2][1]}, c2, (f32x2){mx[2], mx[2]}); SB();
      ar[2][1] = __builtin_elementwise_fma((f32x2){l[2][2], l[2][3]}, c2, (f32x2){mx[2], mx[2]}); SB();
      ar[2][0][0] = __builtin_amdgcn_exp2f(ar[2][0][0]); SB();
      ar[2][0][1] = __builtin_amdgcn_exp2f(ar[2][0][1]); SB();
      ar[2][1][0] = __builtin_amdgcn_exp2f(ar[2][1][0]); SB();
      ar[2][1][1] = __builtin_amdgcn_exp2f(ar[2][1][1]); SB();
      l[4][0] = fmx(l[4][0], sA[4][0]); SB();
      l[4][1] = fmx(l[4][1], sA[4][1]); SB();
      l[4][2] = fmx(l[4][2], sA[4][2]); SB();
      l[4][3] = fmx(l[4][3], sA[4][3]); SB();
      mx[4] = fmx(fmx(l[4][0], l[4][1]), l[4][2]); SB();
      mx[4] = fmx(mx[4], l[4][3]); SB();
      mx[5] = cpy(mx[4]); SB();
      pp[1][1] = pk2(ar[1][1][0], ar[1][1][1]); SB();
      pp[2][0] = pk2(ar[2][0][0], ar[2][0][1]); SB();
      { const auto r_ = __builtin_amdgcn_permlane32_swap(__float_as_uint(mx[4]), __float_as_uint(mx[5]), false, false); mx[4] = __uint_as_float(r_[0]); mx[5] = __uint_as_float(r_[1]); } SB();
      mx[4] = fmx(mx[4], mx[5]); SB();
      mx[5] = cpy(mx[4]); SB();
      pp[2][1] = pk2(ar[2][1][0], ar[2][1][1]); SB();
      pp[0][0] = pk2(ar[0][0][0], ar[0][0][1]); SB();
      { const auto r_ = __builtin_amdgcn_permlane16_swap(__float_as_uint(mx[4]), __float_as_uint(mx[5]), false, false); mx[4] = __uint_as_float(r_[0]); mx[5] = __uint_as_float(r_[1]); } SB();
      pp[0][1] = pk2(ar[0][1][0], ar[0][1][1]); SB();
      mx[4] = fmx(mx[4], mx[5]); SB();
      pf[1][0] = pp[1][0] & mA[0]; SB();
      sm[0] = mfma16h((u32x2){ones[0], ones[1]}, pp[0], zero4); SB();
      pf[1][2] = pp[1][0] & ~mA[0]; SB();
      pf[1][1] = pp[1][1] & mA[1]; SB();
      oA[0] = mfma16h((u32x2){vb[0], vb[1]}, pp[0], zero4); SB();
      pf[1][3] = pp[1][1] & ~mA[1]; SB();
      pf[2][0] = pp[2][0] & mA[0]; SB();
      oT[2] = mfma16h((u32x2){tb_[0], tb_[1]}, pp[0], zero4); SB();
      pf[2][2] = pp[2][0] & ~mA[0]; SB();
      pf[2][1] = pp[2][1] & mA[1]; SB();
      sm[1] = mfma16(ones, pf[1], zero4); SB();
      pf[2][3] = pp[2][1] & ~mA[1]; SB();
      mx[4] = mx[4] * -cs; SB();
      PIN(tb_); oT[0] = mfma16(__builtin_shufflevector(tb_, tb_, 0, 1, 2, 3), pf[1], zero4); SB();
      ar[3][0] = __builtin_elementwise_fma((f32x2){l[3][0], l[3][1]}, c2, (f32x2){mx[3], mx[3]}); SB();
      ar[3][1] = __builtin_elementwise_fma((f32x2){l[3][2], l[3][3]}, c2, (f32x2){mx[3], mx[3]}); SB();
      sm[2] = mfma16(ones, pf[2], zero4); SB();
      ar[4][0] = __builtin_elementwise_fma((f32x2){l[4][0], l[4][1]}, c2, (f32x2){mx[4], mx[4]}); SB();
      ar[4][1] = __builtin_elementwise_fma((f32x2){l[4][2], l[4][3]}, c2, (f32x2){mx[4], mx[4]}); SB();
      PIN(vb); oA[1] = mfma16(__builtin_shufflevector(vb, vb, 0, 1, 2, 3), pf[1], zero4); SB();
      ar[3][0][0] = __builtin_amdgcn_exp2f(ar[3][0][0]); SB();
      PIN(vb); oA[2] = mfma16(__builtin_shufflevector(vb, vb, 2, 3, 4, 5), pf[2], zero4); SB();
      ar[3][0][1] = __builtin_amdgcn_exp2f(ar[3][0][1]); SB();
      ar[3][1][0] = __builtin_amdgcn_exp2f(ar[3][1][0]); SB();
      ar[3][1][1] = __builtin_amdgcn_exp2f(ar[3][1][1]); SB();
      ar[4][0][0] = __builtin_amdgcn_exp2f(ar[4][0][0]); SB();
      ar[4][0][1] = __builtin_amdgcn_exp2f(ar[4][0][1]); SB();
      ar[4][1][0] = __builtin_amdgcn_exp2f(ar[4][1][0]); SB();
      ar[4][1][1] = __builtin_amdgcn_exp2f(ar[4][1][1]); SB();
      pp[3][0] = pk2(ar[3][0][0], ar[3][0][1]); SB();
      pp[3][1] = pk2(ar[3][1][0], ar[3][1][1]); SB();
      pp[4][0] = pk2(ar[4][0][0], ar[4][0][1]); SB();
      pp[4][1] = pk2(ar[4][1][0], ar[4][1][1]); SB();
      inv[0] = __builtin_amdgcn_rcpf(sm[0][0]); PIN(sm[0]); SB();
      inv[1] = __builtin_amdgcn_rcpf(sm[1][0]); PIN(sm[1]); SB();
      inv[2] = __builtin_amdgcn_rcpf(sm[2][0]); PIN(sm[2]); SB();
      pf[3][0] = pp[3][0] & mA[0]; SB();
      pf[3][2] = pp[3][0] & ~mA[0]; SB();
      pf[3][1] = pp[3][1] & mA[1]; SB();
      pf[3][3] = pp[3][1] & ~mA[1]; SB();
      pf[4][0] = pp[4][0] & mA[0]; SB();
      pf[4][2] = pp[4][0] & ~mA[0]; SB();
      sm[3] = mfma16(ones, pf[3], zero4); SB();
      pf[4][1] = pp[4][1] & mA[1]; SB();
      pf[4][3] = pp[4][1] & ~mA[1]; SB();
      tb_[4] = pk2(tx[2][0], tx[2][1]); SB();
      tb_[5] = pk2(tx[2][2], tx[2][3]); SB();
      sm[4] = mfma16(ones, pf[4], zero4); SB();
      nn0[0] = (f32x2){oA[0][0], oA[0][1]} * (f32x2){inv[0], inv[0]}; SB();
      nn0[1] = (f32x2){oA[0][2], oA[0][3]} * (f32x2){inv[0], inv[0]}; SB();
      PIN(tb_); oT[1] = mfma16(__builtin_shufflevector(tb_, tb_, 2, 3, 4, 5), pf[2], zero4); SB();
      ob[0][0] = pk2(nn0[0][0], nn0[0][1]); SB();
      ob[0][1] = pk2(nn0[1][0], nn0[1][1]); SB();
      nn1[0] = (f32x2){oA[1][0], oA[1][1]} * (f32x2){inv[1], inv[1]}; SB();
      nn1[1] = (f32x2){oA[1][2], oA[1][3]} * (f32x2){inv[1], inv[1]}; SB();
      nn2[0] = (f32x2){oA[2][0], oA[2][1]} * (f32x2){inv[2], inv[2]}; SB();
      nn2[1] = (f32x2){oA[2][2], oA[2][3]} * (f32x2){inv[2], inv[2]}; SB();
      vb[6] = pk2(acc[3][2][0], acc[3][2][1]); SB();
      vb[7] = pk2(acc[3][2][2], acc[3][2][3]); SB();
      vb[8] = pk2(acc[4][2][0], acc[4][2][1]); SB();
      vb[9] = pk2(acc[4][2][2], acc[4][2][3]); SB();
      PIN(vb); oA[3] = mfma16(__builtin_shufflevector(vb, vb, 4, 5, 6, 7), pf[3], zero4); SB();
      tb_[6] = pk2(tx[3][0], tx[3][1]); SB();
      tb_[7] = pk2(tx[3][2], tx[3][3]); SB();
      PIN(vb); oA[4] = mfma16(__builtin_shufflevector(vb, vb, 6, 7, 8, 9), pf[4], zero4); SB();
      tb_[8] = pk2(tx[4][0], tx[4][1]); SB();
      tb_[9] = pk2(tx[4][2], tx[4][3]); SB();
      PIN(tb_); oT[0] = mfma16(__builtin_shufflevector(tb_, tb_, 4, 5, 6, 7), pf[3], oT[0]); SB();
      ob[1][0] = pk2(nn1[0][0], nn1[0][1]); SB();
      ob[1][1] = pk2(nn1[1][0], nn1[1][1]); SB();
      PIN(tb_); oT[1] = mfma16(__builtin_shufflevector(tb_, tb_, 6, 7, 8, 9), pf[4], oT[1]); SB();
      ob[2][0] = pk2(nn2[0][0], nn2[0][1]); SB();
      ob[2][1] = pk2(nn2[1][0], nn2[1][1]); SB();
      ob[5][0] = cpyu(ob[0][0]); SB();
      ob[5][1] = cpyu(ob[0][1]); SB();
      nnU[0] = (f32x2){oT[2][0], oT[2][1]} * (f32x2){inv[0], inv[0]}; SB();
      nnU[1] = (f32x2){oT[2][2], oT[2][3]} * (f32x2){inv[0], inv[0]}; SB();
      obU[0] = pk2(nnU[0][0], nnU[0][1]); SB();
      obU[1] = pk2(nnU[1][0], nnU[1][1]); SB();
      { const auto r_ = __builtin_amdgcn_permlane32_swap(ob[0][0], ob[5][0], false, false); os[0][0] = r_[0]; os[0][2] = r_[1]; } SB();
      { const auto r_ = __builtin_amdgcn_permlane32_swap(ob[0][1], ob[5][1], false, false); os[0][1] = r_[0]; os[0][3] = r_[1]; } SB();
      { const auto r_ = __builtin_amdgcn_permlane32_swap(ob[1][0], ob[2][0], false, false); os[1][0] = r_[0]; os[1][2] = r_[1]; } SB();
      { const auto r_ = __builtin_amdgcn_permlane32_swap(ob[1][1], ob[2][1], false, false); os[1][1] = r_[0]; os[1][3] = r_[1]; } SB();
      inv[3] = __builtin_amdgcn_rcpf(sm[3][0]); PIN(sm[3]); SB();
      inv[4] = __builtin_amdgcn_rcpf(sm[4][0]); PIN(sm[4]); SB();
      it = selm(mo, inv[2], inv[1]); SB();
      iu = selm(mo, inv[4], inv[3]); SB();
      nn3[0] = (f32x2){oA[3][0], oA[3][1]} * (f32x2){inv[3], inv[3]}; SB();
      nn3[1] = (f32x2){oA[3][2], oA[3][3]} * (f32x2){inv[3], inv[3]}; SB();
      nn4[0] = (f32x2){oA[4][0], oA[4][1]} * (f32x2){inv[4], inv[4]}; SB();
      nn4[1] = (f32x2){oA[4][2], oA[4][3]} * (f32x2){inv[4], inv[4]}; SB();
      it = selm(mh, iu, it); SB();
      oT[0][0] = selm(mo, oT[1][0], oT[0][0]); SB();
      oT[0][1] = selm(mo, oT[1][1], oT[0][1]); SB();
      oT[0][2] = selm(mo, oT[1][2], oT[0][2]); SB();
      oT[0][3] = selm(mo, oT[1][3], oT[0][3]); SB();
      ob[3][0] = pk2(nn3[0][0], nn3[0][1]); SB();
      ob[3][1] = pk2(nn3[1][0], nn3[1][1]); SB();
      ob[4][0] = pk2(nn4[0][0], nn4[0][1]); SB();
      ob[4][1] = pk2(nn4[1][0], nn4[1][1]); SB();
      nnT[0] = (f32x2){oT[0][0], oT[0][1]} * (f32x2){it, it}; SB();
      nnT[1] = (f32x2){oT[0][2], oT[0][3]} * (f32x2){it, it}; SB();
      obT[0] = pk2(nnT[0][0], nnT[0][1]); SB();
      obT[1] = pk2(nnT[1][0], nnT[1][1]); SB();
      XBAR(); SB();
      { const auto r_ = __builtin_amdgcn_permlane32_swap(ob[3][0], ob[4][0], false, false); os[2][0] = r_[0]; os[2][2] = r_[1]; } SB();
      { const auto r_ = __builtin_amdgcn_permlane32_swap(ob[3][1], ob[4][1], false, false); os[2][1] = r_[0]; os[2][3] = r_[1]; } SB();
      __builtin_amdgcn_raw_buffer_store_b128(os[0], ro, od[0], 0, 16); SB();
      __builtin_amdgcn_raw_buffer_store_b128(os[1], ro, od[1], 0, 16); SB();
      __builtin_amdgcn_raw_buffer_store_b128(os[2], ro, od[2], 0, 16); SB();
      __builtin_amdgcn_raw_buffer_store_b64(obT, ro, od[3], 0, 16); SB();
      __builtin_amdgcn_raw_buffer_store_b64(obU, ro, od[4], 0, 16); SB();
    }
  } else {
    const int f = FMAP ? b - 224 : xcd * 4 + (jj - 28);
    args();
    float* red = (float*)smem;
    u16* hs = (u16*)(smem + 32768);
    const bool do1 = f < 27;
    const bool lead = f == 31;
    if (__builtin_expect(lead, 0)) {
      get_tag();
      const __amdgpu_buffer_rsrc_t rk = __builtin_amdgcn_make_buffer_rsrc((void*)(ws + WS_K), 0, 36864, 0x00020000);
      u32x2 kv[2];
      int ko[2];
      ko[0] = (tid >> 2) * 128 + (tid & 3) * 16;
      ko[1] = tid < 384 ? (128 + (tid >> 2)) * 128 + (tid & 3) * 16 : (224 + ((tid - 384) >> 1)) * 128 + (tid & 1) * 16;
      kv[0] = kv[1] = (u32x2){0u, 0u};
#pragma unroll 1
      for (unsigned spins = 0; spins < SPIN_CAP; ++spins) {
        asm volatile("" ::: "memory");
        u32x4 t_[2];
#pragma unroll
        for (int q = 0; q < 2; ++q) t_[q] = __builtin_amdgcn_raw_buffer_load_b128(rk, ko[q], 0, 16);
        bool ok = true;
#pragma unroll
        for (int q = 0; q < 2; ++q)
          if (ko[q] != 0x7FFFFF00) {
            if ((t_[q][1] == tag) & (t_[q][3] == tag)) {
              kv[q] = (u32x2){t_[q][0], t_[q][2]};
              ko[q] = 0x7FFFFF00;
            } else {
              ok = false;
            }
          }
        if (__all(ok)) break;
        __builtin_amdgcn_s_sleep(4);
      }
      unsigned k1 = max(kv[0][0], kv[0][1]), k2 = min(kv[0][0], kv[0][1]);
      top2_mrg(k1, k2, max(kv[1][0], kv[1][1]), min(kv[1][0], kv[1][1]));
      top2_all(k1, k2);
      unsigned* lk = (unsigned*)(smem + L_VAR + 64);
      if (lane == 0) *(u32x2*)(lk + wave * 2) = (u32x2){k1, k2};
      WAITL0();
      BARRIER();
      k1 = 0u;
      k2 = 0u;
#pragma unroll
      for (int q = 0; q < 4; ++q) {
        const u32x4 a_ = *(const u32x4*)(lk + q * 4);
        top2_mrg(k1, k2, a_[0], a_[1]);
        top2_mrg(k1, k2, a_[2], a_[3]);
      }
      if (tid == 0) {
        const __amdgpu_buffer_rsrc_t rr = __builtin_amdgcn_make_buffer_rsrc((void*)(ws + WS_R), 0, 128, 0x00020000);
        __builtin_amdgcn_raw_buffer_store_b128((u32x4){k1, tag, k2, tag}, rr, 0, 0, 16);
        *(u32x2*)(smem + L_VAR + 32) = (u32x2){k1, k2};
      }
    }
    if (__builtin_expect(f == 30, 0)) {
      get_tag();
      if (wave < 4) {
        const __amdgpu_buffer_rsrc_t rf = __builtin_amdgcn_make_buffer_rsrc((void*)(ws + WS_F2), 0, 32768, 0x00020000);
        int fo = tid < 227 ? tid * 128 : 0x7FFFFF00;
#pragma unroll 1
        for (unsigned spins = 0; spins < SPIN_CAP; ++spins) {
          asm volatile("" ::: "memory");
          if (__builtin_amdgcn_raw_buffer_load_b32(rf, fo, 0, 16) == tag) fo = 0x7FFFFF00;
          if (__all(fo == 0x7FFFFF00)) break;
          __builtin_amdgcn_s_sleep(4);
        }
      }
      BARRIER();
      if (tid == 0) {
        const __amdgpu_buffer_rsrc_t rr = __builtin_amdgcn_make_buffer_rsrc((void*)(ws + WS_R), 0, 256, 0x00020000);
        const unsigned t_ = now();
        __builtin_amdgcn_raw_buffer_store_b128((u32x4){t_, tag, t_, tag}, rr, 128, 0, 16);
        *(u32x4*)(smem + L_CAND2) = (u32x4){t_, tag, t_, tag};
      }
    }
    if (__builtin_expect(f >= 27 && f < 30, 0)) {
      get_tag();
#pragma unroll 1
      for (int c = wave * 3 + f - 27; c < 320; c += 24)
        __builtin_amdgcn_raw_buffer_store_b128((u32x4){SENT, SENT, SENT, SENT}, ro, lane * 16, (c >= 160 ? 112 * 20480 : 56 * 20480) + c * 1024, 16);
      WAITV(0);
      BARRIER();
      if (tid == 0) __hip_atomic_store((gu32*)(ws + WS_F2 + (197 + f) * 128), tag, RLX_AGENT);
    }
    const int g0 = wave < 4 ? wave * 3 : 4 + wave * 2, ng = wave < 4 ? 3 : 2;
    u32x4 r1[3][8], wd[2][8];
    int vo2 = (wave - 3) * 256 + c15 * 16 + g4 * 8 * (DM * 2);
    PIN(vo2);
    if (do1) {
      const char* pa = (const char*)(c15 < 8 ? w_gate : w_up) + f * 128 + (c15 & 7) * 16 + (g0 * 32 + g4 * 8) * (FF * 2);
      const GAS char* pp[12];
#pragma unroll
      for (int j = 0; j < 12; ++j) {
        pp[j] = (const GAS char*)pa + ((j >> 2) * 32 + (j & 3) * 2) * (FF * 2) + FF;
        PIN(pp[j]);
      }
      SB();
      if (FDL) __builtin_amdgcn_s_sleep(FDL);
#pragma unroll
      for (int j = 0; j < 24; ++j)
        if (j < 16 || wave < 4) r1[j >> 3][j & 7] = __builtin_nontemporal_load((const GAS u32x4*)(pp[j >> 1] + ((j & 1) ? FF : -FF)));
    }
    SB();
    BARRIER();
    if (YT && do1 && wave < 2 && lane < 40) touch_line(ws + WS_YP + (f * 2 + wave) * 5120 + lane * 128, lds0 + L_TOUCH + 256);
    auto wd_load = [&](int j0, int j1) {
      const u32x4 r = make_srd(w_down, DM * FF * 2);
#pragma unroll
      for (int j = j0; j < j1; ++j)
        asm volatile("buffer_load_dwordx4 %0, %1, %2, %3 offen nt" : "=v"(wd[j >> 3][j & 7]) : "v"(vo2), "s"(r), "s"((f * 64 + (j >> 3) * 32 + (j & 7)) * (DM * 2)) : "memory");
    };
    if (do1 && wave >= 3) wd_load(0, NW1);
    SB();
    if (f < 30) get_tag();
    prepB();
    auto transp = [&](u32x4(&w)[8]) {
      u32x4 t[8];
#pragma unroll
      for (int d = 0; d < 4; ++d)
#pragma unroll
        for (int p = 0; p < 4; ++p) {
          t[2 * d][p] = perm_lo(w[2 * p + 1][d], w[2 * p][d]);
          t[2 * d + 1][p] = perm_hi(w[2 * p + 1][d], w[2 * p][d]);
        }
#pragma unroll
      for (int j = 0; j < 8; ++j) {
        w[j] = t[j];
        PIN(w[j]);
      }
    };
    if (do1) {
      transp(r1[0]);
      transp(r1[1]);
      if (wave < 4) transp(r1[2]);
    }
    __builtin_amdgcn_s_waitcnt(0x0F70);
    if (wave == 0 && !lead) {
      res_wait(ws, smem, lane, tag, 0);
      if (do1) {
        WAITL0();
        const u32x2 kq = *(const u32x2*)(smem + L_VAR + 32);
#pragma unroll
        for (int t = 0; t < 2; ++t) {
          const u16* xr = x + (2047 - (int)(__builtin_amdgcn_readfirstlane(kq[t]) & 2047u)) * DM;
          dma16(lane * 16, xr, lds0 + 36864 + t * 1296);
          if (lane < 16) dma16(lane * 16, xr + 512, lds0 + 36864 + t * 1296 + 1024);
        }
      }
    }
    if (wave == 1 && f != 30 && !do1) res_wait(ws, smem, lane, tag, 1);
    BARRIER();
    if (do1 && wave >= 3) wd_load(NW1, 16);
    if (tid == 128) __hip_atomic_store((gu32*)(ws + WS_A + f * 128), tag, RLX_AGENT);
    if (OTI < 9 && wave == 1) touch_line((const char*)out + tile_off((lane & 31) + (nt2 & 1) * 32) + ((nt2 & 1) ? 0 : 128), lds0 + L_TOUCH + 512);
    auto out_touch = [&]() {
#pragma unroll
      for (int i = 0; i < 2; ++i) {
        const int u = (tid - 64) + i * 448, r = u / 5;
        if (u < 640) touch_line((const char*)out + (2 * ((xcd & 1) * 512 + (jj & 3) * 128 + r) + ((xcd >> 1) & 1)) * (DM * 2) + (xcd >> 2) * 640 + (u - r * 5) * 128, lds0 + L_TOUCH + 256);
      }
    };
    if (OT == 1 && wave != 0) out_touch();
    if (wave == 0) WAITV(0);
    WAITL0();
    BARRIER();
    const __amdgpu_buffer_rsrc_t ryp = __builtin_amdgcn_make_buffer_rsrc((void*)(ws + WS_YP), 0, 27 * 10240, 0x00020000);
    auto pick = [&](const f32x4(&a)[8]) {
      f32x4 v;
#pragma unroll
      for (int q = 0; q < 4; ++q) {
        const float lo = (g4 & 1) ? a[2 + (q >> 1)][q & 1] : a[q >> 1][q & 1], hi = (g4 & 1) ? a[6 + (q >> 1)][q & 1] : a[4 + (q >> 1)][q & 1];
        v[q] = (g4 & 2) ? hi : lo;
      }
      return v;
    };
    if (do1) {
      u32x4 xf[3];
#pragma unroll
      for (int k = 0; k < 3; ++k) xf[k] = *(const u32x4*)(smem + 36864 + (c15 & 1) * 1296 + ((g0 + (k < ng ? k : 0)) * 32 + g4 * 8) * 2);
      f32x4 acc[8];
#pragma unroll
      for (int ff = 0; ff < 8; ++ff) acc[ff] = (f32x4){0.f, 0.f, 0.f, 0.f};
#pragma unroll
      for (int k = 0; k < 3; ++k)
        if (k < ng) {
#pragma unroll
          for (int j = 0; j < 8; ++j) acc[j] = mfma16(xf[k], r1[k][j], acc[j]);
        }
      *(f32x4*)(red + wave * 256 + c15 * 16 + g4 * 4) = pick(acc);
      WAITL0();
      BARRIER();
      u32x4 wt[2][8];
      if (wave < 2) {
        float gs = 0.f, us = 0.f;
#pragma unroll
        for (int w = 0; w < 8; ++w) {
          gs += red[w * 256 + tid];
          us += red[w * 256 + 128 + tid];
        }
        const float gv = rbf(gs), uv = rbf(us);
        const float hv = rbf(gv * __builtin_amdgcn_rcpf(1.0f + __expf(-gv))) * uv;
        hs[(tid & 1) * 64 + (tid >> 1)] = (u16)pk2(hv, hv);
      } else if (wave >= 3) {
        WAITV(0);
#pragma unroll
        for (int j = 0; j < 16; ++j) {
          wt[j >> 3][j & 7] = wd[j >> 3][j & 7];
          PIN(wt[j >> 3][j & 7]);
        }
        transp(wt[0]);
        transp(wt[1]);
      }
      WAITL0();
      BARRIER();
      if (wave == 2) res_wait(ws, smem, lane, tag, 1);
      if (wave >= 3) {
        const u32x4 h0 = *(const u32x4*)(hs + (c15 & 1) * 64 + g4 * 8), h1 = *(const u32x4*)(hs + (c15 & 1) * 64 + 32 + g4 * 8);
#pragma unroll
        for (int j = 0; j < 8; ++j) acc[j] = mfma16(h0, wt[0][j], (f32x4){0.f, 0.f, 0.f, 0.f});
#pragma unroll
        for (int j = 0; j < 8; ++j) acc[j] = mfma16(h1, wt[1][j], acc[j]);
        *(f32x4*)(red + 2048 + wave * 256 + c15 * 16 + g4 * 4) = pick(acc);
        const f32x4 y = *(const f32x4*)(red + 2048 + wave * 256 + lane * 4);
#pragma unroll
        for (int t = 0; t < 2; ++t)
          __builtin_amdgcn_raw_buffer_store_b128((u32x4){__float_as_uint(y[t]), tag, __float_as_uint(y[2 + t]), tag}, ryp,
                                                 ((f * 2 + t) * 640 + (wave - 3) * 128 + lane * 2) * 8, 0, 16);
      }
      WAITL0();
      BARRIER();
    }
    wo_dma(IC<3>{}, wave, 8, 25);
    if (wave == 0) wo_dma(IC<1>{}, 24, 8, 25);
    if (wave == 1) gate_dma(gate_off(lane));
    if (tid < 8) *(unsigned*)(smem + L_VAR + tid * 4) = 0u;
    if (wave == 2 || f == 30) {
      const unsigned tp = __builtin_amdgcn_readfirstlane(*(const unsigned*)(smem + L_CAND2)) + D1;
#pragma unroll 1
      for (unsigned spins = 0; spins < 128 && (int)(now() - tp) < 0; ++spins) __builtin_amdgcn_s_sleep(2);
    }
    if (wave == 2) {
      const unsigned t_ = now() + D2;
      const u32x4 v = __builtin_amdgcn_raw_buffer_load_b128(rfl, flag_off(), 0, 16);
      *(u32x4*)(smem + L_FS + lane * 16) = (u32x4){(v[1] == tag ? v[0] : t_) + (ETA_F - ETA_G), tag, 0u, tag};
    }
    if (wave == 0) rows_job();
    WAITV(0);
    if (f == 30 && wave == 3) {
      const __amdgpu_buffer_rsrc_t ra = __builtin_amdgcn_make_buffer_rsrc((void*)(ws + WS_A), 0, 4096, 0x00020000);
#pragma unroll 1
      for (unsigned spins = 0; spins < SPIN_CAP; ++spins) {
        asm volatile("" ::: "memory");
        if (__all(__builtin_amdgcn_raw_buffer_load_b32(ra, (lane & 31) * 128, 0, 16) == tag)) break;
        __builtin_amdgcn_s_sleep(8);
      }
      if (lane == 0) __hip_atomic_store((gu32*)(ws + WS_E), tag, RLX_AGENT);
    }
    WAITL0();
    BARRIER();
  }

  {
    const int m = wave & 3, kh = (wave >> 2) ^ (jj & KSW);
    const bool snd = (wave >> 2) != FST;
    const int oo = (rt2 * 4 + m) * 20480 + kh * 8192, ot = (rt2 * 4 + m) * 20480 + 16384 + kh * 2048;
    const int l16 = lane * 16;
    if (PRIO && !snd) __builtin_amdgcn_s_setprio(PRIO);
    u32x4 fs = *(const u32x4*)(smem + L_FS + l16);
    const u32x4 kk = *(const u32x4*)(smem + L_VAR + 32);
    u32x2 g0_ = *(const u32x2*)(smem + glo);
    u32x4 yw[2][7];
    int yo, y6, yp, yx;
    auto y_prep = [&]() {
      int ly = lane;
      PIN(ly);
      const int cp = (ly & 15) < 10 ? ly & 15 : 0, col = m * 20 + cp * 2 + (ly >> 5);
      yo = (ly >> 4) * 71680 + (nt2 * 80 + m * 20 + cp * 2) * 8;
      y6 = ly >= 48 ? 0x7FFFFF00 : yo;
      yp = (((col >> 4) + 5 - (nt2 & 1)) % 5) * 1024 + (col & 12) * 64 + (col & 3) * 4;
      yx = L_XT + col * 2;
      PIN(yo); PIN(y6); PIN(yp); PIN(yx);
    };
    auto y_issue = [&](u32x4(&yv)[7], int tok, bool again) {
      char* wy = ws;
      asm volatile("" : "+s"(wy));
      const u32x4 srd = make_srd(wy + WS_YP + tok * 5120, 27 * 10240);
#pragma unroll
      for (int j = 0; j < 7; ++j) {
        if (again) asm volatile("buffer_load_dwordx4 %0, %1, %2, %3 offen sc1" : "+v"(yv[j]) : "v"(j < 6 ? yo : y6), "s"(srd), "s"(j * 10240) : "memory");
        else asm volatile("buffer_load_dwordx4 %0, %1, %2, %3 offen sc1" : "=v"(yv[j]) : "v"(j < 6 ? yo : y6), "s"(srd), "s"(j * 10240) : "memory");
      }
    };
    auto y_done = [&](u32x4(&yv)[7], int tok, int row) {
      volatile LAS unsigned* fl = (volatile LAS unsigned*)(LAS char*)(smem + L_VAR + 16 + (row >> 4) * 4);
      volatile LAS float* pe = (volatile LAS float*)(LAS char*)(smem + yp + (row >> 4) * 5120 + (row & 15) * 16);
      unsigned f_, xv_;
      float omg, pv;
      auto rd = [&]() {
        f_ = *fl;
        omg = *(volatile LAS float*)(LAS char*)(smem + L_JUNK + row * 4);
        xv_ = *(volatile LAS u16*)(LAS char*)(smem + yx + row * 160);
        pv = *pe;
      };
      rd();
      const float rsel = *(const float*)(smem + L_JUNK + 256 + tok * 4);
      SB();
      unsigned lo = tag, hi = tag;
      float y[2] = {0.f, 0.f};
#pragma unroll
      for (int j = 0; j < 7; ++j) {
        PIN(yv[j]);
        if (j == 6 && g4 == 3) yv[j][1] = yv[j][3] = tag;
        lo = min(min(lo, yv[j][1]), yv[j][3]);
        hi = max(max(hi, yv[j][1]), yv[j][3]);
        y[0] += __uint_as_float(yv[j][0]);
        PIN(y[0]);
        y[1] += __uint_as_float(yv[j][2]);
        PIN(y[1]);
      }
      if (!__all(lo == hi)) return false;
      const auto a = __builtin_amdgcn_permlane32_swap(__float_as_uint(y[0]), __float_as_uint(y[1]), false, false);
      const float h = __uint_as_float(a[0]) + __uint_as_float(a[1]);
      const auto c = __builtin_amdgcn_permlane16_swap(__float_as_uint(h), __float_as_uint(h), false, false);
      const float r = rbf(rbf(__uint_as_float(c[0]) + __uint_as_float(c[1])) * rsel);
#pragma unroll 1
      for (unsigned spins = 0; spins < (SPIN_CAP << 6) && !__builtin_amdgcn_readfirstlane(f_); ++spins) rd();
      const float xv = bflo(xv_);
      *pe = pv + (rbf(xv + r) - xv) * omg;
      asm volatile("ds_add_u32 %0, %1" ::"v"(fl), "v"(1u) : "memory");
      return true;
    };
    const int l0 = __builtin_amdgcn_readfirstlane(kk[2]), l1 = __builtin_amdgcn_readfirstlane(kk[3]);
    unsigned ry = min((unsigned)l0, (unsigned)l1);
    unsigned need = 1u + ((l0 >> 4) == m ? 256u : 0u) + ((l1 >> 4) == m ? 256u : 0u);
    asm volatile("" : "+s"(ry), "+s"(need));
#define yjob (ry < 64u)
    if (wave == 7) {
      if (__builtin_expect((~__builtin_amdgcn_ballot_w64((fs[1] == tag) & (fs[3] == tag)) & 0xFFull) != 0, 0)) {
        flag_wait(lane >= 8 ? 0x7FFFFF00 : flag_off());
        WAITL0();
        if (lane == 0) *(volatile LAS unsigned*)(LAS char*)(smem + L_VAR + 8) = 1u;
      }
    }
    if (!snd) {
      dma16(adr[0], x, lds0 + L_XT + m * 2560);
      dma16(adr[1], x, lds0 + L_XT + m * 2560 + 1024);
      if (lane < 32) dma16(adr[2], x, lds0 + L_XT + m * 2560 + 2048);
    }
    SB();
    const char* wb = smem + L_WO + kh * 51200 + l16;
    u32x4 wq[WPD][5];
    auto wrd = [&](int k) {
#pragma unroll
      for (int f = 0; f < 5; ++f) wq[k % WPD][f] = *(const u32x4*)(wb + (k * 5 + f) * 1024);
    };
#pragma unroll
    for (int k = 0; k < NPRE; ++k) wrd(k);
    if (snd) {
#pragma unroll
      for (int k = NPRE; k < NPRE2; ++k) wrd(k);
    }
    if (__builtin_expect(!__all(g0_[1] == tag), 0)) {
      const __amdgpu_buffer_rsrc_t rg_ = __builtin_amdgcn_make_buffer_rsrc((void*)(ws + WS_K), 0, 36864, 0x00020000);
#pragma unroll 1
      for (unsigned spins = 0; spins < SPIN_CAP; ++spins) {
        asm volatile("" ::: "memory");
        g0_ = __builtin_amdgcn_raw_buffer_load_b64(rg_, gate_off(m * 16 + c15), 0, 16);
        if (__all(g0_[1] == tag)) break;
      }
    }
    const float gt = __uint_as_float(g0_[0]);
    SB();
    {
      const u64 inv = ~__builtin_amdgcn_ballot_w64((fs[1] == tag) & (fs[3] == tag));
      if (__builtin_expect((inv & rel) != 0, 0)) {
#pragma unroll 1
        for (unsigned spins = 0; spins < (SPIN_CAP << 6); ++spins) {
          if (__builtin_amdgcn_readfirstlane(*(volatile LAS unsigned*)(LAS char*)(smem + L_VAR + 8))) break;
          __builtin_amdgcn_s_sleep(1);
        }
        fs[0] = *(volatile LAS unsigned*)(LAS char*)(smem + L_FS + l16);
      }
      const unsigned tg = fs[0] + ETA_G;
      unsigned spins = 128;
#pragma unroll 1
      while ((__builtin_amdgcn_ballot_w64((int)(now() - tg) < 0) & rel) != 0 && --spins) {}
    }
    u32x4 of[8];
    u32x2 ofl[2], ofh[2];
    const u32x4 sro = make_srd(ws + WS_OF, S_LEN * DM * 2);
    int l8 = lane * 8;
    asm volatile("buffer_load_dwordx4 %0, %8, %9, %10 offen sc1\n\tbuffer_load_dwordx4 %1, %8, %9, %10 offen offset:1024 sc1\n\t"
                 "buffer_load_dwordx4 %2, %8, %9, %10 offen offset:2048 sc1\n\tbuffer_load_dwordx4 %3, %8, %9, %10 offen offset:3072 sc1\n\t"
                 "buffer_load_dwordx4 %4, %8, %9, %11 offen sc1\n\tbuffer_load_dwordx4 %5, %8, %9, %11 offen offset:1024 sc1\n\t"
                 "buffer_load_dwordx4 %6, %8, %9, %11 offen offset:2048 sc1\n\tbuffer_load_dwordx4 %7, %8, %9, %11 offen offset:3072 sc1"
                 : "=&v"(of[0]), "=&v"(of[1]), "=&v"(of[2]), "=&v"(of[3]), "=&v"(of[4]), "=&v"(of[5]), "=&v"(of[6]), "=&v"(of[7])
                 : "v"(l16), "s"(sro), "s"(oo), "s"(oo + 4096)
                 : "memory");
    asm volatile("buffer_load_dwordx2 %0, %4, %5, %6 offen sc1\n\tbuffer_load_dwordx2 %1, %4, %5, %6 offen offset:512 sc1\n\t"
                 "buffer_load_dwordx2 %2, %4, %5, %6 offen offset:1024 sc1\n\tbuffer_load_dwordx2 %3, %4, %5, %6 offen offset:1536 sc1"
                 : "=&v"(ofl[0]), "=&v"(ofh[0]), "=&v"(ofl[1]), "=&v"(ofh[1])
                 : "v"(l8), "s"(sro), "s"(ot)
                 : "memory");
    if (!snd) {
#pragma unroll
      for (int k = NPRE; k < NPRE2; ++k) wrd(k);
    }
#pragma unroll
    for (int k = NPRE2; k < WPD; ++k) wrd(k);
    auto o_again = [&](int k) {
      if (k < 8)
        asm volatile("buffer_load_dwordx4 %0, %1, %2, %3 offen sc1" : "+v"(of[k]) : "v"(l16), "s"(sro), "s"(oo + k * 1024) : "memory");
      else
        asm volatile("buffer_load_dwordx2 %0, %2, %3, %4 offen sc1\n\tbuffer_load_dwordx2 %1, %2, %3, %4 offen offset:512 sc1"
                     : "+v"(ofl[k - 8]), "+v"(ofh[k - 8])
                     : "v"(l8), "s"(sro), "s"(ot + (k - 8) * 1024)
                     : "memory");
    };
    auto o_pin = [&](int k) {
      if (k < 8) PIN(of[k]);
      else asm volatile("" : "+v"(ofl[k - 8]), "+v"(ofh[k - 8]));
    };
    auto o_get = [&](int k) { return k < 8 ? of[k] : (u32x4){ofl[k - 8][0], ofl[k - 8][1], ofh[k - 8][0], ofh[k - 8][1]}; };
    auto o_bad = [&](int k) {
      const u32x4 v = o_get(k);
      return __any(max(max(v[0], v[1]), max(v[2], v[3])) == SENT);
    };
    int pa_ = m * 5120 + l16;
    PIN(pa_);
    volatile LAS unsigned* pfl = (volatile LAS unsigned*)(LAS char*)(smem + L_VAR + 16 + m * 4);
    u32x4 rout = make_srd(out, S_LEN * DM * 2);
    asm volatile("" : "+s"(rout));
    f32x4 ac2[5], pp[5];
    unsigned pf_ = 0;
    if (!snd) {
      u32x2 xb[5];
      if (__builtin_expect(yjob, 0)) y_prep();
      WAITV(12);
#pragma unroll
      for (int f = 0; f < 5; ++f) xb[f] = *(const u32x2*)(smem + L_XT + m * 2560 + c15 * 160 + g4 * 8 + ((nt2 & 1) ? (f + 1) % 5 : f) * 32);
      const float omg = 1.0f - gt;
      *(float*)(smem + L_JUNK + (m * 16 + c15) * 4) = omg;
#pragma unroll
      for (int f = 0; f < 5; ++f) {
        pp[f] = (f32x4){bflo(xb[f][0]), bfhi(xb[f][0]), bflo(xb[f][1]), bfhi(xb[f][1])} * omg;
        PIN(pp[f]);
      }
    }
#pragma unroll
    for (int f = 0; f < 5; ++f) ac2[f] = (f32x4){0.f, 0.f, 0.f, 0.f};
    __builtin_amdgcn_sched_barrier(0);
    constexpr int GS[5] = {0, 2, 5, 8, 10};
    auto vleft = [](int k) { return k < 8 ? 11 - k : k == 8 ? 2 : 0; };
    WAITV(10);
    o_pin(0);
    o_pin(1);
    bool bad = o_bad(0) | o_bad(1);
#pragma unroll
    for (int g = 0; g < 4; ++g) {
      if (__builtin_expect(bad, 0)) {
        WAITV(0);
#pragma unroll 1
        for (unsigned spins = 0; spins < SPIN_CAP; ++spins) {
          bool any = false;
#pragma unroll
          for (int k2 = GS[g]; k2 < 10; ++k2) {
            o_pin(k2);
            if (o_bad(k2)) {
              o_again(k2);
              if (k2 < GS[g + 1]) any = true;
            }
          }
          WAITV(0);
          if (!any) break;
        }
      }
      if (g == 3) break;
      unsigned mxv = 0;
      const int nm = (GS[g + 1] - GS[g]) * 5, nn = g < 3 ? GS[g + 2] - GS[g + 1] : 0;
#pragma unroll
      for (int i = 0; i < nm; ++i) {
        const int k = GS[g] + i / 5, f = i % 5;
        const int j = i - (nm - 2 - 2 * nn);
        const int kn = GS[g + 1] + j / 2;
        if (j >= 0 && j < 2 * nn && !(j & 1)) {
          WAITV_(vleft(kn));
          o_pin(kn);
          SB();
        }
        ac2[f] = mfma16(wq[k % WPD][f], o_get(k), ac2[f]);
        SB();
        if (k + WPD < 10) wq[k % WPD][f] = *(const u32x4*)(wb + ((k + WPD) * 5 + f) * 1024);
        if (j >= 0 && j < 2 * nn) {
          const u32x4 nv = o_get(kn);
          mxv = max(max(mxv, nv[(j & 1) * 2]), nv[(j & 1) * 2 + 1]);
        }
        if (nn && j == 2 * nn) bad = __any(mxv == SENT);
        if (g == 2 && i == PRD - 1) __builtin_amdgcn_s_waitcnt(0xC07F);
        if (g == 2 && i == PRD && snd) {
          pf_ = *pfl;
          asm volatile("" ::: "memory");
#pragma unroll
          for (int f = 0; f < 5; ++f) pp[f] = *(const f32x4*)(smem + pa_ + f * 1024);
        }
        SB();
      }
    }
    u32x4 o8 = o_get(8), o9 = o_get(9);
    PIN(o8); PIN(o9);
    auto mm = [&](int f) {
      ac2[f] = mfma16(wq[8 % WPD][f], o8, ac2[f]);
      SB();
      ac2[f] = mfma16(wq[9 % WPD][f], o9, ac2[f]);
      SB();
    };
    auto fin = [&](int f, int q) {
      pp[f][q] = __builtin_fmaf(ac2[f][q], gt, pp[f][q]);
      PIN(pp[f][q]);
    };
    if (!snd) {
      if (__builtin_expect(yjob, 0)) {
        y_issue(yw[0], (unsigned)l0 >= 64u, false);
        if (max((unsigned)l0, (unsigned)l1) < 64u) y_issue(yw[1], 1, false);
      }
      SB();
      auto wr = [&](int f) {
#pragma unroll
        for (int q = 0; q < 4; ++q) fin(f, q);
        *(f32x4*)(smem + pa_ + f * 1024) = pp[f];
        SB();
      };
      mm(0); mm(1); mm(2);
      wr(0); mm(3); wr(1); mm(4);
      wr(2); wr(3); wr(4);
      asm volatile("" ::: "memory");
      *pfl = 1u;
      if (__builtin_expect(yjob, 0)) {
        int tok = (unsigned)l0 >= 64u;
        WAITV(0);
#pragma unroll
        for (int j = 0; j < 7; ++j) PIN(yw[1][j]);
#pragma unroll 1
        for (unsigned spins = 0; spins < SPIN_CAP; ++spins) {
          if (y_done(yw[0], tok, tok ? l1 : l0)) {
            if (tok || (unsigned)l1 >= 64u) break;
            tok = 1;
#pragma unroll
            for (int j = 0; j < 7; ++j) yw[0][j] = yw[1][j];
          } else {
            y_issue(yw[0], tok, true);
            WAITV(0);
          }
        }
      }
    } else {
      if (__builtin_expect(__builtin_amdgcn_readfirstlane(pf_) != need, 0)) {
#pragma unroll 1
        for (unsigned spins = 0; spins < (SPIN_CAP << 6); ++spins)
          if (__builtin_amdgcn_readfirstlane(*pfl) == need) break;
        asm volatile("" ::: "memory");
#pragma unroll
        for (int f = 0; f < 5; ++f) pp[f] = *(const f32x4*)(smem + pa_ + f * 1024);
      }
      SB();
      u32x2 r[5];
      auto cv = [&](int f) {
#pragma unroll
        for (int q = 0; q < 4; ++q) fin(f, q);
        r[f] = (u32x2){pk2(pp[f][0], pp[f][1]), pk2(pp[f][2], pp[f][3])};
        SB();
      };
      auto st = [&](int f) {
        const auto a = __builtin_amdgcn_permlane16_swap(r[f][0], r[f + 1][0], false, false);
        const auto c = __builtin_amdgcn_permlane16_swap(r[f][1], r[f + 1][1], false, false);
        asm volatile("buffer_store_dwordx4 %0, %1, %2, 0 offen offset:%3" OPOL "\n\ts_nop 1" ::"v"((u32x4){a[0], c[0], a[1], c[1]}), "v"(adr[0]), "s"(rout), "i"(f * 32) : "memory");
        SB();
      };
      mm(0); mm(1); mm(2);
      cv(0); mm(3); cv(1); mm(4);
      st(0); cv(2); cv(3); st(2); cv(4);
      asm volatile("buffer_store_dwordx2 %0, %1, %2, 0 offen" OPOL2 ::"v"(r[4]), "v"(adr[1]), "s"(rout) : "memory");
    }
  }
}

extern "C" void kernel_launch(void* const* d_in, const int* in_sizes, int n_in, void* d_out, int out_size, void* d_ws,
                              size_t ws_size, hipStream_t stream) {
  kf<<<256, 512, 0, stream>>>((const u16*)d_in[0], (const u16*)d_in[1], (const u16*)d_in[2], (const u16*)d_in[4],
                              (const u16*)d_in[8], (const u16*)d_in[5], (const u16*)d_in[6], (const u16*)d_in[7],
                              (char*)d_ws, (u16*)d_out);
}
```

```cpp
#include <hip/hip_runtime.h>
#include <stdint.h>

typedef unsigned short u16;
typedef unsigned long long u64;
typedef __attribute__((ext_vector_type(8))) __bf16 bf16x8;
typedef __attribute__((ext_vector_type(2))) __bf16 bf16x2;
typedef __attribute__((ext_vector_type(4))) short s16x4;
typedef __attribute__((ext_vector_type(4))) float f32x4;
typedef __attribute__((ext_vector_type(2))) float f32x2;
typedef __attribute__((ext_vector_type(4))) unsigned u32x4;
typedef __attribute__((ext_vector_type(2))) unsigned u32x2;
typedef __attribute__((ext_vector_type(10))) unsigned u32x10;
#define GAS __attribute__((address_space(1)))
#define LAS __attribute__((address_space(3)))
typedef GAS unsigned gu32;

#define S_LEN 2048
#define DM 640
#define NQKV 1920
#define FF 1728

#define WS_OF 0
#define WS_WOF 2621440
#define WS_E (WS_WOF + 819200)
#define WS_F2 (WS_E + 256)
#define WS_F (WS_F2 + 32768)
#define WS_K (WS_F + 28672)
#define WS_R (WS_K + 36864)
#define WS_A (WS_R + 256)
#define WS_YP (WS_A + 4096)
#define TAG_STEP 0x9E3779B9u
#define SENT 0xFFFFFFFFu
#define SPIN_CAP (1u << 18)
#define NPRE 3
#define WPD 7
#define PRD 10
#define WB8 2
#define TCY 2750
#define ETA_G 15
#define ETA_F 20
#define D1 350
#define D2 75
#define DK 220
#define RLX_AGENT __ATOMIC_RELAXED, __HIP_MEMORY_SCOPE_AGENT

__device__ __forceinline__ float bflo(unsigned u) { return __uint_as_float(u << 16); }
__device__ __forceinline__ float bfhi(unsigned u) { return __uint_as_float(u & 0xFFFF0000u); }
__device__ __forceinline__ unsigned pk2(float a, float b) {
  f32x2 v = {a, b};
  return __builtin_bit_cast(unsigned, __builtin_convertvector(v, bf16x2));
}
__device__ __forceinline__ float rbf(float f) { return bflo(pk2(f, f)); }
__device__ __forceinline__ unsigned perm_lo(unsigned hi_src, unsigned lo_src) {
  return __builtin_amdgcn_perm(hi_src, lo_src, 0x05040100u);
}
__device__ __forceinline__ unsigned perm_hi(unsigned hi_src, unsigned lo_src) {
  return __builtin_amdgcn_perm(hi_src, lo_src, 0x07060302u);
}
__device__ __forceinline__ f32x4 mfma16(u32x4 a, u32x4 b, f32x4 c) {
  return __builtin_amdgcn_mfma_f32_16x16x32_bf16(__builtin_bit_cast(bf16x8, a), __builtin_bit_cast(bf16x8, b), c, 0, 0, 0);
}
__device__ __forceinline__ u32x2 tr_read(const char* p) {
  return __builtin_bit_cast(u32x2, __builtin_amdgcn_ds_read_tr16_b64_v4i16((LAS s16x4*)(p)));
}
#define BARRIER() asm volatile("s_barrier" ::: "memory")
#define WAITV(n) asm volatile("s_waitcnt vmcnt(" #n ")" ::: "memory")
#define WAITV_(n) asm volatile("s_waitcnt vmcnt(%0)" ::"i"(n) : "memory")
#define WAITL0() asm volatile("s_waitcnt lgkmcnt(0)" ::: "memory")

__device__ __forceinline__ void top2_mrg(unsigned& k1, unsigned& k2, unsigned b1, unsigned b2) {
  k2 = max(max(k2, b2), min(k1, b1));
  k1 = max(k1, b1);
}
template <int CTRL>
__device__ __forceinline__ void top2_dpp(unsigned& k1, unsigned& k2) {
  const unsigned b1 = (unsigned)__builtin_amdgcn_update_dpp(0, (int)k1, CTRL, 0xF, 0xF, true);
  const unsigned b2 = (unsigned)__builtin_amdgcn_update_dpp(0, (int)k2, CTRL, 0xF, 0xF, true);
  top2_mrg(k1, k2, b1, b2);
}
__device__ __forceinline__ void top2_row(unsigned& k1, unsigned& k2) {
  top2_dpp<0xB1>(k1, k2);
  top2_dpp<0x4E>(k1, k2);
  top2_dpp<0x141>(k1, k2);
  top2_dpp<0x140>(k1, k2);
}
__device__ __forceinline__ void top2_all(unsigned& k1, unsigned& k2) {
  top2_row(k1, k2);
  {
    auto a = __builtin_amdgcn_permlane16_swap(k1, k1, false, false);
    auto c = __builtin_amdgcn_permlane16_swap(k2, k2, false, false);
    k1 = a[0];
    k2 = c[0];
    top2_mrg(k1, k2, a[1], c[1]);
  }
  {
    auto a = __builtin_amdgcn_permlane32_swap(k1, k1, false, false);
    auto c = __builtin_amdgcn_permlane32_swap(k2, k2, false, false);
    k1 = a[0];
    k2 = c[0];
    top2_mrg(k1, k2, a[1], c[1]);
  }
}
__device__ __forceinline__ void touch_line(const char* p, unsigned lds_off) {
  asm volatile("s_mov_b32 m0, %1\n\ts_nop 0\n\tglobal_load_lds_dword %0, off" ::"v"(p), "s"(lds_off) : "memory");
}
__device__ __forceinline__ void dma16(unsigned voff, const void* sbase, unsigned lds_off) {
  asm volatile("s_mov_b32 m0, %2\n\ts_nop 0\n\tglobal_load_lds_dwordx4 %0, %1" ::"v"(voff), "s"(sbase), "s"(lds_off) : "memory");
}
__device__ __forceinline__ void dma16_sc1(unsigned voff, const void* sbase, unsigned lds_off) {
  asm volatile("s_mov_b32 m0, %2\n\ts_nop 0\n\tglobal_load_lds_dwordx4 %0, %1 sc1" ::"v"(voff), "s"(sbase), "s"(lds_off) : "memory");
}
__device__ __forceinline__ void bdma16(unsigned voff, u32x4 srd, unsigned soff, unsigned lds_off) {
  asm volatile("s_mov_b32 m0, %3\n\ts_nop 0\n\tbuffer_load_dwordx4 %0, %1, %2 offen lds" ::"v"(voff), "s"(srd), "s"(soff), "s"(lds_off) : "memory");
}
__device__ __forceinline__ u32x4 make_srd(const void* p, unsigned bytes) {
  return (u32x4){(unsigned)(uintptr_t)p, (unsigned)((uintptr_t)p >> 32) & 0xFFFFu, bytes, 0x00020000u};
}
__device__ __forceinline__ float key_val(unsigned key) {
  unsigned u = key >> 16;
  u ^= (u & 0x8000u) ? 0x8000u : 0xFFFFu;
  return bflo(u);
}
__device__ __forceinline__ float fmx(float a, float b) { return __builtin_elementwise_maximum(a, b); }
#define SB() __builtin_amdgcn_sched_barrier(0)
__device__ __forceinline__ f32x4 mfma16h(u32x2 a, u32x2 b, f32x4 c) {
  return __builtin_amdgcn_mfma_f32_16x16x16bf16_1k(__builtin_bit_cast(s16x4, a), __builtin_bit_cast(s16x4, b), c, 0, 0, 0);
}

#define SLOT_B 43008
#define L_WO SLOT_B
#define L_EARLY (3 * SLOT_B)
#define L_PIECE L_EARLY
#define L_CAND (L_VAR + 128)
#define L_TOUCH (L_EARLY + 12288)
#define L_JUNK (L_WO + 102400)
#define L_VAR (L_JUNK + 384)
#define L_CAND2 (L_VAR + 48)
#define L_GT (L_VAR + 256)
#define L_FS (L_GT + 1024)
#define L_XT (L_FS + 1024)
#define L_TOTAL (L_XT + 10240)
#define CODE_LINES 255

__device__ __forceinline__ void res_wait(char* ws, char* smem, int lane, unsigned tag, int which) {
  const __amdgpu_buffer_rsrc_t r = __builtin_amdgcn_make_buffer_rsrc((void*)(ws + WS_R), 0, 256, 0x00020000);
  u32x4 v;
#pragma unroll 1
  for (unsigned spins = 0; spins < SPIN_CAP; ++spins) {
    asm volatile("" ::: "memory");
    v = __builtin_amdgcn_raw_buffer_load_b128(r, which * 128, 0, 16);
    if (__all((v[1] == tag) & (v[3] == tag))) break;
    __builtin_amdgcn_s_sleep(2);
  }
  if (lane == 0 && which == 0) *(u32x2*)(smem + L_VAR + 32) = (u32x2){v[0], v[2]};
  if (lane == 0 && which == 1) *(u32x4*)(smem + L_CAND2) = v;
}
__device__ __forceinline__ float dot2(unsigned a, unsigned b, float c) {
  return __builtin_amdgcn_fdot2_f32_bf16(__builtin_bit_cast(bf16x2, a), __builtin_bit_cast(bf16x2, b), c, false);
}
template <int CTRL>
__device__ __forceinline__ float dpp_add(float v) {
  return v + __int_as_float(__builtin_amdgcn_update_dpp(0, __float_as_int(v), CTRL, 0xF, 0xF, true));
}
__device__ __forceinline__ float row_sum(float v) {
  v = dpp_add<0xB1>(v);
  v = dpp_add<0x4E>(v);
  v = dpp_add<0x141>(v);
  return dpp_add<0x140>(v);
}

template <int N> struct IC { static constexpr int value = N; };
#define PIN(v) asm volatile("" : "+v"(v))
__global__ __launch_bounds__(512, 2) void kf(const u16*, const u16*, const u16*, const u16*, const u16*, const u16*, const u16*, const u16*, char*, u16*) {
  __shared__ __attribute__((aligned(16))) char smem[L_TOTAL];
  typedef const __attribute__((address_space(4))) u64* karg_t;
  const karg_t kp0 = (karg_t)__builtin_amdgcn_kernarg_segment_ptr();
  u64 pc;
  asm volatile("s_load_dword s100, %1, 0x0\n\ts_load_dword s101, %1, 0x40\n\ts_getpc_b64 %0" : "=s"(pc) : "s"(kp0) : "s100", "s101");
  const u16 *x, *wqkv, *wo, *w_r, *gate_w, *w_gate, *w_up, *w_down;
  char* ws;
  u16* out;
  auto args = [&]() {
    SB();
    karg_t kp = kp0;
    asm volatile("" : "+s"(kp));
    x = (const u16*)kp[0]; wqkv = (const u16*)kp[1]; wo = (const u16*)kp[2]; w_r = (const u16*)kp[3]; gate_w = (const u16*)kp[4];
    w_gate = (const u16*)kp[5]; w_up = (const u16*)kp[6]; w_down = (const u16*)kp[7]; ws = (char*)kp[8]; out = (u16*)kp[9];
  };
  const int tid = threadIdx.x, b = blockIdx.x;
  {
    const int ln = (b >> 3) + 32 * tid;
    if (ln < CODE_LINES)
      touch_line((const char*)((pc & ~255ull) + ln * 128), (unsigned)(uintptr_t)(LAS char*)smem + L_TOUCH);
  }
  unsigned tag;
  auto get_tag = [&]() {
    asm volatile("s_load_dword %0, %1, 0x0\n\ts_waitcnt lgkmcnt(0)" : "=s"(tag) : "s"(ws + WS_E) : "memory");
    tag += TAG_STEP;
  };
  const int lane = tid & 63;
  const int wave = __builtin_amdgcn_readfirstlane(tid >> 6);
  const int xcd = b & 7, jj = b >> 3;
  const int c15 = lane & 15, g4 = lane >> 4;
  const bool gemm_blk = jj < 28;
  const int q_ = (xcd & 3) * 28 + jj;
  const int rg = q_ >> 2, hg = (xcd >> 2) * 4 + (q_ & 3);
  const int par = rg >= 14, gi = rg - par * 14;
  const int tb = min(73 * gi, 944);
  const int tlo = gi == 0 ? 0 : 73 * gi + 7;
  const int lg = jj >> 2;
  const int rt2 = gemm_blk ? par * 16 + gi : (xcd & 2) * 8 + 14 + (xcd & 1), nt2 = hg;
  const unsigned lds0 = (unsigned)(uintptr_t)(LAS char*)smem;
  auto wo_dma = [&](auto n_, int i0, int st, int lo) {
    const u32x4 srd = make_srd(ws + WS_WOF + nt2 * 102400, 102400);
    int g = __builtin_amdgcn_readfirstlane((i0 + ((lg * (25 - lo)) >> 3)) * 4096);
#pragma unroll
    for (int i = 0; i < decltype(n_)::value; ++i) {
      if (g >= 102400) g -= (25 - lo) * 4096;
      asm volatile("s_add_u32 m0, %3, %2\n\ts_nop 0\n\tbuffer_load_dwordx4 %0, %1, %2 offen sc1 lds\n\t"
                   "buffer_load_dwordx4 %0, %1, %2 offen offset:1024 sc1 lds\n\tbuffer_load_dwordx4 %0, %1, %2 offen offset:2048 sc1 lds\n\t"
                   "buffer_load_dwordx4 %0, %1, %2 offen offset:3072 sc1 lds" ::"v"(lane * 16), "s"(srd), "s"(g), "s"(lds0 + L_WO)
                   : "memory", "scc");
      g += st * 4096;
    }
  };

  int od[5];
  const int pa = tb + c15 - 64 * gi;
  const bool pv = tb + c15 >= tlo;
  auto prepO = [&]() {
    const int h = hg * 4 + (wave & 3);
    const int b0 = (par * 64 + 56 + (pa >> 4)) * 20480, b1 = (par * 64 + gi * 4) * 20480;
    od[0] = (pv && lane < 32) ? b0 + (2 * h + g4) * 256 + (pa & 15) * 16 : 0x7FFFFF00;
    od[1] = b1 + (lane >> 5) * 20480 + (2 * h + (g4 & 1)) * 256 + c15 * 16;
    od[2] = od[1] + 2 * 20480;
    od[3] = b1 + g4 * 20480 + 16384 + h * 128 + c15 * 8;
    od[4] = (pv && g4 == 3) ? b0 + 16384 + h * 128 + (pa & 15) * 8 : 0x7FFFFF00;
#pragma unroll
    for (int e = 0; e < 5; ++e) PIN(od[e]);
  };
  auto row_t = [&](int row) {
    const int a = (xcd & 1) * 64 + row;
    return gemm_blk ? tb + 16 + row : a + (a < 16 ? 0 : (a - 7) / 9) * 64;
  };
  int adr[3], glo;
  u64 rel;
  auto tile_off = [&](int row) { return ((2 * row_t(row) + (rt2 >> 4)) * DM + nt2 * 80) * 2; };
  auto prepB = [&]() {
    const int row = (wave & 3) * 16 + c15;
    if (wave < 4) {
#pragma unroll
      for (int e = 0; e < 3; ++e) {
        const int c = min(e * 64 + lane, 159), r = c / 10;
        adr[e] = tile_off((wave & 3) * 16 + r) + (c - r * 10) * 16;
      }
    } else {
      adr[2] = tile_off(row);
      adr[0] = adr[2] + (nt2 & 1) * 32 + (g4 & 1) * 32 + (g4 >> 1) * 16;
      adr[1] = adr[2] + ((nt2 & 1) ? 0 : 128) + g4 * 8;
    }
    PIN(adr[0]); PIN(adr[1]); PIN(adr[2]);
    glo = L_GT + row * 16 + (((gemm_blk ? tb : 0) + row) & 1) * 8;
    PIN(glo);
    {
      const int a0 = (xcd & 1) * 64 + (wave & 3) * 16, gl = (xcd & 1) ? 6 + (lane >> 3) : min(lane >> 3, 6);
      rel = __builtin_amdgcn_ballot_w64(((lane >> 2) & 1) == (wave >> 2) && (gemm_blk ? lane < 8 : gl >= (a0 < 16 ? 0 : (a0 - 7) / 9) && gl <= (a0 + 8) / 9));
      asm volatile("" : "+s"(rel));
    }
  };
  auto lrow_of = [&](unsigned key) {
    const int i = 2047 - (int)(key & 0xFFFFu);
    const int t = i >> 1, g = max(t - 7, 0) / 73, rho = t - min(73 * g, 944);
    return (i & 1) == (rt2 >> 4) ? (rho >= 16 ? g * 64 + rho - 16 : 896 + t - 64 * g) - (rt2 & 15) * 64 : -1;
  };
  auto rows_job = [&]() {
    if (lane < 2) {
      const unsigned key = *(const unsigned*)(smem + L_VAR + 32 + lane * 4);
      *(int*)(smem + L_VAR + 40 + lane * 4) = lrow_of(key);
      *(float*)(smem + L_JUNK + 256 + lane * 4) = rbf(__builtin_amdgcn_rcpf(1.0f + __expf(-key_val(key))));
    }
  };
  auto gate_off = [&](int row) {
    const int p2 = rt2 >> 4, tg = row_t(row);
    const int q2 = p2 * 2 + (tg >> 9), u = tg & 511;
    return (u < 448 ? (q2 * 56 + (u >> 3)) * 128 + (u & 7) * 8 : (224 + q2 * 16 + ((u - 448) >> 2)) * 128 + (u & 3) * 8) + 64;
  };
  auto flag_off = [&]() {
    const int g = gemm_blk ? gi : (xcd & 1) ? 6 + (lane >> 3) : min(lane >> 3, 6);
    return (((rt2 >> 4) * 14 + g) * 8 + (lane & 7)) * 128 + 56;
  };
#define rfl __builtin_amdgcn_make_buffer_rsrc((void*)(ws + WS_F), 0, 28672, 0x00020000)
  auto gate_dma = [&](int go) { dma16_sc1(go & ~15, ws + WS_K, lds0 + L_GT); };
  auto now = []() { return (unsigned)__builtin_amdgcn_s_memrealtime(); };
  auto flag_wait = [&](int foff) {
#pragma unroll 1
    for (unsigned spins = 0; spins < SPIN_CAP; ++spins) {
      asm volatile("" ::: "memory");
      const u32x4 v = __builtin_amdgcn_raw_buffer_load_b128(rfl, foff, 0, 16);
      if ((v[1] == tag) & (v[3] == tag) & (foff != 0x7FFFFF00)) {
        *(u32x4*)(smem + L_FS + lane * 16) = v;
        foff = 0x7FFFFF00;
      }
      if (__all(foff == 0x7FFFFF00)) break;
      __builtin_amdgcn_s_sleep(8);
    }
  };
#define ro __builtin_amdgcn_make_buffer_rsrc((void*)(ws + WS_OF), 0, S_LEN * DM * 2, 0x00020000)

  if (__builtin_expect(gemm_blk, 1)) {
    if (__builtin_expect(wave >= 4, 1)) {
      const int hw = wave - 4;
      const int rot = ((jj >> 2) % 5) * 2 + (xcd & 1);
      int voffA = (lane >> 3) * (DM * 4) + (((lane & 7) ^ (((hw & 1) * 4 + g4) & 7)) * 16);
      int sA[3];
#pragma unroll
      for (int v = 0; v < 3; ++v) {
        sA[v] = ((tb + 8 * (hw + 4 * v)) * 2 + par) * (DM * 2);
      }
      const int hi = lane >> 5;
      int pair = ((lane & 31) >> 1) ^ (hi | ((hw & 1) << 1) | ((hw >> 1) << 2));
      const int sec = (pair >= 5) + (pair >= 10);
      int voffW = pair == 15 ? 0x7FFFFF00 : hi * (NQKV * 2) + sec * (1280 - 160) + hg * 160 + pair * 32 + ((lane & 1) ^ (sec == 2)) * 16;
      const int n0 = (hw & 1) + 4 * (hw >> 1);
      PIN(voffA); PIN(voffW);
      args();
      const u32x4 rsx = make_srd(x, S_LEN * DM * 2), rsw = make_srd(wqkv, DM * NQKV * 2);
      auto issue = [&](int i, int buf) {
        int kc = rot + (i < 5 ? 2 * i : 2 * i - 9);
        if (kc >= 10) kc -= 10;
        const unsigned dst = lds0 + buf * SLOT_B;
#pragma unroll
        for (int v = 0; v < 3; ++v)
          if (v < 2 || hw < 2) bdma16(voffA, rsx, sA[v] + kc * 128, dst + (hw + 4 * v) * 1024);
        const int so = (kc * 64 + 2 * n0) * (NQKV * 2);
#pragma unroll
        for (int u = 0; u < 8; ++u) {
          const int dn = 2 * (u & 1) + 8 * (u >> 1);
          bdma16(voffW, rsw, so + dn * 2 * (NQKV * 2), dst + 10240 + (n0 + dn) * 1024);
        }
      };
      const int qp = rg * 8 + hg;
      issue(0, 0);
      SB();
      int md = (par * 64 + gi * 4 + 2 * (hw & 1) + (lane >> 5)) * 20480 + 16384 + hg * 512 + (lane & 31) * 16;
      PIN(md);
      int so_ = hw == 0 ? flag_off() : gate_off(lane);
      PIN(so_);
      prepB();
      __builtin_amdgcn_sched_barrier(0);
      issue(1, 1);
      get_tag();
      WAITV(10);
      BARRIER();
      issue(2, 2);
      WAITV(10);
      BARRIER();
      const unsigned r0_ = now(), c0_ = (unsigned)__builtin_amdgcn_s_memtime();
      {
        int buf = 0;
#pragma unroll 1
        for (int i = 3; i < 9; ++i) {
          const bool ex = i == 4 && hw == 2;
          if (ex && lane == 0) __hip_atomic_store((gu32*)(ws + WS_F2 + b * 128), tag, RLX_AGENT);
          issue(i, buf);
          if (i == 3)
            asm volatile("s_load_dword s100, %0, 0x0\n\ts_load_dword s100, %0, 0xa00\n\ts_load_dword s100, %0, 0x1400\n\ts_load_dword s100, %0, 0x1e00\n\t"
                         "s_load_dword s100, %0, 0x2800\n\ts_load_dword s100, %0, 0x3200\n\ts_load_dword s100, %0, 0x3c00\n\ts_load_dword s100, %0, 0x4600" ::"s"(
                             (const char*)out + tile_off(hw * 8 + (nt2 & 1) * 32) + ((nt2 & 1) ? 0 : 128))
                         : "s100");
          if (ex) WAITV(11); else WAITV(10);
          buf = buf == 2 ? 0 : buf + 1;
          BARRIER();
        }
      }
      if (hw == 3) {
        if (lane < 2) dma16_sc1(lane * 128, ws + WS_R, lds0 + L_CAND);
        if (lane < 8) *(unsigned*)(smem + L_VAR + lane * 4) = 0u;
      }
      issue(9, 0);
      auto mark = [&]() {
        const u32x4 sent4 = {SENT, SENT, SENT, SENT};
        const int mo_ = (par * 64 + gi * 4 + (hw & 1) * 2) * 20480 + hg * 2048;
#pragma unroll
        for (int e = 0; e < 5; ++e) {
          if (e == 0) __builtin_amdgcn_raw_buffer_store_b128(sent4, ro, md, 0, 16);
          else __builtin_amdgcn_raw_buffer_store_b128(sent4, ro, lane * 16 + ((e - 1) & 1) * 1024, mo_ + ((e - 1) >> 1) * 20480, 16);
        }
        SB();
      };
      auto words = [&]() {
        const u32x4 rv = *(const u32x4*)(smem + L_CAND + (lane & 1) * 16);
        if (__all((rv[1] == tag) & (rv[3] == tag))) {
          if (tid == 256) *(u32x2*)(smem + L_VAR + 32) = (u32x2){rv[0], rv[2]};
        } else {
          res_wait(ws, smem, lane, tag, 0);
          res_wait(ws, smem, lane, tag, 1);
        }
      };
      if (hw >= 2) {
        mark();
        WAITV(15);
        BARRIER();
        WAITV(5);
        BARRIER();
        unsigned t0_ = now();
        t0_ += (unsigned)((float)(t0_ - r0_) * (float)TCY * __builtin_amdgcn_rcpf((float)((unsigned)__builtin_amdgcn_s_memtime() - c0_)));
        WAITV(0);
        __builtin_amdgcn_raw_buffer_store_b128((u32x4){t0_, tag, t0_, tag}, rfl, lane < 4 ? qp * 128 + (hw & 1) * 64 + lane * 16 : 0x7FFFFF00, 0, 16);
        if (hw == 2) rows_job();
      } else {
        WAITV(10);
        BARRIER();
        words();
        wo_dma(IC<WB8>{}, hw, 2, 25);
        WAITV_(4 * WB8);
        WAITL0();
        BARRIER();
        if (hw == 1) gate_dma(so_);
        wo_dma(IC<12 - WB8>{}, 2 * WB8 + hw, 2, 2 * WB8);
        if (hw == 0 && lane < 8) dma16_sc1(so_, ws + WS_F, lds0 + L_FS);
        if (hw == 0) wo_dma(IC<1>{}, 24, 2, 2 * WB8);
        WAITV(0);
      }
      WAITL0();
      BARRIER();
    } else {
      const int xb_ = b - 160;
      const bool wjob = wave < 2 || (wave == 2 && (unsigned)xb_ < 64u);
      const int xi = (xb_ >> 3) * 2 + (xcd >> 2);
      const int tj0 = (xcd & 1) * 512 + (wave < 2 ? lg * 64 + hg * 8 + wave * 4 : 448 + xi * 4), pj = par;
      u32x4 xs[5], wv[4];
      int jo0 = lane * 16, jo1 = g4 * 2560 + c15 * 16, jo2 = c15 * 16;
      PIN(jo0); PIN(jo1); PIN(jo2);
      args();
      if (wjob) {
        const u16* xr = x + (2 * tj0 + pj) * DM;
        asm volatile("global_load_dwordx4 %0, %9, %12\n\tglobal_load_dwordx4 %1, %9, %12 offset:2560\n\t"
                     "global_load_dwordx4 %2, %9, %13\n\tglobal_load_dwordx4 %3, %9, %13 offset:2560\n\t"
                     "global_load_dwordx4 %4, %10, %12 offset:1024\n\tglobal_load_dwordx4 %5, %9, %14\n\t"
                     "global_load_dwordx4 %6, %11, %14 offset:1024\n\tglobal_load_dwordx4 %7, %9, %15\n\tglobal_load_dwordx4 %8, %11, %15 offset:1024"
                     : "=&v"(xs[0]), "=&v"(xs[1]), "=&v"(xs[2]), "=&v"(xs[3]), "=&v"(xs[4]), "=&v"(wv[0]), "=&v"(wv[1]), "=&v"(wv[2]), "=&v"(wv[3])
                     : "v"(jo0), "v"(jo1), "v"(jo2), "s"(xr), "s"(xr + 4 * DM), "s"(w_r), "s"(gate_w)
                     : "memory");
      }
      f32x4 acc[5][4];
#pragma unroll
      for (int m = 0; m < 5; ++m)
#pragma unroll
        for (int f = 0; f < 4; ++f) acc[m][f] = (f32x4){0.f, 0.f, 0.f, 0.f};
      const int wks = b % 20;
      if (b < 200) {
        const int kh = wks >= 10, s = wks - kh * 10, pr = lane >> 3, g = wave, hi = pr >> 2;
        const int row = (s < 8 ? (2 * (kh * 8 + s) + (g >> 1)) * 20 + 4 * (g & 1) + 8 * hi : (kh * 16 + (s - 8) * 8 + hi * 4 + g) * 20 + 16) + (pr & 3);
        asm volatile("s_mov_b32 m0, %2\n\ts_nop 0\n\tglobal_load_lds_dwordx4 %0, %1 nt" ::"v"((row * DM + (b / 20) * 64 + (lane & 7) * 8) * 2), "s"(wo), "s"(lds0 + L_PIECE + wave * 1024) : "memory");
      }
      const __amdgpu_buffer_rsrc_t rk = __builtin_amdgcn_make_buffer_rsrc((void*)(ws + WS_K), 0, 36864, 0x00020000);
      const int ko = c15 != 0 ? 0x7FFFFF00 : wave < 2 ? (rg * 8 + hg) * 128 + (wave * 4 + g4) * 8 : (224 + (xcd & 3) * 16 + xi) * 128 + g4 * 8;
      auto rows4 = [&](float (&p)[5]) {
        const auto a = __builtin_amdgcn_permlane32_swap(__float_as_uint(p[0]), __float_as_uint(p[2]), false, false);
        const auto c = __builtin_amdgcn_permlane32_swap(__float_as_uint(p[1]), __float_as_uint(p[3]), false, false);
        const auto e = __builtin_amdgcn_permlane16_swap(__float_as_uint(__uint_as_float(a[0]) + __uint_as_float(a[1])),
                                                        __float_as_uint(__uint_as_float(c[0]) + __uint_as_float(c[1])), false, false);
        return row_sum(__uint_as_float(e[0]) + __uint_as_float(e[1]) + p[4]);
      };
      get_tag();
      if (wjob) {
        if (b < 200) WAITV(3); else WAITV(2);
#pragma unroll
        for (int i = 0; i < 5; ++i) PIN(xs[i]);
        PIN(wv[0]); PIN(wv[1]);
        float p[5];
#pragma unroll
        for (int i = 0; i < 5; ++i) {
          p[i] = 0.f;
#pragma unroll
          for (int q = 0; q < 4; ++q) p[i] = dot2(xs[i][q], wv[i == 4][q], p[i]);
        }
        const float r_ = rows4(p);
        unsigned u = pk2(r_, r_) & 0xFFFFu;
        u ^= (u & 0x8000u) ? 0xFFFFu : 0x8000u;
        const unsigned key = (u << 16) | (unsigned)(2047 - (2 * (tj0 + g4) + pj));
        __builtin_amdgcn_raw_buffer_store_b64((u32x2){key, tag}, rk, ko, 0, 16);
        SB();
      }
      u32x2 gv = {0u, 0u};
      int ga = 0x7FFFFF00;
      if (wjob) {
        WAITV(1);
        PIN(wv[2]); PIN(wv[3]);
        float p[5];
#pragma unroll
        for (int i = 0; i < 5; ++i) {
          p[i] = 0.f;
#pragma unroll
          for (int q = 0; q < 4; ++q) {
            const unsigned gw = wv[2 + (i == 4)][q];
            p[i] = dot2(pk2(bflo(xs[i][q]) * bflo(gw), bfhi(xs[i][q]) * bfhi(gw)), 0x3F803F80u, p[i]);
          }
        }
        const float g_ = rbf(rows4(p));
        gv = (u32x2){__float_as_uint(rbf(1.0f / (1.0f + __expf(-g_)))), tag};
        ga = c15 != 0 ? ko : ko + 64;
        SB();
      }
      PIN(gv); PIN(ga);
      prepO();
      const int p4 = lane & 3;
      f32x4 limA, limB;
      const int e0 = c15 - 4 * g4;
#pragma unroll
      for (int i = 0; i < 4; ++i) {
        limA[i] = e0 - i <= -9 ? 0.f : -__builtin_inff();
        limB[i] = (e0 - i >= 0 && e0 - i <= 7) ? 0.f : -__builtin_inff();
      }
      unsigned mA01 = (e0 <= -9 ? 0xFFFFu : 0u) | (e0 - 1 <= -9 ? 0xFFFF0000u : 0u);
      unsigned mA23 = (e0 - 2 <= -9 ? 0xFFFFu : 0u) | (e0 - 3 <= -9 ? 0xFFFF0000u : 0u);
      PIN(limA); PIN(limB); PIN(mA01); PIN(mA23);
      const unsigned mA[2] = {mA01, mA23};
      u32x4 ones = {0x3F803F80u, 0x3F803F80u, 0x3F803F80u, 0x3F803F80u};
      PIN(ones);
      u32x2 sel[4], kz[5];
#pragma unroll
      for (int t = 0; t < 4; ++t) {
        const bool on = g4 == 2 && (((c15 >> 2) - t + 1) & 3) < 2;
        sel[t] = (u32x2){on && p4 < 2 ? 0x3F80u << (16 * p4) : 0u, on && p4 >= 2 ? 0x3F80u << (16 * (p4 - 2)) : 0u};
        PIN(sel[t]);
      }
#pragma unroll
      for (int t = 0; t < 5; ++t) {
        kz[t] = (u32x2){0u, 0u};
        PIN(kz[t]);
      }
      const int swz = (c15 >> 1) & 7;
      const int aoff0 = c15 * 128 + ((g4 ^ swz) * 16);
      const int aoff1 = c15 * 128 + (((4 + g4) ^ swz) * 16);
      int woff[4];
      {
        const int q4 = c15 >> 2, sw = q4 | ((g4 & 1) << 2);
#pragma unroll
        for (int t = 0; t < 4; ++t) {
          const int sc = t < 3 ? t : p4;
          const int by = wave * 40 + (t < 3 ? p4 : 4) * 8;
          woff[t] = 10240 + (8 * g4 + q4) * 512 + (((sc == 3 ? 15 : sc * 5 + (by >> 5)) ^ sw) * 32) + ((by & 31) ^ (sc == 2 ? 16 : sc == 3 ? 8 : 0));
          PIN(woff[t]);
        }
      }
      int wo3[2][4], ao3[2][2];
#pragma unroll
      for (int q = 0; q < 2; ++q) {
#pragma unroll
        for (int j = 0; j < 4; ++j) {
          wo3[q][j] = woff[j] + (q + 1) * SLOT_B;
          PIN(wo3[q][j]);
        }
        ao3[q][0] = aoff0 + (q + 1) * SLOT_B;
        ao3[q][1] = aoff1 + (q + 1) * SLOT_B;
        PIN(ao3[q][0]); PIN(ao3[q][1]);
      }
#define GM(m, f, wf, af) acc[m][f] = (f) == 2 ? mfma16(af[m], wf[f], acc[m][f]) : mfma16(wf[f], af[m], acc[m][f])
      u32x4 af0[5], wf0[4], af1[5], wf1[4];
      auto rd1 = [&](u32x4(&an)[5], u32x4(&wn)[4], int q, int h, int r) {
        const int ao = q ? ao3[q - 1][h] : h ? aoff1 : aoff0;
        if (r == 2) {
          an[0] = *(const u32x4*)(smem + ao);
        } else if (r >= 9) {
          an[r - 8] = *(const u32x4*)(smem + ao + (r - 8) * 2048);
        } else {
          const int k = r < 2 ? r : r - 1;
          const u32x2 t = tr_read(smem + (q ? wo3[q - 1][k >> 1] : woff[k >> 1]) + h * 16384 + (k & 1) * 2048);
          wn[k >> 1][(k & 1) * 2] = t[0];
          wn[k >> 1][(k & 1) * 2 + 1] = t[1];
        }
      };
      auto half = [&](const u32x4(&a)[5], const u32x4(&w)[4], u32x4(&an)[5], u32x4(&wn)[4], int q, int h) {
#pragma unroll
        for (int i = 0; i < 20; ++i) {
          GM(i >> 2, i & 3, w, a);
          SB();
          if (i < 13) rd1(an, wn, q, h, i);
          SB();
        }
      };
      u32x4 wfr;
      int wfa;
      {
        if (!wjob) WAITV(0);
        const char* tp = smem + L_PIECE + wave * 1024 + (c15 >> 2) * 128 + g4 * 32 + (lane & 3) * 8;
        const u32x2 lo = tr_read(tp), hi = tr_read(tp + 512);
        const int T = (b / 20) * 4 + g4;
        wfr = (u32x4){lo[0], lo[1], hi[0], hi[1]};
        wfa = b < 200 ? (((T / 5) * 20 + wks) * 5 + (T + 5 - ((T / 5) & 1)) % 5) * 1024 + (wave * 16 + c15) * 16 : 0x7FFFFF00;
        PIN(wfr); PIN(wfa);
      }
      WAITL0();
      BARRIER();
#pragma unroll
      for (int r = 0; r < 13; ++r) rd1(af0, wf0, 0, 0, r);
      SB();
#pragma unroll
      for (int i = 0; i < 9; ++i) {
        half(af0, wf0, af1, wf1, i % 3, 1);
        if (i == 0) {
          __builtin_amdgcn_raw_buffer_store_b128(wfr, __builtin_amdgcn_make_buffer_rsrc((void*)(ws + WS_WOF), 0, 819200, 0x00020000), wfa, 0, 16);
          __builtin_amdgcn_raw_buffer_store_b64(gv, rk, ga, 0, 16);
        }
        if (i == 1) WAITV(0);
        if (i == 6) prepB();
        SB();
        WAITL0();
        BARRIER();
        if (i < 8) half(af1, wf1, af0, wf0, (i + 1) % 3, 0);
      }
      u32x4 wf2[4], af2[5];
      u32x4 qf[5], kf[5];
      u32x10 vb, tb_;
      u32x4 pf[5], os[3];
      u32x2 pp[5], ob[6], obT, obU;
      f32x4 l[5], sA[5], oA[5], sm[5], tx[5], oT[3];
      f32x2 ar[5][2], nn0[2], nn1[2], nn2[2], nn3[2], nn4[2], nnT[2], nnU[2];
      float mx[6], inv[5], it, iu;
      constexpr u64 mo = 0xFFFF0000FFFF0000ull, mh = 0xFFFFFFFF00000000ull;
      auto selm = [](u64 m_, float a, float c) { return __builtin_amdgcn_inverse_ballot_w64(m_) ? a : c; };
      const float cs = 0.22360679774997896f * 1.4426950408889634f;
      const f32x2 c2 = {cs, cs};
      const f32x4 zero4 = {0.f, 0.f, 0.f, 0.f};
      auto cpy = [](float v) { PIN(v); return v; };
      auto cpyu = [](unsigned v) { PIN(v); return v; };
      auto mulo = [](float v, float c) { PIN(v); return v * c; };
#define XBAR() BARRIER()
      GM(0, 3, wf1, af1); SB();
      af0[0] = *(const u32x4*)(smem + aoff0 + 0); SB();
      { const u32x2 t_ = tr_read(smem + woff[3] + 0); wf0[3][0] = t_[0]; wf0[3][1] = t_[1]; } SB();
      GM(0, 1, wf1, af1); SB();
      { const u32x2 t_ = tr_read(smem + woff[3] + 2048); wf0[3][2] = t_[0]; wf0[3][3] = t_[1]; } SB();
      { const u32x2 t_ = tr_read(smem + woff[1] + 0); wf0[1][0] = t_[0]; wf0[1][1] = t_[1]; } SB();
      GM(0, 0, wf1, af1); SB();
      { const u32x2 t_ = tr_read(smem + woff[1] + 2048); wf0[1][2] = t_[0]; wf0[1][3] = t_[1]; } SB();
      { const u32x2 t_ = tr_read(smem + woff[0] + 0); wf0[0][0] = t_[0]; wf0[0][1] = t_[1]; } SB();
      GM(0, 2, wf1, af1); SB();
      { const u32x2 t_ = tr_read(smem + woff[0] + 2048); wf0[0][2] = t_[0]; wf0[0][3] = t_[1]; } SB();
      af2[0] = *(const u32x4*)(smem + aoff1 + 0); SB();
      GM(1, 3, wf1, af1); SB();
      { const u32x2 t_ = tr_read(smem + woff[2] + 0); wf0[2][0] = t_[0]; wf0[2][1] = t_[1]; } SB();
      { const u32x2 t_ = tr_read(smem + woff[3] + 16384); wf2[3][0] = t_[0]; wf2[3][1] = t_[1]; } SB();
      GM(1, 1, wf1, af1); SB();
      { const u32x2 t_ = tr_read(smem + woff[2] + 2048); wf0[2][2] = t_[0]; wf0[2][3] = t_[1]; } SB();
      { const u32x2 t_ = tr_read(smem + woff[3] + 18432); wf2[3][2] = t_[0]; wf2[3][3] = t_[1]; } SB();
      GM(1, 0, wf1, af1); SB();
      { const u32x2 t_ = tr_read(smem + woff[1] + 16384); wf2[1][0] = t_[0]; wf2[1][1] = t_[1]; } SB();
      { const u32x2 t_ = tr_read(smem + woff[1] + 18432); wf2[1][2] = t_[0]; wf2[1][3] = t_[1]; } SB();
      GM(1, 2, wf1, af1); SB();
      { const u32x2 t_ = tr_read(smem + woff[0] + 16384); wf2[0][0] = t_[0]; wf2[0][1] = t_[1]; } SB();
      { const u32x2 t_ = tr_read(smem + woff[0] + 18432); wf2[0][2] = t_[0]; wf2[0][3] = t_[1]; } SB();
      GM(2, 3, wf1, af1); SB();
      af0[1] = *(const u32x4*)(smem + aoff0 + 2048); SB();
      { const u32x2 t_ = tr_read(smem + woff[2] + 16384); wf2[2][0] = t_[0]; wf2[2][1] = t_[1]; } SB();
      GM(2, 1, wf1, af1); SB();
      af2[1] = *(const u32x4*)(smem + aoff1 + 2048); SB();
      { const u32x2 t_ = tr_read(smem + woff[2] + 18432); wf2[2][2] = t_[0]; wf2[2][3] = t_[1]; } SB();
      GM(2, 0, wf1, af1); SB();
      af0[2] = *(const u32x4*)(smem + aoff0 + 4096); SB();
      af2[2] = *(const u32x4*)(smem + aoff1 + 4096); SB();
      GM(2, 2, wf1, af1); SB();
      af0[3] = *(const u32x4*)(smem + aoff0 + 6144); SB();
      af2[3] = *(const u32x4*)(smem + aoff1 + 6144); SB();
      GM(0, 3, wf0, af0); SB();
      af0[4] = *(const u32x4*)(smem + aoff0 + 8192); SB();
      af2[4] = *(const u32x4*)(smem + aoff1 + 8192); SB();
      GM(0, 1, wf0, af0); SB();
      GM(0, 0, wf0, af0); SB();
      GM(0, 2, wf0, af0); SB();
      GM(0, 3, wf2, af2); SB();
      GM(0, 1, wf2, af2); SB();
      GM(0, 0, wf2, af2); SB();
      GM(0, 2, wf2, af2); SB();
      GM(1, 3, wf0, af0); SB();
      GM(1, 1, wf0, af0); SB();
      GM(1, 0, wf0, af0); SB();
      GM(1, 2, wf0, af0); SB();
      qf[0][2] = pk2(acc[0][3][0], acc[0][3][1]); SB();
      GM(1, 3, wf2, af2); SB();
      qf[0][3] = pk2(acc[0][3][2], acc[0][3][3]); SB();
      { const auto r_ = __builtin_amdgcn_permlane16_swap(qf[0][2], kz[0][0], false, false); qf[0][2] = r_[0]; kf[0][2] = r_[1]; } SB();
      GM(1, 1, wf2, af2); SB();
      kf[0][0] = pk2(acc[0][1][0], acc[0][1][1]); SB();
      { const auto r_ = __builtin_amdgcn_permlane16_swap(qf[0][3], kz[0][1], false, false); qf[0][3] = r_[0]; kf[0][3] = r_[1]; } SB();
      GM(1, 0, wf2, af2); SB();
      kf[0][1] = pk2(acc[0][1][2], acc[0][1][3]); SB();
      qf[0][0] = pk2(acc[0][0][0], acc[0][0][1]); SB();
      tx[0] = mfma16h((u32x2){qf[0][2], qf[0][3]}, sel[0], zero4); SB();
      qf[1][2] = pk2(acc[1][3][0], acc[1][3][1]); SB();
      qf[1][3] = pk2(acc[1][3][2], acc[1][3][3]); SB();
      GM(1, 2, wf2, af2); SB();
      { const auto r_ = __builtin_amdgcn_permlane16_swap(qf[1][2], kz[1][0], false, false); qf[1][2] = r_[0]; kf[1][2] = r_[1]; } SB();
      kf[1][0] = pk2(acc[1][1][0], acc[1][1][1]); SB();
      GM(2, 3, wf0, af0); SB();
      qf[1][0] = pk2(acc[1][0][0], acc[1][0][1]); SB();
      { const auto r_ = __builtin_amdgcn_permlane16_swap(qf[1][3], kz[1][1], false, false); qf[1][3] = r_[0]; kf[1][3] = r_[1]; } SB();
      GM(2, 1, wf0, af0); SB();
      kf[1][1] = pk2(acc[1][1][2], acc[1][1][3]); SB();
      qf[1][1] = pk2(acc[1][0][2], acc[1][0][3]); SB();
      tx[1] = mfma16h((u32x2){qf[1][2], qf[1][3]}, sel[1], zero4); SB();
      qf[0][1] = pk2(acc[0][0][2], acc[0][0][3]); SB();
      tb_[0] = pk2(tx[0][0], tx[0][1]); SB();
      l[1] = mfma16(kf[1], qf[1], limB); SB();
      tb_[1] = pk2(tx[0][2], tx[0][3]); SB();
      vb[0] = pk2(acc[0][2][0], acc[0][2][1]); SB();
      sA[1] = mfma16(kf[0], qf[1], limA); SB();
      vb[1] = pk2(acc[0][2][2], acc[0][2][3]); SB();
      vb[2] = pk2(acc[1][2][0], acc[1][2][1]); SB();
      l[0] = mfma16(kf[0], qf[0], limB); SB();
      vb[3] = pk2(acc[1][2][2], acc[1][2][3]); SB();
      tb_[2] = pk2(tx[1][0], tx[1][1]); SB();
      GM(2, 0, wf0, af0); SB();
      tb_[3] = pk2(tx[1][2], tx[1][3]); SB();
      GM(2, 2, wf0, af0); SB();
      l[1][0] = fmx(l[1][0], sA[1][0]); SB();
      l[1][1] = fmx(l[1][1], sA[1][1]); SB();
      GM(2, 3, wf2, af2); SB();
      l[1][2] = fmx(l[1][2], sA[1][2]); SB();
      mx[0] = fmx(fmx(l[0][0], l[0][1]), l[0][2]); SB();
      GM(2, 1, wf2, af2); SB();
      l[1][3] = fmx(l[1][3], sA[1][3]); SB();
      mx[1] = fmx(fmx(l[1][0], l[1][1]), l[1][2]); SB();
      GM(2, 0, wf2, af2); SB();
      mx[0] = fmx(mx[0], l[0][3]); SB();
      mx[1] = fmx(mx[1], l[1][3]); SB();
      GM(2, 2, wf2, af2); SB();
      qf[2][2] = pk2(acc[2][3][0], acc[2][3][1]); SB();
      qf[2][3] = pk2(acc[2][3][2], acc[2][3][3]); SB();
      GM(3, 3, wf1, af1); SB();
      { const auto r_ = __builtin_amdgcn_permlane16_swap(qf[2][2], kz[2][0], false, false); qf[2][2] = r_[0]; kf[2][2] = r_[1]; } SB();
      kf[2][0] = pk2(acc[2][1][0], acc[2][1][1]); SB();
      GM(3, 1, wf1, af1); SB();
      qf[2][0] = pk2(acc[2][0][0], acc[2][0][1]); SB();
      { const auto r_ = __builtin_amdgcn_permlane16_swap(qf[2][3], kz[2][1], false, false); qf[2][3] = r_[0]; kf[2][3] = r_[1]; } SB();
      GM(3, 0, wf1, af1); SB();
      kf[2][1] = pk2(acc[2][1][2], acc[2][1][3]); SB();
      qf[2][1] = pk2(acc[2][0][2], acc[2][0][3]); SB();
      tx[2] = mfma16h((u32x2){qf[2][2], qf[2][3]}, sel[2], zero4); SB();
      { const auto r_ = __builtin_amdgcn_permlane32_swap(__float_as_uint(mx[0]), __float_as_uint(mx[1]), false, false); mx[0] = __uint_as_float(r_[0]); mx[1] = __uint_as_float(r_[1]); } SB();
      mx[0] = fmx(mx[0], mx[1]); SB();
      l[2] = mfma16(kf[2], qf[2], limB); SB();
      mx[1] = cpy(mx[0]); SB();
      vb[4] = pk2(acc[2][2][0], acc[2][2][1]); SB();
      sA[2] = mfma16(kf[1], qf[2], limA); SB();
      { const auto r_ = __builtin_amdgcn_permlane16_swap(__float_as_uint(mx[0]), __float_as_uint(mx[1]), false, false); mx[0] = __uint_as_float(r_[0]); mx[1] = __uint_as_float(r_[1]); } SB();
      mx[0] = fmx(mx[0], mx[1]); SB();
      GM(3, 2, wf1, af1); SB();
      mx[1] = mx[0] * -cs; SB();
      mx[0] = mulo(mx[0], -cs); SB();
      GM(3, 3, wf0, af0); SB();
      vb[5] = pk2(acc[2][2][2], acc[2][2][3]); SB();
      { const auto r_ = __builtin_amdgcn_permlane32_swap(__float_as_uint(mx[0]), __float_as_uint(mx[1]), false, false); mx[0] = __uint_as_float(r_[0]); mx[1] = __uint_as_float(r_[1]); } SB();
      GM(3, 1, wf0, af0); SB();
      l[2][0] = fmx(l[2][0], sA[2][0]); SB();
      l[2][1] = fmx(l[2][1], sA[2][1]); SB();
      GM(3, 0, wf0, af0); SB();
      l[2][2] = fmx(l[2][2], sA[2][2]); SB();
      l[2][3] = fmx(l[2][3], sA[2][3]); SB();
      GM(3, 2, wf0, af0); SB();
      mx[2] = fmx(fmx(l[2][0], l[2][1]), l[2][2]); SB();
      mx[2] = fmx(mx[2], l[2][3]); SB();
      GM(3, 3, wf2, af2); SB();
      ar[1][0] = __builtin_elementwise_fma((f32x2){l[1][0], l[1][1]}, c2, (f32x2){mx[1], mx[1]}); SB();
      ar[1][1] = __builtin_elementwise_fma((f32x2){l[1][2], l[1][3]}, c2, (f32x2){mx[1], mx[1]}); SB();
      GM(3, 1, wf2, af2); SB();
      ar[0][0] = __builtin_elementwise_fma((f32x2){l[0][0], l[0][1]}, c2, (f32x2){mx[0], mx[0]}); SB();
      ar[0][1] = __builtin_elementwise_fma((f32x2){l[0][2], l[0][3]}, c2, (f32x2){mx[0], mx[0]}); SB();
      GM(3, 0, wf2, af2); SB();
      ar[1][0][0] = __builtin_amdgcn_exp2f(ar[1][0][0]); SB();
      GM(3, 2, wf2, af2); SB();
      qf[3][2] = pk2(acc[3][3][0], acc[3][3][1]); SB();
      qf[3][3] = pk2(acc[3][3][2], acc[3][3][3]); SB();
      GM(4, 3, wf1, af1); SB();
      { const auto r_ = __builtin_amdgcn_permlane16_swap(qf[3][2], kz[3][0], false, false); qf[3][2] = r_[0]; kf[3][2] = r_[1]; } SB();
      kf[3][0] = pk2(acc[3][1][0], acc[3][1][1]); SB();
      GM(4, 1, wf1, af1); SB();
      qf[3][0] = pk2(acc[3][0][0], acc[3][0][1]); SB();
      { const auto r_ = __builtin_amdgcn_permlane16_swap(qf[3][3], kz[3][1], false, false); qf[3][3] = r_[0]; kf[3][3] = r_[1]; } SB();
      GM(4, 0, wf1, af1); SB();
      kf[3][1] = pk2(acc[3][1][2], acc[3][1][3]); SB();
      qf[3][1] = pk2(acc[3][0][2], acc[3][0][3]); SB();
      tx[3] = mfma16h((u32x2){qf[3][2], qf[3][3]}, sel[3], zero4); SB();
      ar[1][0][1] = __builtin_amdgcn_exp2f(ar[1][0][1]); SB();
      l[3] = mfma16(kf[3], qf[3], limB); SB();
      ar[1][1][0] = __builtin_amdgcn_exp2f(ar[1][1][0]); SB();
      sA[3] = mfma16(kf[2], qf[3], limA); SB();
      ar[1][1][1] = __builtin_amdgcn_exp2f(ar[1][1][1]); SB();
      GM(4, 2, wf1, af1); SB();
      ar[0][0][0] = __builtin_amdgcn_exp2f(ar[0][0][0]); SB();
      GM(4, 3, wf0, af0); SB();
      ar[0][0][1] = __builtin_amdgcn_exp2f(ar[0][0][1]); SB();
      GM(4, 1, wf0, af0); SB();
      ar[0][1][0] = __builtin_amdgcn_exp2f(ar[0][1][0]); SB();
      GM(4, 0, wf0, af0); SB();
      l[3][0] = fmx(l[3][0], sA[3][0]); SB();
      l[3][1] = fmx(l[3][1], sA[3][1]); SB();
      GM(4, 2, wf0, af0); SB();
      l[3][2] = fmx(l[3][2], sA[3][2]); SB();
      l[3][3] = fmx(l[3][3], sA[3][3]); SB();
      GM(4, 3, wf2, af2); SB();
      mx[3] = fmx(fmx(l[3][0], l[3][1]), l[3][2]); SB();
      mx[3] = fmx(mx[3], l[3][3]); SB();
      GM(4, 1, wf2, af2); SB();
      ar[0][1][1] = __builtin_amdgcn_exp2f(ar[0][1][1]); SB();
      GM(4, 0, wf2, af2); SB();
      { const auto r_ = __builtin_amdgcn_permlane32_swap(__float_as_uint(mx[2]), __float_as_uint(mx[3]), false, false); mx[2] = __uint_as_float(r_[0]); mx[3] = __uint_as_float(r_[1]); } SB();
      mx[2] = fmx(mx[2], mx[3]); SB();
      GM(4, 2, wf2, af2); SB();
      qf[4][2] = pk2(acc[4][3][0], acc[4][3][1]); SB();
      qf[4][3] = pk2(acc[4][3][2], acc[4][3][3]); SB();
      mx[3] = cpy(mx[2]); SB();
      { const auto r_ = __builtin_amdgcn_permlane16_swap(qf[4][2], kz[4][0], false, false); qf[4][2] = r_[0]; kf[4][2] = r_[1]; } SB();
      kf[4][0] = pk2(acc[4][1][0], acc[4][1][1]); SB();
      qf[4][0] = pk2(acc[4][0][0], acc[4][0][1]); SB();
      { const auto r_ = __builtin_amdgcn_permlane16_swap(qf[4][3], kz[4][1], false, false); qf[4][3] = r_[0]; kf[4][3] = r_[1]; } SB();
      kf[4][1] = pk2(acc[4][1][2], acc[4][1][3]); SB();
      qf[4][1] = pk2(acc[4][0][2], acc[4][0][3]); SB();
      tx[4] = mfma16h((u32x2){qf[4][2], qf[4][3]}, sel[0], zero4); SB();
      { const auto r_ = __builtin_amdgcn_permlane16_swap(__float_as_uint(mx[2]), __float_as_uint(mx[3]), false, false); mx[2] = __uint_as_float(r_[0]); mx[3] = __uint_as_float(r_[1]); } SB();
      mx[2] = fmx(mx[2], mx[3]); SB();
      l[4] = mfma16(kf[4], qf[4], limB); SB();
      mx[3] = mx[2] * -cs; SB();
      mx[2] = mulo(mx[2], -cs); SB();
      sA[4] = mfma16(kf[3], qf[4], limA); SB();
      pp[1][0] = pk2(ar[1][0][0], ar[1][0][1]); SB();
      { const auto r_ = __builtin_amdgcn_permlane32_swap(__float_as_uint(mx[2]), __float_as_uint(mx[3]), false, false); mx[2] = __uint_as_float(r_[0]); mx[3] = __uint_as_float(r_[1]); } SB();
      ar[2][0] = __builtin_elementwise_fma((f32x2){l[2][0], l[2][1]}, c2, (f32x2){mx[2], mx[2]}); SB();
      ar[2][1] = __builtin_elementwise_fma((f32x2){l[2][2], l[2][3]}, c2, (f32x2){mx[2], mx[2]}); SB();
      ar[2][0][0] = __builtin_amdgcn_exp2f(ar[2][0][0]); SB();
      ar[2][0][1] = __builtin_amdgcn_exp2f(ar[2][0][1]); SB();
      ar[2][1][0] = __builtin_amdgcn_exp2f(ar[2][1][0]); SB();
      ar[2][1][1] = __builtin_amdgcn_exp2f(ar[2][1][1]); SB();
      l[4][0] = fmx(l[4][0], sA[4][0]); SB();
      l[4][1] = fmx(l[4][1], sA[4][1]); SB();
      l[4][2] = fmx(l[4][2], sA[4][2]); SB();
      l[4][3] = fmx(l[4][3], sA[4][3]); SB();
      mx[4] = fmx(fmx(l[4][0], l[4][1]), l[4][2]); SB();
      mx[4] = fmx(mx[4], l[4][3]); SB();
      mx[5] = cpy(mx[4]); SB();
      pp[1][1] = pk2(ar[1][1][0], ar[1][1][1]); SB();
      pp[2][0] = pk2(ar[2][0][0], ar[2][0][1]); SB();
      { const auto r_ = __builtin_amdgcn_permlane32_swap(__float_as_uint(mx[4]), __float_as_uint(mx[5]), false, false); mx[4] = __uint_as_float(r_[0]); mx[5] = __uint_as_float(r_[1]); } SB();
      mx[4] = fmx(mx[4], mx[5]); SB();
      mx[5] = cpy(mx[4]); SB();
      pp[2][1] = pk2(ar[2][1][0], ar[2][1][1]); SB();
      pp[0][0] = pk2(ar[0][0][0], ar[0][0][1]); SB();
      { const auto r_ = __builtin_amdgcn_permlane16_swap(__float_as_uint(mx[4]), __float_as_uint(mx[5]), false, false); mx[4] = __uint_as_float(r_[0]); mx[5] = __uint_as_float(r_[1]); } SB();
      pp[0][1] = pk2(ar[0][1][0], ar[0][1][1]); SB();
      mx[4] = fmx(mx[4], mx[5]); SB();
      pf[1][0] = pp[1][0] & mA[0]; SB();
      sm[0] = mfma16h((u32x2){ones[0], ones[1]}, pp[0], zero4); SB();
      pf[1][2] = pp[1][0] & ~mA[0]; SB();
      pf[1][1] = pp[1][1] & mA[1]; SB();
      oA[0] = mfma16h((u32x2){vb[0], vb[1]}, pp[0], zero4); SB();
      pf[1][3] = pp[1][1] & ~mA[1]; SB();
      pf[2][0] = pp[2][0] & mA[0]; SB();
      oT[2] = mfma16h((u32x2){tb_[0], tb_[1]}, pp[0], zero4); SB();
      pf[2][2] = pp[2][0] & ~mA[0]; SB();
      pf[2][1] = pp[2][1] & mA[1]; SB();
      sm[1] = mfma16(ones, pf[1], zero4); SB();
      pf[2][3] = pp[2][1] & ~mA[1]; SB();
      mx[4] = mx[4] * -cs; SB();
      PIN(tb_); oT[0] = mfma16(__builtin_shufflevector(tb_, tb_, 0, 1, 2, 3), pf[1], zero4); SB();
      ar[3][0] = __builtin_elementwise_fma((f32x2){l[3][0], l[3][1]}, c2, (f32x2){mx[3], mx[3]}); SB();
      ar[3][1] = __builtin_elementwise_fma((f32x2){l[3][2], l[3][3]}, c2, (f32x2){mx[3], mx[3]}); SB();
      sm[2] = mfma16(ones, pf[2], zero4); SB();
      ar[4][0] = __builtin_elementwise_fma((f32x2){l[4][0], l[4][1]}, c2, (f32x2){mx[4], mx[4]}); SB();
      ar[4][1] = __builtin_elementwise_fma((f32x2){l[4][2], l[4][3]}, c2, (f32x2){mx[4], mx[4]}); SB();
      PIN(vb); oA[1] = mfma16(__builtin_shufflevector(vb, vb, 0, 1, 2, 3), pf[1], zero4); SB();
      ar[3][0][0] = __builtin_amdgcn_exp2f(ar[3][0][0]); SB();
      PIN(vb); oA[2] = mfma16(__builtin_shufflevector(vb, vb, 2, 3, 4, 5), pf[2], zero4); SB();
      ar[3][0][1] = __builtin_amdgcn_exp2f(ar[3][0][1]); SB();
      ar[3][1][0] = __builtin_amdgcn_exp2f(ar[3][1][0]); SB();
      ar[3][1][1] = __builtin_amdgcn_exp2f(ar[3][1][1]); SB();
      ar[4][0][0] = __builtin_amdgcn_exp2f(ar[4][0][0]); SB();
      ar[4][0][1] = __builtin_amdgcn_exp2f(ar[4][0][1]); SB();
      ar[4][1][0] = __builtin_amdgcn_exp2f(ar[4][1][0]); SB();
      ar[4][1][1] = __builtin_amdgcn_exp2f(ar[4][1][1]); SB();
      pp[3][0] = pk2(ar[3][0][0], ar[3][0][1]); SB();
      pp[3][1] = pk2(ar[3][1][0], ar[3][1][1]); SB();
      pp[4][0] = pk2(ar[4][0][0], ar[4][0][1]); SB();
      pp[4][1] = pk2(ar[4][1][0], ar[4][1][1]); SB();
      inv[0] = __builtin_amdgcn_rcpf(sm[0][0]); PIN(sm[0]); SB();
      inv[1] = __builtin_amdgcn_rcpf(sm[1][0]); PIN(sm[1]); SB();
      inv[2] = __builtin_amdgcn_rcpf(sm[2][0]); PIN(sm[2]); SB();
      pf[3][0] = pp[3][0] & mA[0]; SB();
      pf[3][2] = pp[3][0] & ~mA[0]; SB();
      pf[3][1] = pp[3][1] & mA[1]; SB();
      pf[3][3] = pp[3][1] & ~mA[1]; SB();
      pf[4][0] = pp[4][0] & mA[0]; SB();
      pf[4][2] = pp[4][0] & ~mA[0]; SB();
      sm[3] = mfma16(ones, pf[3], zero4); SB();
      pf[4][1] = pp[4][1] & mA[1]; SB();
      pf[4][3] = pp[4][1] & ~mA[1]; SB();
      tb_[4] = pk2(tx[2][0], tx[2][1]); SB();
      tb_[5] = pk2(tx[2][2], tx[2][3]); SB();
      sm[4] = mfma16(ones, pf[4], zero4); SB();
      nn0[0] = (f32x2){oA[0][0], oA[0][1]} * (f32x2){inv[0], inv[0]}; SB();
      nn0[1] = (f32x2){oA[0][2], oA[0][3]} * (f32x2){inv[0], inv[0]}; SB();
      PIN(tb_); oT[1] = mfma16(__builtin_shufflevector(tb_, tb_, 2, 3, 4, 5), pf[2], zero4); SB();
      ob[0][0] = pk2(nn0[0][0], nn0[0][1]); SB();
      ob[0][1] = pk2(nn0[1][0], nn0[1][1]); SB();
      nn1[0] = (f32x2){oA[1][0], oA[1][1]} * (f32x2){inv[1], inv[1]}; SB();
      nn1[1] = (f32x2){oA[1][2], oA[1][3]} * (f32x2){inv[1], inv[1]}; SB();
      nn2[0] = (f32x2){oA[2][0], oA[2][1]} * (f32x2){inv[2], inv[2]}; SB();
      nn2[1] = (f32x2){oA[2][2], oA[2][3]} * (f32x2){inv[2], inv[2]}; SB();
      vb[6] = pk2(acc[3][2][0], acc[3][2][1]); SB();
      vb[7] = pk2(acc[3][2][2], acc[3][2][3]); SB();
      vb[8] = pk2(acc[4][2][0], acc[4][2][1]); SB();
      vb[9] = pk2(acc[4][2][2], acc[4][2][3]); SB();
      PIN(vb); oA[3] = mfma16(__builtin_shufflevector(vb, vb, 4, 5, 6, 7), pf[3], zero4); SB();
      tb_[6] = pk2(tx[3][0], tx[3][1]); SB();
      tb_[7] = pk2(tx[3][2], tx[3][3]); SB();
      PIN(vb); oA[4] = mfma16(__builtin_shufflevector(vb, vb, 6, 7, 8, 9), pf[4], zero4); SB();
      tb_[8] = pk2(tx[4][0], tx[4][1]); SB();
      tb_[9] = pk2(tx[4][2], tx[4][3]); SB();
      PIN(tb_); oT[0] = mfma16(__builtin_shufflevector(tb_, tb_, 4, 5, 6, 7), pf[3], oT[0]); SB();
      ob[1][0] = pk2(nn1[0][0], nn1[0][1]); SB();
      ob[1][1] = pk2(nn1[1][0], nn1[1][1]); SB();
      PIN(tb_); oT[1] = mfma16(__builtin_shufflevector(tb_, tb_, 6, 7, 8, 9), pf[4], oT[1]); SB();
      ob[2][0] = pk2(nn2[0][0], nn2[0][1]); SB();
      ob[2][1] = pk2(nn2[1][0], nn2[1][1]); SB();
      ob[5][0] = cpyu(ob[0][0]); SB();
      ob[5][1] = cpyu(ob[0][1]); SB();
      nnU[0] = (f32x2){oT[2][0], oT[2][1]} * (f32x2){inv[0], inv[0]}; SB();
      nnU[1] = (f32x2){oT[2][2], oT[2][3]} * (f32x2){inv[0], inv[0]}; SB();
      obU[0] = pk2(nnU[0][0], nnU[0][1]); SB();
      obU[1] = pk2(nnU[1][0], nnU[1][1]); SB();
      { const auto r_ = __builtin_amdgcn_permlane32_swap(ob[0][0], ob[5][0], false, false); os[0][0] = r_[0]; os[0][2] = r_[1]; } SB();
      { const auto r_ = __builtin_amdgcn_permlane32_swap(ob[0][1], ob[5][1], false, false); os[0][1] = r_[0]; os[0][3] = r_[1]; } SB();
      { const auto r_ = __builtin_amdgcn_permlane32_swap(ob[1][0], ob[2][0], false, false); os[1][0] = r_[0]; os[1][2] = r_[1]; } SB();
      { const auto r_ = __builtin_amdgcn_permlane32_swap(ob[1][1], ob[2][1], false, false); os[1][1] = r_[0]; os[1][3] = r_[1]; } SB();
      inv[3] = __builtin_amdgcn_rcpf(sm[3][0]); PIN(sm[3]); SB();
      inv[4] = __builtin_amdgcn_rcpf(sm[4][0]); PIN(sm[4]); SB();
      it = selm(mo, inv[2], inv[1]); SB();
      iu = selm(mo, inv[4], inv[3]); SB();
      nn3[0] = (f32x2){oA[3][0], oA[3][1]} * (f32x2){inv[3], inv[3]}; SB();
      nn3[1] = (f32x2){oA[3][2], oA[3][3]} * (f32x2){inv[3], inv[3]}; SB();
      nn4[0] = (f32x2){oA[4][0], oA[4][1]} * (f32x2){inv[4], inv[4]}; SB();
      nn4[1] = (f32x2){oA[4][2], oA[4][3]} * (f32x2){inv[4], inv[4]}; SB();
      it = selm(mh, iu, it); SB();
      oT[0][0] = selm(mo, oT[1][0], oT[0][0]); SB();
      oT[0][1] = selm(mo, oT[1][1], oT[0][1]); SB();
      oT[0][2] = selm(mo, oT[1][2], oT[0][2]); SB();
      oT[0][3] = selm(mo, oT[1][3], oT[0][3]); SB();
      ob[3][0] = pk2(nn3[0][0], nn3[0][1]); SB();
      ob[3][1] = pk2(nn3[1][0], nn3[1][1]); SB();
      ob[4][0] = pk2(nn4[0][0], nn4[0][1]); SB();
      ob[4][1] = pk2(nn4[1][0], nn4[1][1]); SB();
      nnT[0] = (f32x2){oT[0][0], oT[0][1]} * (f32x2){it, it}; SB();
      nnT[1] = (f32x2){oT[0][2], oT[0][3]} * (f32x2){it, it}; SB();
      obT[0] = pk2(nnT[0][0], nnT[0][1]); SB();
      obT[1] = pk2(nnT[1][0], nnT[1][1]); SB();
      { const auto r_ = __builtin_amdgcn_permlane32_swap(ob[3][0], ob[4][0], false, false); os[2][0] = r_[0]; os[2][2] = r_[1]; } SB();
      { const auto r_ = __builtin_amdgcn_permlane32_swap(ob[3][1], ob[4][1], false, false); os[2][1] = r_[0]; os[2][3] = r_[1]; } SB();
      XBAR(); SB();
      __builtin_amdgcn_raw_buffer_store_b128(os[1], ro, od[1], 0, 16); SB();
      __builtin_amdgcn_raw_buffer_store_b128(os[2], ro, od[2], 0, 16); SB();
      __builtin_amdgcn_raw_buffer_store_b64(obT, ro, od[3], 0, 16); SB();
      __builtin_amdgcn_raw_buffer_store_b128(os[0], ro, od[0], 0, 16); SB();
      __builtin_amdgcn_raw_buffer_store_b64(obU, ro, od[4], 0, 16); SB();
    }
  } else {
    const int f = b - 224;
    args();
    float* red = (float*)smem;
    u16* hs = (u16*)(smem + 32768);
    const bool do1 = f < 27;
    const bool lead = f == 31;
    if (__builtin_expect(lead, 0)) {
      get_tag();
      const __amdgpu_buffer_rsrc_t rk = __builtin_amdgcn_make_buffer_rsrc((void*)(ws + WS_K), 0, 36864, 0x00020000);
      u32x2 kv[2];
      int ko[2];
      ko[0] = (tid >> 2) * 128 + (tid & 3) * 16;
      ko[1] = tid < 384 ? (128 + (tid >> 2)) * 128 + (tid & 3) * 16 : (224 + ((tid - 384) >> 1)) * 128 + (tid & 1) * 16;
      kv[0] = kv[1] = (u32x2){0u, 0u};
#pragma unroll 1
      for (unsigned spins = 0; spins < SPIN_CAP; ++spins) {
        asm volatile("" ::: "memory");
        u32x4 t_[2];
#pragma unroll
        for (int q = 0; q < 2; ++q) t_[q] = __builtin_amdgcn_raw_buffer_load_b128(rk, ko[q], 0, 16);
        bool ok = true;
#pragma unroll
        for (int q = 0; q < 2; ++q)
          if (ko[q] != 0x7FFFFF00) {
            if ((t_[q][1] == tag) & (t_[q][3] == tag)) {
              kv[q] = (u32x2){t_[q][0], t_[q][2]};
              ko[q] = 0x7FFFFF00;
            } else {
              ok = false;
            }
          }
        if (__all(ok)) break;
        __builtin_amdgcn_s_sleep(4);
      }
      unsigned k1 = max(kv[0][0], kv[0][1]), k2 = min(kv[0][0], kv[0][1]);
      top2_mrg(k1, k2, max(kv[1][0], kv[1][1]), min(kv[1][0], kv[1][1]));
      top2_all(k1, k2);
      unsigned* lk = (unsigned*)(smem + L_VAR + 64);
      if (lane == 0) *(u32x2*)(lk + wave * 2) = (u32x2){k1, k2};
      WAITL0();
      BARRIER();
      k1 = 0u;
      k2 = 0u;
#pragma unroll
      for (int q = 0; q < 4; ++q) {
        const u32x4 a_ = *(const u32x4*)(lk + q * 4);
        top2_mrg(k1, k2, a_[0], a_[1]);
        top2_mrg(k1, k2, a_[2], a_[3]);
      }
      if (tid == 0) {
        const __amdgpu_buffer_rsrc_t rr = __builtin_amdgcn_make_buffer_rsrc((void*)(ws + WS_R), 0, 128, 0x00020000);
        __builtin_amdgcn_raw_buffer_store_b128((u32x4){k1, tag, k2, tag}, rr, 0, 0, 16);
        *(u32x2*)(smem + L_VAR + 32) = (u32x2){k1, k2};
      }
    }
    if (__builtin_expect(f == 30, 0)) {
      get_tag();
      if (wave < 4) {
        const __amdgpu_buffer_rsrc_t rf = __builtin_amdgcn_make_buffer_rsrc((void*)(ws + WS_F2), 0, 32768, 0x00020000);
        int fo = tid < 227 ? tid * 128 : 0x7FFFFF00;
#pragma unroll 1
        for (unsigned spins = 0; spins < SPIN_CAP; ++spins) {
          asm volatile("" ::: "memory");
          if (__builtin_amdgcn_raw_buffer_load_b32(rf, fo, 0, 16) == tag) fo = 0x7FFFFF00;
          if (__all(fo == 0x7FFFFF00)) break;
          __builtin_amdgcn_s_sleep(4);
        }
      }
      BARRIER();
      if (tid == 0) {
        const __amdgpu_buffer_rsrc_t rr = __builtin_amdgcn_make_buffer_rsrc((void*)(ws + WS_R), 0, 256, 0x00020000);
        const unsigned t_ = now();
        __builtin_amdgcn_raw_buffer_store_b128((u32x4){t_, tag, t_, tag}, rr, 128, 0, 16);
        *(u32x4*)(smem + L_CAND2) = (u32x4){t_, tag, t_, tag};
      }
    }
    if (__builtin_expect(f >= 27 && f < 30, 0)) {
      get_tag();
#pragma unroll 1
      for (int c = wave * 3 + f - 27; c < 320; c += 24)
        __builtin_amdgcn_raw_buffer_store_b128((u32x4){SENT, SENT, SENT, SENT}, ro, lane * 16, (c >= 160 ? 112 * 20480 : 56 * 20480) + c * 1024, 16);
      WAITV(0);
      BARRIER();
      if (tid == 0) __hip_atomic_store((gu32*)(ws + WS_F2 + (197 + f) * 128), tag, RLX_AGENT);
    }
    const int g0 = wave < 4 ? wave * 3 : 4 + wave * 2, ng = wave < 4 ? 3 : 2;
    u32x4 r1[3][8], wd[2][8];
    int vo2 = (wave - 3) * 256 + c15 * 16 + g4 * 8 * (DM * 2);
    PIN(vo2);
    if (do1) {
      const char* pa = (const char*)(c15 < 8 ? w_gate : w_up) + f * 128 + (c15 & 7) * 16 + (g0 * 32 + g4 * 8) * (FF * 2);
      const GAS char* pp[12];
#pragma unroll
      for (int j = 0; j < 12; ++j) {
        pp[j] = (const GAS char*)pa + ((j >> 2) * 32 + (j & 3) * 2) * (FF * 2) + FF;
        PIN(pp[j]);
      }
      SB();
#pragma unroll
      for (int j = 0; j < 24; ++j)
        if (j < 16 || wave < 4) r1[j >> 3][j & 7] = __builtin_nontemporal_load((const GAS u32x4*)(pp[j >> 1] + ((j & 1) ? FF : -FF)));
    }
    SB();
    BARRIER();
    if (do1 && wave >= 3) {
      const u32x4 r = make_srd(w_down, DM * FF * 2);
#pragma unroll
      for (int j = 0; j < 16; ++j)
        asm volatile("buffer_load_dwordx4 %0, %1, %2, %3 offen nt" : "=v"(wd[j >> 3][j & 7]) : "v"(vo2), "s"(r), "s"((f * 64 + (j >> 3) * 32 + (j & 7)) * (DM * 2)) : "memory");
    }
    SB();
    if (f < 30) get_tag();
    prepB();
    auto transp = [&](u32x4(&w)[8]) {
      u32x4 t[8];
#pragma unroll
      for (int d = 0; d < 4; ++d)
#pragma unroll
        for (int p = 0; p < 4; ++p) {
          t[2 * d][p] = perm_lo(w[2 * p + 1][d], w[2 * p][d]);
          t[2 * d + 1][p] = perm_hi(w[2 * p + 1][d], w[2 * p][d]);
        }
#pragma unroll
      for (int j = 0; j < 8; ++j) {
        w[j] = t[j];
        PIN(w[j]);
      }
    };
    if (do1) {
      transp(r1[0]);
      transp(r1[1]);
      if (wave < 4) transp(r1[2]);
    }
    __builtin_amdgcn_s_waitcnt(0x0F70);
    if (wave == 0 && !lead) {
      res_wait(ws, smem, lane, tag, 0);
      if (do1) {
        WAITL0();
        const u32x2 kq = *(const u32x2*)(smem + L_VAR + 32);
#pragma unroll
        for (int t = 0; t < 2; ++t) {
          const u16* xr = x + (2047 - (int)(__builtin_amdgcn_readfirstlane(kq[t]) & 2047u)) * DM;
          dma16(lane * 16, xr, lds0 + 36864 + t * 1296);
          if (lane < 16) dma16(lane * 16, xr + 512, lds0 + 36864 + t * 1296 + 1024);
        }
      }
    }
    if (wave == 1) touch_line((const char*)out + tile_off((lane & 31) + (nt2 & 1) * 32) + ((nt2 & 1) ? 0 : 128), lds0 + L_TOUCH + 512);
    if (wave == 1 && f != 30 && !do1) res_wait(ws, smem, lane, tag, 1);
    if (wave < 2) WAITV(0);
    WAITL0();
    BARRIER();
    if (tid == 128) __hip_atomic_store((gu32*)(ws + WS_A + f * 128), tag, RLX_AGENT);
    const __amdgpu_buffer_rsrc_t ryp = __builtin_amdgcn_make_buffer_rsrc((void*)(ws + WS_YP), 0, 27 * 10240, 0x00020000);
    auto pick = [&](const f32x4(&a)[8]) {
      f32x4 v;
#pragma unroll
      for (int q = 0; q < 4; ++q) {
        const float lo = (g4 & 1) ? a[2 + (q >> 1)][q & 1] : a[q >> 1][q & 1], hi = (g4 & 1) ? a[6 + (q >> 1)][q & 1] : a[4 + (q >> 1)][q & 1];
        v[q] = (g4 & 2) ? hi : lo;
      }
      return v;
    };
    if (do1) {
      u32x4 xf[3];
#pragma unroll
      for (int k = 0; k < 3; ++k) xf[k] = *(const u32x4*)(smem + 36864 + (c15 & 1) * 1296 + ((g0 + (k < ng ? k : 0)) * 32 + g4 * 8) * 2);
      f32x4 acc[8];
#pragma unroll
      for (int ff = 0; ff < 8; ++ff) acc[ff] = (f32x4){0.f, 0.f, 0.f, 0.f};
#pragma unroll
      for (int k = 0; k < 3; ++k)
        if (k < ng) {
#pragma unroll
          for (int j = 0; j < 8; ++j) acc[j] = mfma16(xf[k], r1[k][j], acc[j]);
        }
      *(f32x4*)(red + wave * 256 + c15 * 16 + g4 * 4) = pick(acc);
      WAITL0();
      BARRIER();
      u32x4 wt[2][8];
      if (wave < 2) {
        float gs = 0.f, us = 0.f;
#pragma unroll
        for (int w = 0; w < 8; ++w) {
          gs += red[w * 256 + tid];
          us += red[w * 256 + 128 + tid];
        }
        const float gv = rbf(gs), uv = rbf(us);
        const float hv = rbf(gv * __builtin_amdgcn_rcpf(1.0f + __expf(-gv))) * uv;
        hs[(tid & 1) * 64 + (tid >> 1)] = (u16)pk2(hv, hv);
      } else if (wave >= 3) {
        WAITV(0);
#pragma unroll
        for (int j = 0; j < 16; ++j) {
          wt[j >> 3][j & 7] = wd[j >> 3][j & 7];
          PIN(wt[j >> 3][j & 7]);
        }
        transp(wt[0]);
        transp(wt[1]);
      }
      WAITL0();
      BARRIER();
      if (wave == 2) res_wait(ws, smem, lane, tag, 1);
      if (wave >= 3) {
        const u32x4 h0 = *(const u32x4*)(hs + (c15 & 1) * 64 + g4 * 8), h1 = *(const u32x4*)(hs + (c15 & 1) * 64 + 32 + g4 * 8);
#pragma unroll
        for (int j = 0; j < 8; ++j) acc[j] = mfma16(h0, wt[0][j], (f32x4){0.f, 0.f, 0.f, 0.f});
#pragma unroll
        for (int j = 0; j < 8; ++j) acc[j] = mfma16(h1, wt[1][j], acc[j]);
        *(f32x4*)(red + 2048 + wave * 256 + c15 * 16 + g4 * 4) = pick(acc);
        const f32x4 y = *(const f32x4*)(red + 2048 + wave * 256 + lane * 4);
#pragma unroll
        for (int t = 0; t < 2; ++t)
          __builtin_amdgcn_raw_buffer_store_b128((u32x4){__float_as_uint(y[t]), tag, __float_as_uint(y[2 + t]), tag}, ryp,
                                                 ((f * 2 + t) * 640 + (wave - 3) * 128 + lane * 2) * 8, 0, 16);
      }
      WAITL0();
      BARRIER();
    }
    wo_dma(IC<3>{}, wave, 8, 25);
    if (wave == 0) wo_dma(IC<1>{}, 24, 8, 25);
    if (wave == 1) gate_dma(gate_off(lane));
    if (tid < 8) *(unsigned*)(smem + L_VAR + tid * 4) = 0u;
    if (wave == 2 || (f == 30 && wave == 3)) {
      const unsigned tp = __builtin_amdgcn_readfirstlane(*(const unsigned*)(smem + L_CAND2)) + (wave == 2 ? D1 : DK);
#pragma unroll 1
      for (unsigned spins = 0; spins < 128 && (int)(now() - tp) < 0; ++spins) __builtin_amdgcn_s_sleep(2);
    }
    if (f == 30 && wave == 3) {
      const __amdgpu_buffer_rsrc_t ra = __builtin_amdgcn_make_buffer_rsrc((void*)(ws + WS_A), 0, 4096, 0x00020000);
#pragma unroll 1
      for (unsigned spins = 0; spins < SPIN_CAP; ++spins) {
        asm volatile("" ::: "memory");
        if (__all(__builtin_amdgcn_raw_buffer_load_b32(ra, (lane & 31) * 128, 0, 16) == tag)) break;
        __builtin_amdgcn_s_sleep(8);
      }
      if (lane == 0) __hip_atomic_store((gu32*)(ws + WS_E), tag, RLX_AGENT);
    }
    if (wave == 2) {
      const unsigned t_ = now() + D2;
      const u32x4 v = __builtin_amdgcn_raw_buffer_load_b128(rfl, flag_off(), 0, 16);
      *(u32x4*)(smem + L_FS + lane * 16) = (u32x4){(v[1] == tag ? v[0] : t_) + (ETA_F - ETA_G), tag, 0u, tag};
    }
    if (wave == 0) rows_job();
    WAITV(0);
    WAITL0();
    BARRIER();
  }

  {
    const int m = wave & 3, kh = wave >> 2;
    const bool snd = kh != 0;
    const int oo = (rt2 * 4 + m) * 20480 + kh * 8192, ot = (rt2 * 4 + m) * 20480 + 16384 + kh * 2048;
    const int l16 = lane * 16;
    u32x4 fs = *(const u32x4*)(smem + L_FS + l16);
    const u32x4 kk = *(const u32x4*)(smem + L_VAR + 32);
    u32x2 g0_ = *(const u32x2*)(smem + glo);
    u32x4 yw[2][7];
    int yo, y6, yp, yx;
    auto y_prep = [&]() {
      int ly = lane;
      PIN(ly);
      const int cp = (ly & 15) < 10 ? ly & 15 : 0, col = m * 20 + cp * 2 + (ly >> 5);
      yo = (ly >> 4) * 71680 + (nt2 * 80 + m * 20 + cp * 2) * 8;
      y6 = ly >= 48 ? 0x7FFFFF00 : yo;
      yp = (((col >> 4) + 5 - (nt2 & 1)) % 5) * 1024 + (col & 12) * 64 + (col & 3) * 4;
      yx = L_XT + col * 2;
      PIN(yo); PIN(y6); PIN(yp); PIN(yx);
    };
    auto y_srd = [&](int tok) {
      char* wy = ws;
      asm volatile("" : "+s"(wy));
      return make_srd(wy + WS_YP + tok * 5120, 27 * 10240);
    };
    auto y_issue = [&](u32x4(&yv)[7], u32x4 srd, bool again, int j0 = 0, int j1 = 7) {
#pragma unroll
      for (int j = 0; j < 7; ++j) {
        if (j < j0 || j >= j1) continue;
        if (again) asm volatile("buffer_load_dwordx4 %0, %1, %2, %3 offen sc1" : "+v"(yv[j]) : "v"(j < 6 ? yo : y6), "s"(srd), "s"(j * 10240) : "memory");
        else asm volatile("buffer_load_dwordx4 %0, %1, %2, %3 offen sc1" : "=v"(yv[j]) : "v"(j < 6 ? yo : y6), "s"(srd), "s"(j * 10240) : "memory");
      }
    };
    auto y_done = [&](u32x4(&yv)[7], int tok, int row) {
      volatile LAS unsigned* fl = (volatile LAS unsigned*)(LAS char*)(smem + L_VAR + 16 + (row >> 4) * 4);
      volatile LAS float* pe = (volatile LAS float*)(LAS char*)(smem + yp + (row >> 4) * 5120 + (row & 15) * 16);
      unsigned f_, xv_;
      float omg, pv;
      auto rd = [&]() {
        f_ = *fl;
        omg = *(volatile LAS float*)(LAS char*)(smem + L_JUNK + row * 4);
        xv_ = *(volatile LAS u16*)(LAS char*)(smem + yx + row * 160);
        pv = *pe;
      };
      rd();
      const float rsel = *(const float*)(smem + L_JUNK + 256 + tok * 4);
      SB();
      unsigned lo = tag, hi = tag;
      float y[2] = {0.f, 0.f};
#pragma unroll
      for (int j = 0; j < 7; ++j) {
        PIN(yv[j]);
        if (j == 6 && g4 == 3) yv[j][1] = yv[j][3] = tag;
        lo = min(min(lo, yv[j][1]), yv[j][3]);
        hi = max(max(hi, yv[j][1]), yv[j][3]);
        y[0] += __uint_as_float(yv[j][0]);
        PIN(y[0]);
        y[1] += __uint_as_float(yv[j][2]);
        PIN(y[1]);
      }
      if (!__all(lo == hi)) return false;
      const auto a = __builtin_amdgcn_permlane32_swap(__float_as_uint(y[0]), __float_as_uint(y[1]), false, false);
      const float h = __uint_as_float(a[0]) + __uint_as_float(a[1]);
      const auto c = __builtin_amdgcn_permlane16_swap(__float_as_uint(h), __float_as_uint(h), false, false);
      const float r = rbf(rbf(__uint_as_float(c[0]) + __uint_as_float(c[1])) * rsel);
#pragma unroll 1
      for (unsigned spins = 0; spins < (SPIN_CAP << 6) && !__builtin_amdgcn_readfirstlane(f_); ++spins) rd();
      const float xv = bflo(xv_);
      *pe = pv + (rbf(xv + r) - xv) * omg;
      asm volatile("ds_add_u32 %0, %1" ::"v"(fl), "v"(1u) : "memory");
      return true;
    };
    const int l0 = __builtin_amdgcn_readfirstlane(kk[2]), l1 = __builtin_amdgcn_readfirstlane(kk[3]);
    unsigned ry = min((unsigned)l0, (unsigned)l1);
    unsigned need = 1u + ((l0 >> 4) == m ? 256u : 0u) + ((l1 >> 4) == m ? 256u : 0u);
    asm volatile("" : "+s"(ry), "+s"(need));
#define yjob (ry < 64u)
    if (wave == 7) {
      if (__builtin_expect((~__builtin_amdgcn_ballot_w64((fs[1] == tag) & (fs[3] == tag)) & 0xFFull) != 0, 0)) {
        flag_wait(lane >= 8 ? 0x7FFFFF00 : flag_off());
        WAITL0();
        if (lane == 0) *(volatile LAS unsigned*)(LAS char*)(smem + L_VAR + 8) = 1u;
      }
    }
    if (!snd) {
      dma16(adr[0], x, lds0 + L_XT + m * 2560);
      dma16(adr[1], x, lds0 + L_XT + m * 2560 + 1024);
      if (lane < 32) dma16(adr[2], x, lds0 + L_XT + m * 2560 + 2048);
    }
    SB();
    const char* wb = smem + L_WO + kh * 51200 + l16;
    u32x4 wq[WPD][5];
    auto wrd = [&](int k) {
#pragma unroll
      for (int f = 0; f < 5; ++f) wq[k % WPD][f] = *(const u32x4*)(wb + (k * 5 + f) * 1024);
    };
#pragma unroll
    for (int k = 0; k < NPRE; ++k) wrd(k);
    if (snd) {
#pragma unroll
      for (int k = NPRE; k < WPD; ++k) wrd(k);
    }
    if (__builtin_expect(!__all(g0_[1] == tag), 0)) {
      const __amdgpu_buffer_rsrc_t rg_ = __builtin_amdgcn_make_buffer_rsrc((void*)(ws + WS_K), 0, 36864, 0x00020000);
#pragma unroll 1
      for (unsigned spins = 0; spins < SPIN_CAP; ++spins) {
        asm volatile("" ::: "memory");
        g0_ = __builtin_amdgcn_raw_buffer_load_b64(rg_, gate_off(m * 16 + c15), 0, 16);
        if (__all(g0_[1] == tag)) break;
      }
    }
    const float gt = __uint_as_float(g0_[0]);
    SB();
    {
      const u64 inv = ~__builtin_amdgcn_ballot_w64((fs[1] == tag) & (fs[3] == tag));
      if (__builtin_expect((inv & rel) != 0, 0)) {
#pragma unroll 1
        for (unsigned spins = 0; spins < (SPIN_CAP << 6); ++spins) {
          if (__builtin_amdgcn_readfirstlane(*(volatile LAS unsigned*)(LAS char*)(smem + L_VAR + 8))) break;
          __builtin_amdgcn_s_sleep(1);
        }
        fs[0] = *(volatile LAS unsigned*)(LAS char*)(smem + L_FS + l16);
      }
      const unsigned tg = fs[0] + ETA_G;
      unsigned spins = 128;
#pragma unroll 1
      while ((__builtin_amdgcn_ballot_w64((int)(now() - tg) < 0) & rel) != 0 && --spins) {}
    }
    u32x4 of[8];
    u32x2 ofl[2], ofh[2];
    const u32x4 sro = make_srd(ws + WS_OF, S_LEN * DM * 2);
    int l8 = lane * 8;
    asm volatile("buffer_load_dwordx4 %0, %8, %9, %10 offen sc1\n\tbuffer_load_dwordx4 %1, %8, %9, %10 offen offset:1024 sc1\n\t"
                 "buffer_load_dwordx4 %2, %8, %9, %10 offen offset:2048 sc1\n\tbuffer_load_dwordx4 %3, %8, %9, %10 offen offset:3072 sc1\n\t"
                 "buffer_load_dwordx4 %4, %8, %9, %11 offen sc1\n\tbuffer_load_dwordx4 %5, %8, %9, %11 offen offset:1024 sc1\n\t"
                 "buffer_load_dwordx4 %6, %8, %9, %11 offen offset:2048 sc1\n\tbuffer_load_dwordx4 %7, %8, %9, %11 offen offset:3072 sc1"
                 : "=&v"(of[0]), "=&v"(of[1]), "=&v"(of[2]), "=&v"(of[3]), "=&v"(of[4]), "=&v"(of[5]), "=&v"(of[6]), "=&v"(of[7])
                 : "v"(l16), "s"(sro), "s"(oo), "s"(oo + 4096)
                 : "memory");
    asm volatile("buffer_load_dwordx2 %0, %4, %5, %6 offen sc1\n\tbuffer_load_dwordx2 %1, %4, %5, %6 offen offset:512 sc1\n\t"
                 "buffer_load_dwordx2 %2, %4, %5, %6 offen offset:1024 sc1\n\tbuffer_load_dwordx2 %3, %4, %5, %6 offen offset:1536 sc1"
                 : "=&v"(ofl[0]), "=&v"(ofh[0]), "=&v"(ofl[1]), "=&v"(ofh[1])
                 : "v"(l8), "s"(sro), "s"(ot)
                 : "memory");
    if (!snd) {
#pragma unroll
      for (int k = NPRE; k < WPD; ++k) wrd(k);
    }
    auto o_again = [&](int k) {
      if (k < 8)
        asm volatile("buffer_load_dwordx4 %0, %1, %2, %3 offen sc1" : "+v"(of[k]) : "v"(l16), "s"(sro), "s"(oo + k * 1024) : "memory");
      else
        asm volatile("buffer_load_dwordx2 %0, %2, %3, %4 offen sc1\n\tbuffer_load_dwordx2 %1, %2, %3, %4 offen offset:512 sc1"
                     : "+v"(ofl[k - 8]), "+v"(ofh[k - 8])
                     : "v"(l8), "s"(sro), "s"(ot + (k - 8) * 1024)
                     : "memory");
    };
    auto o_pin = [&](int k) {
      if (k < 8) PIN(of[k]);
      else asm volatile("" : "+v"(ofl[k - 8]), "+v"(ofh[k - 8]));
    };
    auto o_get = [&](int k) { return k < 8 ? of[k] : (u32x4){ofl[k - 8][0], ofl[k - 8][1], ofh[k - 8][0], ofh[k - 8][1]}; };
    auto o_bad = [&](int k) {
      const u32x4 v = o_get(k);
      return __any(max(max(v[0], v[1]), max(v[2], v[3])) == SENT);
    };
    int pa_ = m * 5120 + l16;
    PIN(pa_);
    volatile LAS unsigned* pfl = (volatile LAS unsigned*)(LAS char*)(smem + L_VAR + 16 + m * 4);
    u32x4 rout = make_srd(out, S_LEN * DM * 2);
    asm volatile("" : "+s"(rout));
    f32x4 ac2[5], pp[5];
    unsigned pf_ = 0;
    if (!snd) {
      u32x2 xb[5];
      if (__builtin_expect(yjob, 0)) y_prep();
      WAITV(12);
#pragma unroll
      for (int f = 0; f < 5; ++f) xb[f] = *(const u32x2*)(smem + L_XT + m * 2560 + c15 * 160 + g4 * 8 + ((nt2 & 1) ? (f + 1) % 5 : f) * 32);
      const float omg = 1.0f - gt;
      *(float*)(smem + L_JUNK + (m * 16 + c15) * 4) = omg;
#pragma unroll
      for (int f = 0; f < 5; ++f) {
        pp[f] = (f32x4){bflo(xb[f][0]), bfhi(xb[f][0]), bflo(xb[f][1]), bfhi(xb[f][1])} * omg;
        PIN(pp[f]);
      }
    }
#pragma unroll
    for (int f = 0; f < 5; ++f) ac2[f] = (f32x4){0.f, 0.f, 0.f, 0.f};
    __builtin_amdgcn_sched_barrier(0);
    constexpr int GS[5] = {0, 2, 5, 8, 10};
    auto vleft = [](int k) { return k < 8 ? 11 - k : k == 8 ? 2 : 0; };
    WAITV(10);
    o_pin(0);
    o_pin(1);
    bool bad = o_bad(0) | o_bad(1);
#pragma unroll
    for (int g = 0; g < 4; ++g) {
      if (__builtin_expect(bad, 0)) {
        WAITV(0);
#pragma unroll 1
        for (unsigned spins = 0; spins < SPIN_CAP; ++spins) {
          bool any = false;
#pragma unroll
          for (int k2 = GS[g]; k2 < 10; ++k2) {
            o_pin(k2);
            if (o_bad(k2)) {
              o_again(k2);
              if (k2 < GS[g + 1]) any = true;
            }
          }
          WAITV(0);
          if (!any) break;
        }
      }
      if (g == 3) break;
      unsigned mxv = 0;
      const int nm = (GS[g + 1] - GS[g]) * 5, nn = g < 3 ? GS[g + 2] - GS[g + 1] : 0;
#pragma unroll
      for (int i = 0; i < nm; ++i) {
        const int k = GS[g] + i / 5, f = i % 5;
        const int j = i - (nm - 2 - 2 * nn);
        const int kn = GS[g + 1] + j / 2;
        if (j >= 0 && j < 2 * nn && !(j & 1)) {
          WAITV_(vleft(kn));
          o_pin(kn);
          SB();
        }
        ac2[f] = mfma16(wq[k % WPD][f], o_get(k), ac2[f]);
        SB();
        if (k + WPD < 10) wq[k % WPD][f] = *(const u32x4*)(wb + ((k + WPD) * 5 + f) * 1024);
        if (j >= 0 && j < 2 * nn) {
          const u32x4 nv = o_get(kn);
          mxv = max(max(mxv, nv[(j & 1) * 2]), nv[(j & 1) * 2 + 1]);
        }
        if (nn && j == 2 * nn) bad = __any(mxv == SENT);
        if (g == 2 && i == PRD - 1) __builtin_amdgcn_s_waitcnt(0xC07F);
        if (g == 2 && i == PRD && snd) {
          pf_ = *pfl;
          asm volatile("" ::: "memory");
#pragma unroll
          for (int f = 0; f < 5; ++f) pp[f] = *(const f32x4*)(smem + pa_ + f * 1024);
        }
        SB();
      }
    }
    u32x4 o8 = o_get(8), o9 = o_get(9);
    PIN(o8); PIN(o9);
    auto mm = [&](int f) {
      ac2[f] = mfma16(wq[8 % WPD][f], o8, ac2[f]);
      SB();
      ac2[f] = mfma16(wq[9 % WPD][f], o9, ac2[f]);
      SB();
    };
    auto fin = [&](int f, int q) {
      pp[f][q] = __builtin_fmaf(ac2[f][q], gt, pp[f][q]);
      PIN(pp[f][q]);
    };
    if (!snd) {
      SB();
      auto wr = [&](int f) {
#pragma unroll
        for (int q = 0; q < 4; ++q) fin(f, q);
        *(f32x4*)(smem + pa_ + f * 1024) = pp[f];
        SB();
      };
      auto done = [&]() {
        asm volatile("" ::: "memory");
        *pfl = 1u;
      };
      if (__builtin_expect(yjob, 0)) {
        int tok = (unsigned)l0 >= 64u;
        const u32x4 sy = y_srd(tok);
        mm(0); y_issue(yw[0], sy, false, 0, 2); SB(); mm(1); y_issue(yw[0], sy, false, 2, 4); SB(); mm(2); y_issue(yw[0], sy, false, 4, 7); SB();
        if (max((unsigned)l0, (unsigned)l1) < 64u) y_issue(yw[1], y_srd(1), false);
        wr(0); mm(3); wr(1); mm(4); wr(2); wr(3); wr(4);
        done();
        WAITV(0);
#pragma unroll
        for (int j = 0; j < 7; ++j) PIN(yw[1][j]);
#pragma unroll 1
        for (unsigned spins = 0; spins < SPIN_CAP; ++spins) {
          if (y_done(yw[0], tok, tok ? l1 : l0)) {
            if (tok || (unsigned)l1 >= 64u) break;
            tok = 1;
#pragma unroll
            for (int j = 0; j < 7; ++j) yw[0][j] = yw[1][j];
          } else {
            y_issue(yw[0], y_srd(tok), true);
            WAITV(0);
          }
        }
      } else {
        mm(0); mm(1); mm(2);
        wr(0); mm(3); wr(1); mm(4);
        wr(2); wr(3); wr(4);
        done();
      }
    } else {
      SB();
      u32x2 r[5];
      auto cv = [&](int f) {
#pragma unroll
        for (int q = 0; q < 4; ++q) fin(f, q);
        r[f] = (u32x2){pk2(pp[f][0], pp[f][1]), pk2(pp[f][2], pp[f][3])};
        SB();
      };
      auto st = [&](int f) {
        const auto a = __builtin_amdgcn_permlane16_swap(r[f][0], r[f + 1][0], false, false);
        const auto c = __builtin_amdgcn_permlane16_swap(r[f][1], r[f + 1][1], false, false);
        asm volatile("buffer_store_dwordx4 %0, %1, %2, 0 offen offset:%3 sc1\n\ts_nop 1" ::"v"((u32x4){a[0], c[0], a[1], c[1]}), "v"(adr[0]), "s"(rout), "i"(f * 32) : "memory");
        SB();
      };
      if (__builtin_expect(__builtin_amdgcn_readfirstlane(pf_) != need, 0)) {
        mm(0); mm(1); mm(2); mm(3); mm(4);
#pragma unroll 1
        for (unsigned spins = 0; spins < (SPIN_CAP << 6); ++spins) {
          pf_ = *pfl;
          asm volatile("" ::: "memory");
#pragma unroll
          for (int f = 0; f < 5; ++f) pp[f] = *(const f32x4*)(smem + pa_ + f * 1024);
          asm volatile("" ::: "memory");
          if (__builtin_amdgcn_readfirstlane(pf_) == need) break;
        }
        cv(0); cv(1);
      } else {
        mm(0); mm(1); mm(2);
        cv(0); mm(3); cv(1); mm(4);
      }
      st(0); cv(2); cv(3); st(2); cv(4);
      asm volatile("buffer_store_dwordx2 %0, %1, %2, 0 offen sc1" ::"v"(r[4]), "v"(adr[1]), "s"(rout) : "memory");
    }
  }
}

extern "C" void kernel_launch(void* const* d_in, const int* in_sizes, int n_in, void* d_out, int out_size, void* d_ws,
                              size_t ws_size, hipStream_t stream) {
  kf<<<256, 512, 0, stream>>>((const u16*)d_in[0], (const u16*)d_in[1], (const u16*)d_in[2], (const u16*)d_in[4],
                              (const u16*)d_in[8], (const u16*)d_in[5], (const u16*)d_in[6], (const u16*)d_in[7],
                              (char*)d_ws, (u16*)d_out);
}
```

```cpp
#include <hip/hip_runtime.h>
#include <stdint.h>

typedef unsigned short u16;
typedef unsigned long long u64;
typedef __attribute__((ext_vector_type(8))) __bf16 bf16x8;
typedef __attribute__((ext_vector_type(2))) __bf16 bf16x2;
typedef __attribute__((ext_vector_type(4))) short s16x4;
typedef __attribute__((ext_vector_type(4))) float f32x4;
typedef __attribute__((ext_vector_type(2))) float f32x2;
typedef __attribute__((ext_vector_type(4))) unsigned u32x4;
typedef __attribute__((ext_vector_type(2))) unsigned u32x2;
typedef __attribute__((ext_vector_type(10))) unsigned u32x10;
#define GAS __attribute__((address_space(1)))
#define LAS __attribute__((address_space(3)))
typedef GAS unsigned gu32;

#define S_LEN 2048
#define DM 640
#define NQKV 1920
#define FF 1728

#define WS_OF 0
#define WS_WOF 2621440
#define WS_E (WS_WOF + 819200)
#define WS_F2 (WS_E + 256)
#define WS_F (WS_F2 + 32768)
#define WS_K (WS_F + 28672)
#define WS_R (WS_K + 36864)
#define WS_A (WS_R + 256)
#define WS_YP (WS_A + 4096)
#define TAG_STEP 0x9E3779B9u
#define SENT 0xFFFFFFFFu
#define SPIN_CAP (1u << 18)
#define NPRE 3
#define WPD 7
#define PRD 10
#define WB8 2
#define TCY 2750
#define ETA_G 15
#define ETA_F 20
#define D1 6700
#define D2 75
#define DK 4200
#define RLX_AGENT __ATOMIC_RELAXED, __HIP_MEMORY_SCOPE_AGENT

__device__ __forceinline__ float bflo(unsigned u) { return __uint_as_float(u << 16); }
__device__ __forceinline__ float bfhi(unsigned u) { return __uint_as_float(u & 0xFFFF0000u); }
__device__ __forceinline__ unsigned pk2(float a, float b) {
  f32x2 v = {a, b};
  return __builtin_bit_cast(unsigned, __builtin_convertvector(v, bf16x2));
}
__device__ __forceinline__ float rbf(float f) { return bflo(pk2(f, f)); }
__device__ __forceinline__ unsigned perm_lo(unsigned hi_src, unsigned lo_src) {
  return __builtin_amdgcn_perm(hi_src, lo_src, 0x05040100u);
}
__device__ __forceinline__ unsigned perm_hi(unsigned hi_src, unsigned lo_src) {
  return __builtin_amdgcn_perm(hi_src, lo_src, 0x07060302u);
}
__device__ __forceinline__ f32x4 mfma16(u32x4 a, u32x4 b, f32x4 c) {
  return __builtin_amdgcn_mfma_f32_16x16x32_bf16(__builtin_bit_cast(bf16x8, a), __builtin_bit_cast(bf16x8, b), c, 0, 0, 0);
}
__device__ __forceinline__ u32x2 tr_read(const char* p) {
  return __builtin_bit_cast(u32x2, __builtin_amdgcn_ds_read_tr16_b64_v4i16((LAS s16x4*)(p)));
}
#define BARRIER() asm volatile("s_barrier" ::: "memory")
#define WAITV(n) asm volatile("s_waitcnt vmcnt(" #n ")" ::: "memory")
#define WAITV_(n) asm volatile("s_waitcnt vmcnt(%0)" ::"i"(n) : "memory")
#define WAITL0() asm volatile("s_waitcnt lgkmcnt(0)" ::: "memory")

__device__ __forceinline__ void top2_mrg(unsigned& k1, unsigned& k2, unsigned b1, unsigned b2) {
  k2 = max(max(k2, b2), min(k1, b1));
  k1 = max(k1, b1);
}
template <int CTRL>
__device__ __forceinline__ void top2_dpp(unsigned& k1, unsigned& k2) {
  const unsigned b1 = (unsigned)__builtin_amdgcn_update_dpp(0, (int)k1, CTRL, 0xF, 0xF, true);
  const unsigned b2 = (unsigned)__builtin_amdgcn_update_dpp(0, (int)k2, CTRL, 0xF, 0xF, true);
  top2_mrg(k1, k2, b1, b2);
}
__device__ __forceinline__ void top2_row(unsigned& k1, unsigned& k2) {
  top2_dpp<0xB1>(k1, k2);
  top2_dpp<0x4E>(k1, k2);
  top2_dpp<0x141>(k1, k2);
  top2_dpp<0x140>(k1, k2);
}
__device__ __forceinline__ void top2_all(unsigned& k1, unsigned& k2) {
  top2_row(k1, k2);
  {
    auto a = __builtin_amdgcn_permlane16_swap(k1, k1, false, false);
    auto c = __builtin_amdgcn_permlane16_swap(k2, k2, false, false);
    k1 = a[0];
    k2 = c[0];
    top2_mrg(k1, k2, a[1], c[1]);
  }
  {
    auto a = __builtin_amdgcn_permlane32_swap(k1, k1, false, false);
    auto c = __builtin_amdgcn_permlane32_swap(k2, k2, false, false);
    k1 = a[0];
    k2 = c[0];
    top2_mrg(k1, k2, a[1], c[1]);
  }
}
__device__ __forceinline__ void touch_line(const char* p, unsigned lds_off) {
  asm volatile("s_mov_b32 m0, %1\n\ts_nop 0\n\tglobal_load_lds_dword %0, off" ::"v"(p), "s"(lds_off) : "memory");
}
__device__ __forceinline__ void dma16(unsigned voff, const void* sbase, unsigned lds_off) {
  asm volatile("s_mov_b32 m0, %2\n\ts_nop 0\n\tglobal_load_lds_dwordx4 %0, %1" ::"v"(voff), "s"(sbase), "s"(lds_off) : "memory");
}
__device__ __forceinline__ void dma16_sc1(unsigned voff, const void* sbase, unsigned lds_off) {
  asm volatile("s_mov_b32 m0, %2\n\ts_nop 0\n\tglobal_load_lds_dwordx4 %0, %1 sc1" ::"v"(voff), "s"(sbase), "s"(lds_off) : "memory");
}
__device__ __forceinline__ void bdma16(unsigned voff, u32x4 srd, unsigned soff, unsigned lds_off) {
  asm volatile("s_mov_b32 m0, %3\n\ts_nop 0\n\tbuffer_load_dwordx4 %0, %1, %2 offen lds" ::"v"(voff), "s"(srd), "s"(soff), "s"(lds_off) : "memory");
}
__device__ __forceinline__ u32x4 make_srd(const void* p, unsigned bytes) {
  return (u32x4){(unsigned)(uintptr_t)p, (unsigned)((uintptr_t)p >> 32) & 0xFFFFu, bytes, 0x00020000u};
}
__device__ __forceinline__ float key_val(unsigned key) {
  unsigned u = key >> 16;
  u ^= (u & 0x8000u) ? 0x8000u : 0xFFFFu;
  return bflo(u);
}
__device__ __forceinline__ float fmx(float a, float b) { return __builtin_elementwise_maximum(a, b); }
#define SB() __builtin_amdgcn_sched_barrier(0)
__device__ __forceinline__ f32x4 mfma16h(u32x2 a, u32x2 b, f32x4 c) {
  return __builtin_amdgcn_mfma_f32_16x16x16bf16_1k(__builtin_bit_cast(s16x4, a), __builtin_bit_cast(s16x4, b), c, 0, 0, 0);
}

#define SLOT_B 43008
#define L_WO SLOT_B
#define L_EARLY (3 * SLOT_B)
#define L_PIECE L_EARLY
#define L_CAND (L_VAR + 128)
#define L_TOUCH (L_EARLY + 12288)
#define L_JUNK (L_WO + 102400)
#define L_VAR (L_JUNK + 384)
#define L_CAND2 (L_VAR + 48)
#define L_GT (L_VAR + 256)
#define L_FS (L_GT + 1024)
#define L_XT (L_FS + 1024)
#define L_TOTAL (L_XT + 10240)
#define CODE_LINES 255

__device__ __forceinline__ void res_wait(char* ws, char* smem, int lane, unsigned tag, int which) {
  const __amdgpu_buffer_rsrc_t r = __builtin_amdgcn_make_buffer_rsrc((void*)(ws + WS_R), 0, 256, 0x00020000);
  u32x4 v;
#pragma unroll 1
  for (unsigned spins = 0; spins < SPIN_CAP; ++spins) {
    asm volatile("" ::: "memory");
    v = __builtin_amdgcn_raw_buffer_load_b128(r, which * 128, 0, 16);
    if (__all((v[1] == tag) & (v[3] == tag))) break;
    __builtin_amdgcn_s_sleep(2);
  }
  if (lane == 0 && which == 0) *(u32x2*)(smem + L_VAR + 32) = (u32x2){v[0], v[2]};
  if (lane == 0 && which == 1) *(u32x4*)(smem + L_CAND2) = v;
}
__device__ __forceinline__ float dot2(unsigned a, unsigned b, float c) {
  return __builtin_amdgcn_fdot2_f32_bf16(__builtin_bit_cast(bf16x2, a), __builtin_bit_cast(bf16x2, b), c, false);
}
template <int CTRL>
__device__ __forceinline__ float dpp_add(float v) {
  return v + __int_as_float(__builtin_amdgcn_update_dpp(0, __float_as_int(v), CTRL, 0xF, 0xF, true));
}
__device__ __forceinline__ float row_sum(float v) {
  v = dpp_add<0xB1>(v);
  v = dpp_add<0x4E>(v);
  v = dpp_add<0x141>(v);
  return dpp_add<0x140>(v);
}

template <int N> struct IC { static constexpr int value = N; };
#define PIN(v) asm volatile("" : "+v"(v))
__global__ __launch_bounds__(512, 2) void kf(const u16*, const u16*, const u16*, const u16*, const u16*, const u16*, const u16*, const u16*, char*, u16*) {
  __shared__ __attribute__((aligned(16))) char smem[L_TOTAL];
  typedef const __attribute__((address_space(4))) u64* karg_t;
  const karg_t kp0 = (karg_t)__builtin_amdgcn_kernarg_segment_ptr();
  u64 pc;
  asm volatile("s_load_dword s100, %1, 0x0\n\ts_load_dword s101, %1, 0x40\n\ts_getpc_b64 %0" : "=s"(pc) : "s"(kp0) : "s100", "s101");
  const u16 *x, *wqkv, *wo, *w_r, *gate_w, *w_gate, *w_up, *w_down;
  char* ws;
  u16* out;
  auto args = [&]() {
    SB();
    karg_t kp = kp0;
    asm volatile("" : "+s"(kp));
    x = (const u16*)kp[0]; wqkv = (const u16*)kp[1]; wo = (const u16*)kp[2]; w_r = (const u16*)kp[3]; gate_w = (const u16*)kp[4];
    w_gate = (const u16*)kp[5]; w_up = (const u16*)kp[6]; w_down = (const u16*)kp[7]; ws = (char*)kp[8]; out = (u16*)kp[9];
  };
  const int tid = threadIdx.x, b = blockIdx.x;
  {
    const int ln = (b >> 3) + 32 * tid;
    if (ln < CODE_LINES)
      touch_line((const char*)((pc & ~255ull) + ln * 128), (unsigned)(uintptr_t)(LAS char*)smem + L_TOUCH);
  }
  unsigned tag;
  auto get_tag = [&]() {
    asm volatile("s_load_dword %0, %1, 0x0\n\ts_waitcnt lgkmcnt(0)" : "=s"(tag) : "s"(ws + WS_E) : "memory");
    tag += TAG_STEP;
  };
  const int lane = tid & 63;
  const int wave = __builtin_amdgcn_readfirstlane(tid >> 6);
  const int xcd = b & 7, jj = b >> 3;
  const int c15 = lane & 15, g4 = lane >> 4;
  const bool gemm_blk = jj < 28;
  const int q_ = (xcd & 3) * 28 + jj;
  const int rg = q_ >> 2, hg = (xcd >> 2) * 4 + (q_ & 3);
  const int par = rg >= 14, gi = rg - par * 14;
  const int tb = min(73 * gi, 944);
  const int tlo = gi == 0 ? 0 : 73 * gi + 7;
  const int lg = jj >> 2;
  const int rt2 = gemm_blk ? par * 16 + gi : (xcd & 2) * 8 + 14 + (xcd & 1), nt2 = hg;
  const unsigned lds0 = (unsigned)(uintptr_t)(LAS char*)smem;
  auto wo_dma = [&](auto n_, int i0, int st, int lo) {
    const u32x4 srd = make_srd(ws + WS_WOF + nt2 * 102400, 102400);
    int g = __builtin_amdgcn_readfirstlane((i0 + ((lg * (25 - lo)) >> 3)) * 4096);
#pragma unroll
    for (int i = 0; i < decltype(n_)::value; ++i) {
      if (g >= 102400) g -= (25 - lo) * 4096;
      asm volatile("s_add_u32 m0, %3, %2\n\ts_nop 0\n\tbuffer_load_dwordx4 %0, %1, %2 offen sc1 lds\n\t"
                   "buffer_load_dwordx4 %0, %1, %2 offen offset:1024 sc1 lds\n\tbuffer_load_dwordx4 %0, %1, %2 offen offset:2048 sc1 lds\n\t"
                   "buffer_load_dwordx4 %0, %1, %2 offen offset:3072 sc1 lds" ::"v"(lane * 16), "s"(srd), "s"(g), "s"(lds0 + L_WO)
                   : "memory", "scc");
      g += st * 4096;
    }
  };

  int od[5];
  const int pa = tb + c15 - 64 * gi;
  const bool pv = tb + c15 >= tlo;
  auto prepO = [&]() {
    const int h = hg * 4 + (wave & 3);
    const int b0 = (par * 64 + 56 + (pa >> 4)) * 20480, b1 = (par * 64 + gi * 4) * 20480;
    od[0] = (pv && lane < 32) ? b0 + (2 * h + g4) * 256 + (pa & 15) * 16 : 0x7FFFFF00;
    od[1] = b1 + (lane >> 5) * 20480 + (2 * h + (g4 & 1)) * 256 + c15 * 16;
    od[2] = od[1] + 2 * 20480;
    od[3] = b1 + g4 * 20480 + 16384 + h * 128 + c15 * 8;
    od[4] = (pv && g4 == 3) ? b0 + 16384 + h * 128 + (pa & 15) * 8 : 0x7FFFFF00;
#pragma unroll
    for (int e = 0; e < 5; ++e) PIN(od[e]);
  };
  auto row_t = [&](int row) {
    const int a = (xcd & 1) * 64 + row;
    return gemm_blk ? tb + 16 + row : a + (a < 16 ? 0 : (a - 7) / 9) * 64;
  };
  int adr[3], glo;
  u64 rel;
  auto tile_off = [&](int row) { return ((2 * row_t(row) + (rt2 >> 4)) * DM + nt2 * 80) * 2; };
  auto prepB = [&]() {
    const int row = (wave & 3) * 16 + c15;
    if (wave < 4) {
#pragma unroll
      for (int e = 0; e < 3; ++e) {
        const int c = min(e * 64 + lane, 159), r = c / 10;
        adr[e] = tile_off((wave & 3) * 16 + r) + (c - r * 10) * 16;
      }
    } else {
      adr[2] = tile_off(row);
      adr[0] = adr[2] + (nt2 & 1) * 32 + (g4 & 1) * 32 + (g4 >> 1) * 16;
      adr[1] = adr[2] + ((nt2 & 1) ? 0 : 128) + g4 * 8;
    }
    PIN(adr[0]); PIN(adr[1]); PIN(adr[2]);
    glo = L_GT + row * 16 + (((gemm_blk ? tb : 0) + row) & 1) * 8;
    PIN(glo);
    {
      const int a0 = (xcd & 1) * 64 + (wave & 3) * 16, gl = (xcd & 1) ? 6 + (lane >> 3) : min(lane >> 3, 6);
      rel = __builtin_amdgcn_ballot_w64(((lane >> 2) & 1) == (wave >> 2) && (gemm_blk ? lane < 8 : gl >= (a0 < 16 ? 0 : (a0 - 7) / 9) && gl <= (a0 + 8) / 9));
      asm volatile("" : "+s"(rel));
    }
  };
  auto lrow_of = [&](unsigned key) {
    const int i = 2047 - (int)(key & 0xFFFFu);
    const int t = i >> 1, g = max(t - 7, 0) / 73, rho = t - min(73 * g, 944);
    return (i & 1) == (rt2 >> 4) ? (rho >= 16 ? g * 64 + rho - 16 : 896 + t - 64 * g) - (rt2 & 15) * 64 : -1;
  };
  auto rows_job = [&]() {
    if (lane < 2) {
      const unsigned key = *(const unsigned*)(smem + L_VAR + 32 + lane * 4);
      *(int*)(smem + L_VAR + 40 + lane * 4) = lrow_of(key);
      *(float*)(smem + L_JUNK + 256 + lane * 4) = rbf(__builtin_amdgcn_rcpf(1.0f + __expf(-key_val(key))));
    }
  };
  auto gate_off = [&](int row) {
    const int p2 = rt2 >> 4, tg = row_t(row);
    const int q2 = p2 * 2 + (tg >> 9), u = tg & 511;
    return (u < 448 ? (q2 * 56 + (u >> 3)) * 128 + (u & 7) * 8 : (224 + q2 * 16 + ((u - 448) >> 2)) * 128 + (u & 3) * 8) + 64;
  };
  auto flag_off = [&]() {
    const int g = gemm_blk ? gi : (xcd & 1) ? 6 + (lane >> 3) : min(lane >> 3, 6);
    return (((rt2 >> 4) * 14 + g) * 8 + (lane & 7)) * 128 + 56;
  };
#define rfl __builtin_amdgcn_make_buffer_rsrc((void*)(ws + WS_F), 0, 28672, 0x00020000)
  auto gate_dma = [&](int go) { dma16_sc1(go & ~15, ws + WS_K, lds0 + L_GT); };
  auto now = []() { return (unsigned)__builtin_amdgcn_s_memrealtime(); };
  auto flag_wait = [&](int foff) {
#pragma unroll 1
    for (unsigned spins = 0; spins < SPIN_CAP; ++spins) {
      asm volatile("" ::: "memory");
      const u32x4 v = __builtin_amdgcn_raw_buffer_load_b128(rfl, foff, 0, 16);
      if ((v[1] == tag) & (v[3] == tag) & (foff != 0x7FFFFF00)) {
        *(u32x4*)(smem + L_FS + lane * 16) = v;
        foff = 0x7FFFFF00;
      }
      if (__all(foff == 0x7FFFFF00)) break;
      __builtin_amdgcn_s_sleep(8);
    }
  };
#define ro __builtin_amdgcn_make_buffer_rsrc((void*)(ws + WS_OF), 0, S_LEN * DM * 2, 0x00020000)

  if (__builtin_expect(gemm_blk, 1)) {
    if (__builtin_expect(wave >= 4, 1)) {
      const int hw = wave - 4;
      const int rot = (lg % 5) * 2 + (xcd & 1);
      int voffA = (lane >> 3) * (DM * 4) + (((lane & 7) ^ (((hw & 1) * 4 + g4) & 7)) * 16);
      int sA[3];
#pragma unroll
      for (int v = 0; v < 3; ++v) {
        sA[v] = ((tb + 8 * (hw + 4 * v)) * 2 + par) * (DM * 2);
      }
      const int hi = lane >> 5;
      int pair = ((lane & 31) >> 1) ^ (hi | ((hw & 1) << 1) | ((hw >> 1) << 2));
      const int sec = (pair >= 5) + (pair >= 10);
      int voffW = pair == 15 ? 0x7FFFFF00 : hi * (NQKV * 2) + sec * (1280 - 160) + hg * 160 + pair * 32 + ((lane & 1) ^ (sec == 2)) * 16;
      const int n0 = (hw & 1) + 4 * (hw >> 1);
      PIN(voffA); PIN(voffW);
      args();
      const u32x4 rsx = make_srd(x, S_LEN * DM * 2), rsw = make_srd(wqkv, DM * NQKV * 2);
      auto issue = [&](int i, int buf) {
        int kc = rot + (i < 5 ? 2 * i : 2 * i - 9);
        if (kc >= 10) kc -= 10;
        const unsigned dst = lds0 + buf * SLOT_B;
#pragma unroll
        for (int v = 0; v < 3; ++v)
          if (v < 2 || hw < 2) bdma16(voffA, rsx, sA[v] + kc * 128, dst + (hw + 4 * v) * 1024);
        const int so = (kc * 64 + 2 * n0) * (NQKV * 2);
#pragma unroll
        for (int u = 0; u < 8; ++u) {
          const int dn = 2 * (u & 1) + 8 * (u >> 1);
          bdma16(voffW, rsw, so + dn * 2 * (NQKV * 2), dst + 10240 + (n0 + dn) * 1024);
        }
      };
      const int qp = rg * 8 + hg;
      issue(0, 0);
      SB();
      int md = (par * 64 + gi * 4 + 2 * (hw & 1) + (lane >> 5)) * 20480 + 16384 + hg * 512 + (lane & 31) * 16;
      PIN(md);
      int so_ = hw == 0 ? flag_off() : gate_off(lane);
      PIN(so_);
      prepB();
      __builtin_amdgcn_sched_barrier(0);
      issue(1, 1);
      get_tag();
      WAITV(10);
      BARRIER();
      issue(2, 2);
      WAITV(10);
      BARRIER();
      const unsigned r0_ = now(), c0_ = (unsigned)__builtin_amdgcn_s_memtime();
      {
        int buf = 0;
#pragma unroll 1
        for (int i = 3; i < 9; ++i) {
          const bool ex = i == 4 && hw == 2;
          if (ex && lane == 0) __hip_atomic_store((gu32*)(ws + WS_F2 + b * 128), tag, RLX_AGENT);
          issue(i, buf);
          if (i == 3)
            asm volatile("s_load_dword s100, %0, 0x0\n\ts_load_dword s100, %0, 0xa00\n\ts_load_dword s100, %0, 0x1400\n\ts_load_dword s100, %0, 0x1e00\n\t"
                         "s_load_dword s100, %0, 0x2800\n\ts_load_dword s100, %0, 0x3200\n\ts_load_dword s100, %0, 0x3c00\n\ts_load_dword s100, %0, 0x4600" ::"s"(
                             (const char*)out + tile_off(hw * 8 + (nt2 & 1) * 32) + ((nt2 & 1) ? 0 : 128))
                         : "s100");
          if (ex) WAITV(11); else WAITV(10);
          buf = buf == 2 ? 0 : buf + 1;
          BARRIER();
        }
      }
      if (hw == 3) {
        if (lane < 2) dma16_sc1(lane * 128, ws + WS_R, lds0 + L_CAND);
        if (lane < 8) *(unsigned*)(smem + L_VAR + lane * 4) = 0u;
      }
      issue(9, 0);
      auto mark = [&]() {
        const u32x4 sent4 = {SENT, SENT, SENT, SENT};
        const int mo_ = (par * 64 + gi * 4 + (hw & 1) * 2) * 20480 + hg * 2048;
#pragma unroll
        for (int e = 0; e < 5; ++e) {
          if (e == 0) __builtin_amdgcn_raw_buffer_store_b128(sent4, ro, md, 0, 16);
          else __builtin_amdgcn_raw_buffer_store_b128(sent4, ro, lane * 16 + ((e - 1) & 1) * 1024, mo_ + ((e - 1) >> 1) * 20480, 16);
        }
        SB();
      };
      auto words = [&]() {
        const u32x4 rv = *(const u32x4*)(smem + L_CAND + (lane & 1) * 16);
        if (__all((rv[1] == tag) & (rv[3] == tag))) {
          if (tid == 256) *(u32x2*)(smem + L_VAR + 32) = (u32x2){rv[0], rv[2]};
        } else {
          res_wait(ws, smem, lane, tag, 0);
          res_wait(ws, smem, lane, tag, 1);
        }
      };
      if (hw >= 2) {
        mark();
        WAITV(15);
        BARRIER();
        WAITV(5);
        BARRIER();
        unsigned t0_ = now();
        t0_ += (unsigned)((float)(t0_ - r0_) * (float)TCY * __builtin_amdgcn_rcpf((float)((unsigned)__builtin_amdgcn_s_memtime() - c0_)));
        WAITV(0);
        __builtin_amdgcn_raw_buffer_store_b128((u32x4){t0_, tag, t0_, tag}, rfl, lane < 4 ? qp * 128 + (hw & 1) * 64 + lane * 16 : 0x7FFFFF00, 0, 16);
        if (hw == 2) rows_job();
      } else {
        WAITV(10);
        BARRIER();
        words();
        wo_dma(IC<WB8>{}, hw, 2, 25);
        WAITV_(4 * WB8);
        WAITL0();
        BARRIER();
        if (hw == 1) gate_dma(so_);
        wo_dma(IC<12 - WB8>{}, 2 * WB8 + hw, 2, 2 * WB8);
        if (hw == 0 && lane < 8) dma16_sc1(so_, ws + WS_F, lds0 + L_FS);
        if (hw == 0) wo_dma(IC<1>{}, 24, 2, 2 * WB8);
        WAITV(0);
      }
      WAITL0();
      BARRIER();
    } else {
      const int xb_ = b - 160;
      const bool wjob = wave < 2 || (wave == 2 && (unsigned)xb_ < 64u);
      const int xi = (xb_ >> 3) * 2 + (xcd >> 2);
      const int tj0 = (xcd & 1) * 512 + (wave < 2 ? lg * 64 + hg * 8 + wave * 4 : 448 + xi * 4), pj = par;
      u32x4 xs[5], wv[4];
      int jo0 = lane * 16, jo1 = g4 * 2560 + c15 * 16, jo2 = c15 * 16;
      PIN(jo0); PIN(jo1); PIN(jo2);
      args();
      if (wjob) {
        const u16* xr = x + (2 * tj0 + pj) * DM;
        asm volatile("global_load_dwordx4 %0, %9, %12\n\tglobal_load_dwordx4 %1, %9, %12 offset:2560\n\t"
                     "global_load_dwordx4 %2, %9, %13\n\tglobal_load_dwordx4 %3, %9, %13 offset:2560\n\t"
                     "global_load_dwordx4 %4, %10, %12 offset:1024\n\tglobal_load_dwordx4 %5, %9, %14\n\t"
                     "global_load_dwordx4 %6, %11, %14 offset:1024\n\tglobal_load_dwordx4 %7, %9, %15\n\tglobal_load_dwordx4 %8, %11, %15 offset:1024"
                     : "=&v"(xs[0]), "=&v"(xs[1]), "=&v"(xs[2]), "=&v"(xs[3]), "=&v"(xs[4]), "=&v"(wv[0]), "=&v"(wv[1]), "=&v"(wv[2]), "=&v"(wv[3])
                     : "v"(jo0), "v"(jo1), "v"(jo2), "s"(xr), "s"(xr + 4 * DM), "s"(w_r), "s"(gate_w)
                     : "memory");
      }
      f32x4 acc[5][4];
#pragma unroll
      for (int m = 0; m < 5; ++m)
#pragma unroll
        for (int f = 0; f < 4; ++f) acc[m][f] = (f32x4){0.f, 0.f, 0.f, 0.f};
      const int wks = b % 20;
      if (b < 200) {
        const int kh = wks >= 10, s = wks - kh * 10, pr = lane >> 3, g = wave, hi = pr >> 2;
        const int row = (s < 8 ? (2 * (kh * 8 + s) + (g >> 1)) * 20 + 4 * (g & 1) + 8 * hi : (kh * 16 + (s - 8) * 8 + hi * 4 + g) * 20 + 16) + (pr & 3);
        asm volatile("s_mov_b32 m0, %2\n\ts_nop 0\n\tglobal_load_lds_dwordx4 %0, %1 nt" ::"v"((row * DM + (b / 20) * 64 + (lane & 7) * 8) * 2), "s"(wo), "s"(lds0 + L_PIECE + wave * 1024) : "memory");
      }
      const __amdgpu_buffer_rsrc_t rk = __builtin_amdgcn_make_buffer_rsrc((void*)(ws + WS_K), 0, 36864, 0x00020000);
      const int ko = c15 != 0 ? 0x7FFFFF00 : wave < 2 ? (rg * 8 + hg) * 128 + (wave * 4 + g4) * 8 : (224 + (xcd & 3) * 16 + xi) * 128 + g4 * 8;
      auto rows4 = [&](float (&p)[5]) {
        const auto a = __builtin_amdgcn_permlane32_swap(__float_as_uint(p[0]), __float_as_uint(p[2]), false, false);
        const auto c = __builtin_amdgcn_permlane32_swap(__float_as_uint(p[1]), __float_as_uint(p[3]), false, false);
        const auto e = __builtin_amdgcn_permlane16_swap(__float_as_uint(__uint_as_float(a[0]) + __uint_as_float(a[1])),
                                                        __float_as_uint(__uint_as_float(c[0]) + __uint_as_float(c[1])), false, false);
        return row_sum(__uint_as_float(e[0]) + __uint_as_float(e[1]) + p[4]);
      };
      get_tag();
      if (wjob) {
        if (b < 200) WAITV(3); else WAITV(2);
#pragma unroll
        for (int i = 0; i < 5; ++i) PIN(xs[i]);
        PIN(wv[0]); PIN(wv[1]);
        float p[5];
#pragma unroll
        for (int i = 0; i < 5; ++i) {
          p[i] = 0.f;
#pragma unroll
          for (int q = 0; q < 4; ++q) p[i] = dot2(xs[i][q], wv[i == 4][q], p[i]);
        }
        const float r_ = rows4(p);
        unsigned u = pk2(r_, r_) & 0xFFFFu;
        u ^= (u & 0x8000u) ? 0xFFFFu : 0x8000u;
        const unsigned key = (u << 16) | (unsigned)(2047 - (2 * (tj0 + g4) + pj));
        __builtin_amdgcn_raw_buffer_store_b64((u32x2){key, tag}, rk, ko, 0, 16);
        SB();
      }
      u32x2 gv = {0u, 0u};
      int ga = 0x7FFFFF00;
      if (wjob) {
        WAITV(1);
        PIN(wv[2]); PIN(wv[3]);
        float p[5];
#pragma unroll
        for (int i = 0; i < 5; ++i) {
          p[i] = 0.f;
#pragma unroll
          for (int q = 0; q < 4; ++q) {
            const unsigned gw = wv[2 + (i == 4)][q];
            p[i] = dot2(pk2(bflo(xs[i][q]) * bflo(gw), bfhi(xs[i][q]) * bfhi(gw)), 0x3F803F80u, p[i]);
          }
        }
        const float g_ = rbf(rows4(p));
        gv = (u32x2){__float_as_uint(rbf(1.0f / (1.0f + __expf(-g_)))), tag};
        ga = c15 != 0 ? ko : ko + 64;
        SB();
      }
      PIN(gv); PIN(ga);
      prepO();
      const int p4 = lane & 3;
      f32x4 limA, limB;
      const int e0 = c15 - 4 * g4;
#pragma unroll
      for (int i = 0; i < 4; ++i) {
        limA[i] = e0 - i <= -9 ? 0.f : -__builtin_inff();
        limB[i] = (e0 - i >= 0 && e0 - i <= 7) ? 0.f : -__builtin_inff();
      }
      unsigned mA01 = (e0 <= -9 ? 0xFFFFu : 0u) | (e0 - 1 <= -9 ? 0xFFFF0000u : 0u);
      unsigned mA23 = (e0 - 2 <= -9 ? 0xFFFFu : 0u) | (e0 - 3 <= -9 ? 0xFFFF0000u : 0u);
      PIN(limA); PIN(limB); PIN(mA01); PIN(mA23);
      const unsigned mA[2] = {mA01, mA23};
      u32x4 ones = {0x3F803F80u, 0x3F803F80u, 0x3F803F80u, 0x3F803F80u};
      PIN(ones);
      u32x2 sel[4], kz[5];
#pragma unroll
      for (int t = 0; t < 4; ++t) {
        const bool on = g4 == 2 && (((c15 >> 2) - t + 1) & 3) < 2;
        sel[t] = (u32x2){on && p4 < 2 ? 0x3F80u << (16 * p4) : 0u, on && p4 >= 2 ? 0x3F80u << (16 * (p4 - 2)) : 0u};
        PIN(sel[t]);
      }
#pragma unroll
      for (int t = 0; t < 5; ++t) {
        kz[t] = (u32x2){0u, 0u};
        PIN(kz[t]);
      }
      const int swz = (c15 >> 1) & 7;
      const int aoff0 = c15 * 128 + ((g4 ^ swz) * 16);
      const int aoff1 = c15 * 128 + (((4 + g4) ^ swz) * 16);
      int woff[4];
      {
        const int q4 = c15 >> 2, sw = q4 | ((g4 & 1) << 2);
#pragma unroll
        for (int t = 0; t < 4; ++t) {
          const int sc = t < 3 ? t : p4;
          const int by = wave * 40 + (t < 3 ? p4 : 4) * 8;
          woff[t] = 10240 + (8 * g4 + q4) * 512 + (((sc == 3 ? 15 : sc * 5 + (by >> 5)) ^ sw) * 32) + ((by & 31) ^ (sc == 2 ? 16 : sc == 3 ? 8 : 0));
          PIN(woff[t]);
        }
      }
      int wo3[2][4], ao3[2][2];
#pragma unroll
      for (int q = 0; q < 2; ++q) {
#pragma unroll
        for (int j = 0; j < 4; ++j) {
          wo3[q][j] = woff[j] + (q + 1) * SLOT_B;
          PIN(wo3[q][j]);
        }
        ao3[q][0] = aoff0 + (q + 1) * SLOT_B;
        ao3[q][1] = aoff1 + (q + 1) * SLOT_B;
        PIN(ao3[q][0]); PIN(ao3[q][1]);
      }
#define GM(m, f, wf, af) acc[m][f] = (f) == 2 ? mfma16(af[m], wf[f], acc[m][f]) : mfma16(wf[f], af[m], acc[m][f])
      u32x4 af0[5], wf0[4], af1[5], wf1[4];
      auto rd1 = [&](u32x4(&an)[5], u32x4(&wn)[4], int q, int h, int r) {
        const int ao = q ? ao3[q - 1][h] : h ? aoff1 : aoff0;
        if (r == 2) {
          an[0] = *(const u32x4*)(smem + ao);
        } else if (r >= 9) {
          an[r - 8] = *(const u32x4*)(smem + ao + (r - 8) * 2048);
        } else {
          const int k = r < 2 ? r : r - 1;
          const u32x2 t = tr_read(smem + (q ? wo3[q - 1][k >> 1] : woff[k >> 1]) + h * 16384 + (k & 1) * 2048);
          wn[k >> 1][(k & 1) * 2] = t[0];
          wn[k >> 1][(k & 1) * 2 + 1] = t[1];
        }
      };
      auto half = [&](const u32x4(&a)[5], const u32x4(&w)[4], u32x4(&an)[5], u32x4(&wn)[4], int q, int h) {
#pragma unroll
        for (int i = 0; i < 20; ++i) {
          const int m_ = i < 15 ? i / 3 : 19 - i, j_ = ((i / 3) & 1) ? 2 - i % 3 : i % 3, f_ = i < 15 ? j_ + (j_ == 2) : 2;
          GM(m_, f_, w, a);
          SB();
          if (i < 13) rd1(an, wn, q, h, i);
          SB();
        }
      };
      u32x4 wfr;
      int wfa;
      {
        if (!wjob) WAITV(0);
        const char* tp = smem + L_PIECE + wave * 1024 + (c15 >> 2) * 128 + g4 * 32 + (lane & 3) * 8;
        const u32x2 lo = tr_read(tp), hi = tr_read(tp + 512);
        const int T = (b / 20) * 4 + g4;
        wfr = (u32x4){lo[0], lo[1], hi[0], hi[1]};
        wfa = b < 200 ? (((T / 5) * 20 + wks) * 5 + (T + 5 - ((T / 5) & 1)) % 5) * 1024 + (wave * 16 + c15) * 16 : 0x7FFFFF00;
        PIN(wfr); PIN(wfa);
      }
      WAITL0();
      BARRIER();
#pragma unroll
      for (int r = 0; r < 13; ++r) rd1(af0, wf0, 0, 0, r);
      SB();
#pragma unroll
      for (int i = 0; i < 9; ++i) {
        half(af0, wf0, af1, wf1, i % 3, 1);
        if (i == 0) {
          __builtin_amdgcn_raw_buffer_store_b128(wfr, __builtin_amdgcn_make_buffer_rsrc((void*)(ws + WS_WOF), 0, 819200, 0x00020000), wfa, 0, 16);
          __builtin_amdgcn_raw_buffer_store_b64(gv, rk, ga, 0, 16);
        }
        if (i == 1) WAITV(0);
        if (i == 6) prepB();
        SB();
        WAITL0();
        BARRIER();
        if (i < 8) half(af1, wf1, af0, wf0, (i + 1) % 3, 0);
      }
      u32x4 wf2[4], af2[5];
      u32x4 qf[5], kf[5];
      u32x10 vb, tb_;
      u32x4 pf[5], os[3];
      u32x2 pp[5], ob[6], obT, obU;
      f32x4 l[5], sA[5], oA[5], sm[5], tx[5], oT[3];
      f32x2 ar[5][2], nn0[2], nn1[2], nn2[2], nn3[2], nn4[2], nnT[2], nnU[2];
      float mx[6], inv[5], it, iu;
      constexpr u64 mo = 0xFFFF0000FFFF0000ull, mh = 0xFFFFFFFF00000000ull;
      auto selm = [](u64 m_, float a, float c) { return __builtin_amdgcn_inverse_ballot_w64(m_) ? a : c; };
      const float cs = 0.22360679774997896f * 1.4426950408889634f;
      const f32x2 c2 = {cs, cs};
      const f32x4 zero4 = {0.f, 0.f, 0.f, 0.f};
      auto cpy = [](float v) { PIN(v); return v; };
      auto cpyu = [](unsigned v) { PIN(v); return v; };
      auto mulo = [](float v, float c) { PIN(v); return v * c; };
#define XBAR() BARRIER()
      GM(0, 3, wf1, af1); SB();
      af0[0] = *(const u32x4*)(smem + aoff0 + 0); SB();
      { const u32x2 t_ = tr_read(smem + woff[3] + 0); wf0[3][0] = t_[0]; wf0[3][1] = t_[1]; } SB();
      GM(0, 1, wf1, af1); SB();
      { const u32x2 t_ = tr_read(smem + woff[3] + 2048); wf0[3][2] = t_[0]; wf0[3][3] = t_[1]; } SB();
      { const u32x2 t_ = tr_read(smem + woff[1] + 0); wf0[1][0] = t_[0]; wf0[1][1] = t_[1]; } SB();
      GM(0, 0, wf1, af1); SB();
      { const u32x2 t_ = tr_read(smem + woff[1] + 2048); wf0[1][2] = t_[0]; wf0[1][3] = t_[1]; } SB();
      { const u32x2 t_ = tr_read(smem + woff[0] + 0); wf0[0][0] = t_[0]; wf0[0][1] = t_[1]; } SB();
      GM(0, 2, wf1, af1); SB();
      { const u32x2 t_ = tr_read(smem + woff[0] + 2048); wf0[0][2] = t_[0]; wf0[0][3] = t_[1]; } SB();
      af2[0] = *(const u32x4*)(smem + aoff1 + 0); SB();
      GM(1, 3, wf1, af1); SB();
      { const u32x2 t_ = tr_read(smem + woff[2] + 0); wf0[2][0] = t_[0]; wf0[2][1] = t_[1]; } SB();
      { const u32x2 t_ = tr_read(smem + woff[3] + 16384); wf2[3][0] = t_[0]; wf2[3][1] = t_[1]; } SB();
      GM(1, 1, wf1, af1); SB();
      { const u32x2 t_ = tr_read(smem + woff[2] + 2048); wf0[2][2] = t_[0]; wf0[2][3] = t_[1]; } SB();
      { const u32x2 t_ = tr_read(smem + woff[3] + 18432); wf2[3][2] = t_[0]; wf2[3][3] = t_[1]; } SB();
      GM(1, 0, wf1, af1); SB();
      { const u32x2 t_ = tr_read(smem + woff[1] + 16384); wf2[1][0] = t_[0]; wf2[1][1] = t_[1]; } SB();
      { const u32x2 t_ = tr_read(smem + woff[1] + 18432); wf2[1][2] = t_[0]; wf2[1][3] = t_[1]; } SB();
      GM(1, 2, wf1, af1); SB();
      { const u32x2 t_ = tr_read(smem + woff[0] + 16384); wf2[0][0] = t_[0]; wf2[0][1] = t_[1]; } SB();
      { const u32x2 t_ = tr_read(smem + woff[0] + 18432); wf2[0][2] = t_[0]; wf2[0][3] = t_[1]; } SB();
      GM(2, 3, wf1, af1); SB();
      af0[1] = *(const u32x4*)(smem + aoff0 + 2048); SB();
      { const u32x2 t_ = tr_read(smem + woff[2] + 16384); wf2[2][0] = t_[0]; wf2[2][1] = t_[1]; } SB();
      GM(2, 1, wf1, af1); SB();
      af2[1] = *(const u32x4*)(smem + aoff1 + 2048); SB();
      { const u32x2 t_ = tr_read(smem + woff[2] + 18432); wf2[2][2] = t_[0]; wf2[2][3] = t_[1]; } SB();
      GM(2, 0, wf1, af1); SB();
      af0[2] = *(const u32x4*)(smem + aoff0 + 4096); SB();
      af2[2] = *(const u32x4*)(smem + aoff1 + 4096); SB();
      GM(2, 2, wf1, af1); SB();
      af0[3] = *(const u32x4*)(smem + aoff0 + 6144); SB();
      af2[3] = *(const u32x4*)(smem + aoff1 + 6144); SB();
      GM(0, 3, wf0, af0); SB();
      af0[4] = *(const u32x4*)(smem + aoff0 + 8192); SB();
      af2[4] = *(const u32x4*)(smem + aoff1 + 8192); SB();
      GM(0, 1, wf0, af0); SB();
      GM(0, 0, wf0, af0); SB();
      GM(0, 2, wf0, af0); SB();
      GM(0, 3, wf2, af2); SB();
      GM(0, 1, wf2, af2); SB();
      GM(0, 0, wf2, af2); SB();
      GM(0, 2, wf2, af2); SB();
      GM(1, 3, wf0, af0); SB();
      GM(1, 1, wf0, af0); SB();
      GM(1, 0, wf0, af0); SB();
      GM(1, 2, wf0, af0); SB();
      qf[0][2] = pk2(acc[0][3][0], acc[0][3][1]); SB();
      GM(1, 3, wf2, af2); SB();
      qf[0][3] = pk2(acc[0][3][2], acc[0][3][3]); SB();
      { const auto r_ = __builtin_amdgcn_permlane16_swap(qf[0][2], kz[0][0], false, false); qf[0][2] = r_[0]; kf[0][2] = r_[1]; } SB();
      GM(1, 1, wf2, af2); SB();
      kf[0][0] = pk2(acc[0][1][0], acc[0][1][1]); SB();
      { const auto r_ = __builtin_amdgcn_permlane16_swap(qf[0][3], kz[0][1], false, false); qf[0][3] = r_[0]; kf[0][3] = r_[1]; } SB();
      GM(1, 0, wf2, af2); SB();
      kf[0][1] = pk2(acc[0][1][2], acc[0][1][3]); SB();
      qf[0][0] = pk2(acc[0][0][0], acc[0][0][1]); SB();
      tx[0] = mfma16h((u32x2){qf[0][2], qf[0][3]}, sel[0], zero4); SB();
      qf[1][2] = pk2(acc[1][3][0], acc[1][3][1]); SB();
      qf[1][3] = pk2(acc[1][3][2], acc[1][3][3]); SB();
      GM(1, 2, wf2, af2); SB();
      { const auto r_ = __builtin_amdgcn_permlane16_swap(qf[1][2], kz[1][0], false, false); qf[1][2] = r_[0]; kf[1][2] = r_[1]; } SB();
      kf[1][0] = pk2(acc[1][1][0], acc[1][1][1]); SB();
      GM(2, 3, wf0, af0); SB();
      qf[1][0] = pk2(acc[1][0][0], acc[1][0][1]); SB();
      { const auto r_ = __builtin_amdgcn_permlane16_swap(qf[1][3], kz[1][1], false, false); qf[1][3] = r_[0]; kf[1][3] = r_[1]; } SB();
      GM(2, 1, wf0, af0); SB();
      kf[1][1] = pk2(acc[1][1][2], acc[1][1][3]); SB();
      qf[1][1] = pk2(acc[1][0][2], acc[1][0][3]); SB();
      tx[1] = mfma16h((u32x2){qf[1][2], qf[1][3]}, sel[1], zero4); SB();
      qf[0][1] = pk2(acc[0][0][2], acc[0][0][3]); SB();
      tb_[0] = pk2(tx[0][0], tx[0][1]); SB();
      l[1] = mfma16(kf[1], qf[1], limB); SB();
      tb_[1] = pk2(tx[0][2], tx[0][3]); SB();
      vb[0] = pk2(acc[0][2][0], acc[0][2][1]); SB();
      sA[1] = mfma16(kf[0], qf[1], limA); SB();
      vb[1] = pk2(acc[0][2][2], acc[0][2][3]); SB();
      vb[2] = pk2(acc[1][2][0], acc[1][2][1]); SB();
      l[0] = mfma16(kf[0], qf[0], limB); SB();
      vb[3] = pk2(acc[1][2][2], acc[1][2][3]); SB();
      tb_[2] = pk2(tx[1][0], tx[1][1]); SB();
      GM(2, 0, wf0, af0); SB();
      tb_[3] = pk2(tx[1][2], tx[1][3]); SB();
      GM(2, 2, wf0, af0); SB();
      l[1][0] = fmx(l[1][0], sA[1][0]); SB();
      l[1][1] = fmx(l[1][1], sA[1][1]); SB();
      GM(2, 3, wf2, af2); SB();
      l[1][2] = fmx(l[1][2], sA[1][2]); SB();
      mx[0] = fmx(fmx(l[0][0], l[0][1]), l[0][2]); SB();
      GM(2, 1, wf2, af2); SB();
      l[1][3] = fmx(l[1][3], sA[1][3]); SB();
      mx[1] = fmx(fmx(l[1][0], l[1][1]), l[1][2]); SB();
      GM(2, 0, wf2, af2); SB();
      mx[0] = fmx(mx[0], l[0][3]); SB();
      mx[1] = fmx(mx[1], l[1][3]); SB();
      GM(2, 2, wf2, af2); SB();
      qf[2][2] = pk2(acc[2][3][0], acc[2][3][1]); SB();
      qf[2][3] = pk2(acc[2][3][2], acc[2][3][3]); SB();
      GM(3, 3, wf1, af1); SB();
      { const auto r_ = __builtin_amdgcn_permlane16_swap(qf[2][2], kz[2][0], false, false); qf[2][2] = r_[0]; kf[2][2] = r_[1]; } SB();
      kf[2][0] = pk2(acc[2][1][0], acc[2][1][1]); SB();
      GM(3, 1, wf1, af1); SB();
      qf[2][0] = pk2(acc[2][0][0], acc[2][0][1]); SB();
      { const auto r_ = __builtin_amdgcn_permlane16_swap(qf[2][3], kz[2][1], false, false); qf[2][3] = r_[0]; kf[2][3] = r_[1]; } SB();
      GM(3, 0, wf1, af1); SB();
      kf[2][1] = pk2(acc[2][1][2], acc[2][1][3]); SB();
      qf[2][1] = pk2(acc[2][0][2], acc[2][0][3]); SB();
      tx[2] = mfma16h((u32x2){qf[2][2], qf[2][3]}, sel[2], zero4); SB();
      { const auto r_ = __builtin_amdgcn_permlane32_swap(__float_as_uint(mx[0]), __float_as_uint(mx[1]), false, false); mx[0] = __uint_as_float(r_[0]); mx[1] = __uint_as_float(r_[1]); } SB();
      mx[0] = fmx(mx[0], mx[1]); SB();
      l[2] = mfma16(kf[2], qf[2], limB); SB();
      mx[1] = cpy(mx[0]); SB();
      vb[4] = pk2(acc[2][2][0], acc[2][2][1]); SB();
      sA[2] = mfma16(kf[1], qf[2], limA); SB();
      { const auto r_ = __builtin_amdgcn_permlane16_swap(__float_as_uint(mx[0]), __float_as_uint(mx[1]), false, false); mx[0] = __uint_as_float(r_[0]); mx[1] = __uint_as_float(r_[1]); } SB();
      mx[0] = fmx(mx[0], mx[1]); SB();
      GM(3, 2, wf1, af1); SB();
      mx[1] = mx[0] * -cs; SB();
      mx[0] = mulo(mx[0], -cs); SB();
      GM(3, 3, wf0, af0); SB();
      vb[5] = pk2(acc[2][2][2], acc[2][2][3]); SB();
      { const auto r_ = __builtin_amdgcn_permlane32_swap(__float_as_uint(mx[0]), __float_as_uint(mx[1]), false, false); mx[0] = __uint_as_float(r_[0]); mx[1] = __uint_as_float(r_[1]); } SB();
      GM(3, 1, wf0, af0); SB();
      l[2][0] = fmx(l[2][0], sA[2][0]); SB();
      l[2][1] = fmx(l[2][1], sA[2][1]); SB();
      GM(3, 0, wf0, af0); SB();
      l[2][2] = fmx(l[2][2], sA[2][2]); SB();
      l[2][3] = fmx(l[2][3], sA[2][3]); SB();
      GM(3, 2, wf0, af0); SB();
      mx[2] = fmx(fmx(l[2][0], l[2][1]), l[2][2]); SB();
      mx[2] = fmx(mx[2], l[2][3]); SB();
      GM(3, 3, wf2, af2); SB();
      ar[1][0] = __builtin_elementwise_fma((f32x2){l[1][0], l[1][1]}, c2, (f32x2){mx[1], mx[1]}); SB();
      ar[1][1] = __builtin_elementwise_fma((f32x2){l[1][2], l[1][3]}, c2, (f32x2){mx[1], mx[1]}); SB();
      GM(3, 1, wf2, af2); SB();
      ar[0][0] = __builtin_elementwise_fma((f32x2){l[0][0], l[0][1]}, c2, (f32x2){mx[0], mx[0]}); SB();
      ar[0][1] = __builtin_elementwise_fma((f32x2){l[0][2], l[0][3]}, c2, (f32x2){mx[0], mx[0]}); SB();
      GM(3, 0, wf2, af2); SB();
      ar[1][0][0] = __builtin_amdgcn_exp2f(ar[1][0][0]); SB();
      GM(3, 2, wf2, af2); SB();
      qf[3][2] = pk2(acc[3][3][0], acc[3][3][1]); SB();
      qf[3][3] = pk2(acc[3][3][2], acc[3][3][3]); SB();
      GM(4, 3, wf1, af1); SB();
      { const auto r_ = __builtin_amdgcn_permlane16_swap(qf[3][2], kz[3][0], false, false); qf[3][2] = r_[0]; kf[3][2] = r_[1]; } SB();
      kf[3][0] = pk2(acc[3][1][0], acc[3][1][1]); SB();
      GM(4, 1, wf1, af1); SB();
      qf[3][0] = pk2(acc[3][0][0], acc[3][0][1]); SB();
      { const auto r_ = __builtin_amdgcn_permlane16_swap(qf[3][3], kz[3][1], false, false); qf[3][3] = r_[0]; kf[3][3] = r_[1]; } SB();
      GM(4, 0, wf1, af1); SB();
      kf[3][1] = pk2(acc[3][1][2], acc[3][1][3]); SB();
      qf[3][1] = pk2(acc[3][0][2], acc[3][0][3]); SB();
      tx[3] = mfma16h((u32x2){qf[3][2], qf[3][3]}, sel[3], zero4); SB();
      ar[1][0][1] = __builtin_amdgcn_exp2f(ar[1][0][1]); SB();
      l[3] = mfma16(kf[3], qf[3], limB); SB();
      ar[1][1][0] = __builtin_amdgcn_exp2f(ar[1][1][0]); SB();
      sA[3] = mfma16(kf[2], qf[3], limA); SB();
      ar[1][1][1] = __builtin_amdgcn_exp2f(ar[1][1][1]); SB();
      GM(4, 2, wf1, af1); SB();
      ar[0][0][0] = __builtin_amdgcn_exp2f(ar[0][0][0]); SB();
      GM(4, 3, wf0, af0); SB();
      ar[0][0][1] = __builtin_amdgcn_exp2f(ar[0][0][1]); SB();
      GM(4, 1, wf0, af0); SB();
      ar[0][1][0] = __builtin_amdgcn_exp2f(ar[0][1][0]); SB();
      GM(4, 0, wf0, af0); SB();
      l[3][0] = fmx(l[3][0], sA[3][0]); SB();
      l[3][1] = fmx(l[3][1], sA[3][1]); SB();
      GM(4, 2, wf0, af0); SB();
      l[3][2] = fmx(l[3][2], sA[3][2]); SB();
      l[3][3] = fmx(l[3][3], sA[3][3]); SB();
      GM(4, 3, wf2, af2); SB();
      mx[3] = fmx(fmx(l[3][0], l[3][1]), l[3][2]); SB();
      mx[3] = fmx(mx[3], l[3][3]); SB();
      GM(4, 1, wf2, af2); SB();
      ar[0][1][1] = __builtin_amdgcn_exp2f(ar[0][1][1]); SB();
      GM(4, 0, wf2, af2); SB();
      { const auto r_ = __builtin_amdgcn_permlane32_swap(__float_as_uint(mx[2]), __float_as_uint(mx[3]), false, false); mx[2] = __uint_as_float(r_[0]); mx[3] = __uint_as_float(r_[1]); } SB();
      mx[2] = fmx(mx[2], mx[3]); SB();
      GM(4, 2, wf2, af2); SB();
      qf[4][2] = pk2(acc[4][3][0], acc[4][3][1]); SB();
      qf[4][3] = pk2(acc[4][3][2], acc[4][3][3]); SB();
      mx[3] = cpy(mx[2]); SB();
      { const auto r_ = __builtin_amdgcn_permlane16_swap(qf[4][2], kz[4][0], false, false); qf[4][2] = r_[0]; kf[4][2] = r_[1]; } SB();
      kf[4][0] = pk2(acc[4][1][0], acc[4][1][1]); SB();
      qf[4][0] = pk2(acc[4][0][0], acc[4][0][1]); SB();
      { const auto r_ = __builtin_amdgcn_permlane16_swap(qf[4][3], kz[4][1], false, false); qf[4][3] = r_[0]; kf[4][3] = r_[1]; } SB();
      kf[4][1] = pk2(acc[4][1][2], acc[4][1][3]); SB();
      qf[4][1] = pk2(acc[4][0][2], acc[4][0][3]); SB();
      tx[4] = mfma16h((u32x2){qf[4][2], qf[4][3]}, sel[0], zero4); SB();
      { const auto r_ = __builtin_amdgcn_permlane16_swap(__float_as_uint(mx[2]), __float_as_uint(mx[3]), false, false); mx[2] = __uint_as_float(r_[0]); mx[3] = __uint_as_float(r_[1]); } SB();
      mx[2] = fmx(mx[2], mx[3]); SB();
      l[4] = mfma16(kf[4], qf[4], limB); SB();
      mx[3] = mx[2] * -cs; SB();
      mx[2] = mulo(mx[2], -cs); SB();
      sA[4] = mfma16(kf[3], qf[4], limA); SB();
      pp[1][0] = pk2(ar[1][0][0], ar[1][0][1]); SB();
      { const auto r_ = __builtin_amdgcn_permlane32_swap(__float_as_uint(mx[2]), __float_as_uint(mx[3]), false, false); mx[2] = __uint_as_float(r_[0]); mx[3] = __uint_as_float(r_[1]); } SB();
      ar[2][0] = __builtin_elementwise_fma((f32x2){l[2][0], l[2][1]}, c2, (f32x2){mx[2], mx[2]}); SB();
      ar[2][1] = __builtin_elementwise_fma((f32x2){l[2][2], l[2][3]}, c2, (f32x2){mx[2], mx[2]}); SB();
      ar[2][0][0] = __builtin_amdgcn_exp2f(ar[2][0][0]); SB();
      ar[2][0][1] = __builtin_amdgcn_exp2f(ar[2][0][1]); SB();
      ar[2][1][0] = __builtin_amdgcn_exp2f(ar[2][1][0]); SB();
      ar[2][1][1] = __builtin_amdgcn_exp2f(ar[2][1][1]); SB();
      l[4][0] = fmx(l[4][0], sA[4][0]); SB();
      l[4][1] = fmx(l[4][1], sA[4][1]); SB();
      l[4][2] = fmx(l[4][2], sA[4][2]); SB();
      l[4][3] = fmx(l[4][3], sA[4][3]); SB();
      mx[4] = fmx(fmx(l[4][0], l[4][1]), l[4][2]); SB();
      mx[4] = fmx(mx[4], l[4][3]); SB();
      mx[5] = cpy(mx[4]); SB();
      pp[1][1] = pk2(ar[1][1][0], ar[1][1][1]); SB();
      pp[2][0] = pk2(ar[2][0][0], ar[2][0][1]); SB();
      { const auto r_ = __builtin_amdgcn_permlane32_swap(__float_as_uint(mx[4]), __float_as_uint(mx[5]), false, false); mx[4] = __uint_as_float(r_[0]); mx[5] = __uint_as_float(r_[1]); } SB();
      mx[4] = fmx(mx[4], mx[5]); SB();
      mx[5] = cpy(mx[4]); SB();
      pp[2][1] = pk2(ar[2][1][0], ar[2][1][1]); SB();
      pp[0][0] = pk2(ar[0][0][0], ar[0][0][1]); SB();
      { const auto r_ = __builtin_amdgcn_permlane16_swap(__float_as_uint(mx[4]), __float_as_uint(mx[5]), false, false); mx[4] = __uint_as_float(r_[0]); mx[5] = __uint_as_float(r_[1]); } SB();
      pp[0][1] = pk2(ar[0][1][0], ar[0][1][1]); SB();
      mx[4] = fmx(mx[4], mx[5]); SB();
      pf[1][0] = pp[1][0] & mA[0]; SB();
      sm[0] = mfma16h((u32x2){ones[0], ones[1]}, pp[0], zero4); SB();
      pf[1][2] = pp[1][0] & ~mA[0]; SB();
      pf[1][1] = pp[1][1] & mA[1]; SB();
      oA[0] = mfma16h((u32x2){vb[0], vb[1]}, pp[0], zero4); SB();
      pf[1][3] = pp[1][1] & ~mA[1]; SB();
      pf[2][0] = pp[2][0] & mA[0]; SB();
      oT[2] = mfma16h((u32x2){tb_[0], tb_[1]}, pp[0], zero4); SB();
      pf[2][2] = pp[2][0] & ~mA[0]; SB();
      pf[2][1] = pp[2][1] & mA[1]; SB();
      sm[1] = mfma16(ones, pf[1], zero4); SB();
      pf[2][3] = pp[2][1] & ~mA[1]; SB();
      mx[4] = mx[4] * -cs; SB();
      PIN(tb_); oT[0] = mfma16(__builtin_shufflevector(tb_, tb_, 0, 1, 2, 3), pf[1], zero4); SB();
      ar[3][0] = __builtin_elementwise_fma((f32x2){l[3][0], l[3][1]}, c2, (f32x2){mx[3], mx[3]}); SB();
      ar[3][1] = __builtin_elementwise_fma((f32x2){l[3][2], l[3][3]}, c2, (f32x2){mx[3], mx[3]}); SB();
      sm[2] = mfma16(ones, pf[2], zero4); SB();
      ar[4][0] = __builtin_elementwise_fma((f32x2){l[4][0], l[4][1]}, c2, (f32x2){mx[4], mx[4]}); SB();
      ar[4][1] = __builtin_elementwise_fma((f32x2){l[4][2], l[4][3]}, c2, (f32x2){mx[4], mx[4]}); SB();
      PIN(vb); oA[1] = mfma16(__builtin_shufflevector(vb, vb, 0, 1, 2, 3), pf[1], zero4); SB();
      ar[3][0][0] = __builtin_amdgcn_exp2f(ar[3][0][0]); SB();
      PIN(vb); oA[2] = mfma16(__builtin_shufflevector(vb, vb, 2, 3, 4, 5), pf[2], zero4); SB();
      ar[3][0][1] = __builtin_amdgcn_exp2f(ar[3][0][1]); SB();
      ar[3][1][0] = __builtin_amdgcn_exp2f(ar[3][1][0]); SB();
      ar[3][1][1] = __builtin_amdgcn_exp2f(ar[3][1][1]); SB();
      ar[4][0][0] = __builtin_amdgcn_exp2f(ar[4][0][0]); SB();
      ar[4][0][1] = __builtin_amdgcn_exp2f(ar[4][0][1]); SB();
      ar[4][1][0] = __builtin_amdgcn_exp2f(ar[4][1][0]); SB();
      ar[4][1][1] = __builtin_amdgcn_exp2f(ar[4][1][1]); SB();
      pp[3][0] = pk2(ar[3][0][0], ar[3][0][1]); SB();
      pp[3][1] = pk2(ar[3][1][0], ar[3][1][1]); SB();
      pp[4][0] = pk2(ar[4][0][0], ar[4][0][1]); SB();
      pp[4][1] = pk2(ar[4][1][0], ar[4][1][1]); SB();
      inv[0] = __builtin_amdgcn_rcpf(sm[0][0]); PIN(sm[0]); SB();
      inv[1] = __builtin_amdgcn_rcpf(sm[1][0]); PIN(sm[1]); SB();
      inv[2] = __builtin_amdgcn_rcpf(sm[2][0]); PIN(sm[2]); SB();
      pf[3][0] = pp[3][0] & mA[0]; SB();
      pf[3][2] = pp[3][0] & ~mA[0]; SB();
      pf[3][1] = pp[3][1] & mA[1]; SB();
      pf[3][3] = pp[3][1] & ~mA[1]; SB();
      pf[4][0] = pp[4][0] & mA[0]; SB();
      pf[4][2] = pp[4][0] & ~mA[0]; SB();
      sm[3] = mfma16(ones, pf[3], zero4); SB();
      pf[4][1] = pp[4][1] & mA[1]; SB();
      pf[4][3] = pp[4][1] & ~mA[1]; SB();
      tb_[4] = pk2(tx[2][0], tx[2][1]); SB();
      tb_[5] = pk2(tx[2][2], tx[2][3]); SB();
      sm[4] = mfma16(ones, pf[4], zero4); SB();
      nn0[0] = (f32x2){oA[0][0], oA[0][1]} * (f32x2){inv[0], inv[0]}; SB();
      nn0[1] = (f32x2){oA[0][2], oA[0][3]} * (f32x2){inv[0], inv[0]}; SB();
      PIN(tb_); oT[1] = mfma16(__builtin_shufflevector(tb_, tb_, 2, 3, 4, 5), pf[2], zero4); SB();
      ob[0][0] = pk2(nn0[0][0], nn0[0][1]); SB();
      ob[0][1] = pk2(nn0[1][0], nn0[1][1]); SB();
      nn1[0] = (f32x2){oA[1][0], oA[1][1]} * (f32x2){inv[1], inv[1]}; SB();
      nn1[1] = (f32x2){oA[1][2], oA[1][3]} * (f32x2){inv[1], inv[1]}; SB();
      nn2[0] = (f32x2){oA[2][0], oA[2][1]} * (f32x2){inv[2], inv[2]}; SB();
      nn2[1] = (f32x2){oA[2][2], oA[2][3]} * (f32x2){inv[2], inv[2]}; SB();
      vb[6] = pk2(acc[3][2][0], acc[3][2][1]); SB();
      vb[7] = pk2(acc[3][2][2], acc[3][2][3]); SB();
      vb[8] = pk2(acc[4][2][0], acc[4][2][1]); SB();
      vb[9] = pk2(acc[4][2][2], acc[4][2][3]); SB();
      PIN(vb); oA[3] = mfma16(__builtin_shufflevector(vb, vb, 4, 5, 6, 7), pf[3], zero4); SB();
      tb_[6] = pk2(tx[3][0], tx[3][1]); SB();
      tb_[7] = pk2(tx[3][2], tx[3][3]); SB();
      PIN(vb); oA[4] = mfma16(__builtin_shufflevector(vb, vb, 6, 7, 8, 9), pf[4], zero4); SB();
      tb_[8] = pk2(tx[4][0], tx[4][1]); SB();
      tb_[9] = pk2(tx[4][2], tx[4][3]); SB();
      PIN(tb_); oT[0] = mfma16(__builtin_shufflevector(tb_, tb_, 4, 5, 6, 7), pf[3], oT[0]); SB();
      ob[1][0] = pk2(nn1[0][0], nn1[0][1]); SB();
      ob[1][1] = pk2(nn1[1][0], nn1[1][1]); SB();
      PIN(tb_); oT[1] = mfma16(__builtin_shufflevector(tb_, tb_, 6, 7, 8, 9), pf[4], oT[1]); SB();
      ob[2][0] = pk2(nn2[0][0], nn2[0][1]); SB();
      ob[2][1] = pk2(nn2[1][0], nn2[1][1]); SB();
      ob[5][0] = cpyu(ob[0][0]); SB();
      ob[5][1] = cpyu(ob[0][1]); SB();
      nnU[0] = (f32x2){oT[2][0], oT[2][1]} * (f32x2){inv[0], inv[0]}; SB();
      nnU[1] = (f32x2){oT[2][2], oT[2][3]} * (f32x2){inv[0], inv[0]}; SB();
      obU[0] = pk2(nnU[0][0], nnU[0][1]); SB();
      obU[1] = pk2(nnU[1][0], nnU[1][1]); SB();
      { const auto r_ = __builtin_amdgcn_permlane32_swap(ob[0][0], ob[5][0], false, false); os[0][0] = r_[0]; os[0][2] = r_[1]; } SB();
      { const auto r_ = __builtin_amdgcn_permlane32_swap(ob[0][1], ob[5][1], false, false); os[0][1] = r_[0]; os[0][3] = r_[1]; } SB();
      { const auto r_ = __builtin_amdgcn_permlane32_swap(ob[1][0], ob[2][0], false, false); os[1][0] = r_[0]; os[1][2] = r_[1]; } SB();
      { const auto r_ = __builtin_amdgcn_permlane32_swap(ob[1][1], ob[2][1], false, false); os[1][1] = r_[0]; os[1][3] = r_[1]; } SB();
      inv[3] = __builtin_amdgcn_rcpf(sm[3][0]); PIN(sm[3]); SB();
      inv[4] = __builtin_amdgcn_rcpf(sm[4][0]); PIN(sm[4]); SB();
      it = selm(mo, inv[2], inv[1]); SB();
      iu = selm(mo, inv[4], inv[3]); SB();
      nn3[0] = (f32x2){oA[3][0], oA[3][1]} * (f32x2){inv[3], inv[3]}; SB();
      nn3[1] = (f32x2){oA[3][2], oA[3][3]} * (f32x2){inv[3], inv[3]}; SB();
      nn4[0] = (f32x2){oA[4][0], oA[4][1]} * (f32x2){inv[4], inv[4]}; SB();
      nn4[1] = (f32x2){oA[4][2], oA[4][3]} * (f32x2){inv[4], inv[4]}; SB();
      it = selm(mh, iu, it); SB();
      oT[0][0] = selm(mo, oT[1][0], oT[0][0]); SB();
      oT[0][1] = selm(mo, oT[1][1], oT[0][1]); SB();
      oT[0][2] = selm(mo, oT[1][2], oT[0][2]); SB();
      oT[0][3] = selm(mo, oT[1][3], oT[0][3]); SB();
      ob[3][0] = pk2(nn3[0][0], nn3[0][1]); SB();
      ob[3][1] = pk2(nn3[1][0], nn3[1][1]); SB();
      ob[4][0] = pk2(nn4[0][0], nn4[0][1]); SB();
      ob[4][1] = pk2(nn4[1][0], nn4[1][1]); SB();
      nnT[0] = (f32x2){oT[0][0], oT[0][1]} * (f32x2){it, it}; SB();
      nnT[1] = (f32x2){oT[0][2], oT[0][3]} * (f32x2){it, it}; SB();
      obT[0] = pk2(nnT[0][0], nnT[0][1]); SB();
      obT[1] = pk2(nnT[1][0], nnT[1][1]); SB();
      { const auto r_ = __builtin_amdgcn_permlane32_swap(ob[3][0], ob[4][0], false, false); os[2][0] = r_[0]; os[2][2] = r_[1]; } SB();
      { const auto r_ = __builtin_amdgcn_permlane32_swap(ob[3][1], ob[4][1], false, false); os[2][1] = r_[0]; os[2][3] = r_[1]; } SB();
      XBAR(); SB();
      __builtin_amdgcn_raw_buffer_store_b128(os[1], ro, od[1], 0, 16); SB();
      __builtin_amdgcn_raw_buffer_store_b128(os[2], ro, od[2], 0, 16); SB();
      __builtin_amdgcn_raw_buffer_store_b64(obT, ro, od[3], 0, 16); SB();
      __builtin_amdgcn_raw_buffer_store_b128(os[0], ro, od[0], 0, 16); SB();
      __builtin_amdgcn_raw_buffer_store_b64(obU, ro, od[4], 0, 16); SB();
    }
  } else {
    const int f = b - 224;
    args();
    float* red = (float*)smem;
    u16* hs = (u16*)(smem + 32768);
    const bool do1 = f < 27;
    const bool lead = f == 31;
    if (__builtin_expect(lead, 0)) {
      get_tag();
      const __amdgpu_buffer_rsrc_t rk = __builtin_amdgcn_make_buffer_rsrc((void*)(ws + WS_K), 0, 36864, 0x00020000);
      u32x2 kv[2];
      int ko[2];
      ko[0] = (tid >> 2) * 128 + (tid & 3) * 16;
      ko[1] = tid < 384 ? (128 + (tid >> 2)) * 128 + (tid & 3) * 16 : (224 + ((tid - 384) >> 1)) * 128 + (tid & 1) * 16;
      kv[0] = kv[1] = (u32x2){0u, 0u};
#pragma unroll 1
      for (unsigned spins = 0; spins < SPIN_CAP; ++spins) {
        asm volatile("" ::: "memory");
        u32x4 t_[2];
#pragma unroll
        for (int q = 0; q < 2; ++q) t_[q] = __builtin_amdgcn_raw_buffer_load_b128(rk, ko[q], 0, 16);
        bool ok = true;
#pragma unroll
        for (int q = 0; q < 2; ++q)
          if (ko[q] != 0x7FFFFF00) {
            if ((t_[q][1] == tag) & (t_[q][3] == tag)) {
              kv[q] = (u32x2){t_[q][0], t_[q][2]};
              ko[q] = 0x7FFFFF00;
            } else {
              ok = false;
            }
          }
        if (__all(ok)) break;
        __builtin_amdgcn_s_sleep(4);
      }
      unsigned k1 = max(kv[0][0], kv[0][1]), k2 = min(kv[0][0], kv[0][1]);
      top2_mrg(k1, k2, max(kv[1][0], kv[1][1]), min(kv[1][0], kv[1][1]));
      top2_all(k1, k2);
      unsigned* lk = (unsigned*)(smem + L_VAR + 64);
      if (lane == 0) *(u32x2*)(lk + wave * 2) = (u32x2){k1, k2};
      WAITL0();
      BARRIER();
      k1 = 0u;
      k2 = 0u;
#pragma unroll
      for (int q = 0; q < 4; ++q) {
        const u32x4 a_ = *(const u32x4*)(lk + q * 4);
        top2_mrg(k1, k2, a_[0], a_[1]);
        top2_mrg(k1, k2, a_[2], a_[3]);
      }
      if (tid == 0) {
        const __amdgpu_buffer_rsrc_t rr = __builtin_amdgcn_make_buffer_rsrc((void*)(ws + WS_R), 0, 128, 0x00020000);
        __builtin_amdgcn_raw_buffer_store_b128((u32x4){k1, tag, k2, tag}, rr, 0, 0, 16);
        *(u32x2*)(smem + L_VAR + 32) = (u32x2){k1, k2};
      }
    }
    if (__builtin_expect(f == 30, 0)) {
      get_tag();
      if (wave < 4) {
        const __amdgpu_buffer_rsrc_t rf = __builtin_amdgcn_make_buffer_rsrc((void*)(ws + WS_F2), 0, 32768, 0x00020000);
        int fo = tid < 227 ? tid * 128 : 0x7FFFFF00;
#pragma unroll 1
        for (unsigned spins = 0; spins < SPIN_CAP; ++spins) {
          asm volatile("" ::: "memory");
          if (__builtin_amdgcn_raw_buffer_load_b32(rf, fo, 0, 16) == tag) fo = 0x7FFFFF00;
          if (__all(fo == 0x7FFFFF00)) break;
          __builtin_amdgcn_s_sleep(4);
        }
      }
      BARRIER();
      if (tid == 0) {
        const __amdgpu_buffer_rsrc_t rr = __builtin_amdgcn_make_buffer_rsrc((void*)(ws + WS_R), 0, 256, 0x00020000);
        const unsigned t_ = now();
        __builtin_amdgcn_raw_buffer_store_b128((u32x4){t_, tag, t_, tag}, rr, 128, 0, 16);
        *(u32x4*)(smem + L_CAND2) = (u32x4){t_, tag, t_, tag};
      }
    }
    if (__builtin_expect(f >= 27 && f < 30, 0)) {
      get_tag();
#pragma unroll 1
      for (int c = wave * 3 + f - 27; c < 320; c += 24)
        __builtin_amdgcn_raw_buffer_store_b128((u32x4){SENT, SENT, SENT, SENT}, ro, lane * 16, (c >= 160 ? 112 * 20480 : 56 * 20480) + c * 1024, 16);
      WAITV(0);
      BARRIER();
      if (tid == 0) __hip_atomic_store((gu32*)(ws + WS_F2 + (197 + f) * 128), tag, RLX_AGENT);
    }
    const int g0 = wave < 4 ? wave * 3 : 4 + wave * 2, ng = wave < 4 ? 3 : 2;
    u32x4 r1[3][8], wd[2][8];
    int vo2 = (wave - 3) * 256 + c15 * 16 + g4 * 8 * (DM * 2);
    PIN(vo2);
    if (do1) {
      const char* pa = (const char*)(c15 < 8 ? w_gate : w_up) + f * 128 + (c15 & 7) * 16 + (g0 * 32 + g4 * 8) * (FF * 2);
      const GAS char* pp[12];
#pragma unroll
      for (int j = 0; j < 12; ++j) {
        pp[j] = (const GAS char*)pa + ((j >> 2) * 32 + (j & 3) * 2) * (FF * 2) + FF;
        PIN(pp[j]);
      }
      SB();
#pragma unroll
      for (int j = 0; j < 24; ++j)
        if (j < 16 || wave < 4) r1[j >> 3][j & 7] = __builtin_nontemporal_load((const GAS u32x4*)(pp[j >> 1] + ((j & 1) ? FF : -FF)));
    }
    SB();
    BARRIER();
    if (do1 && wave >= 3) {
      const u32x4 r = make_srd(w_down, DM * FF * 2);
#pragma unroll
      for (int j = 0; j < 16; ++j)
        asm volatile("buffer_load_dwordx4 %0, %1, %2, %3 offen nt" : "=v"(wd[j >> 3][j & 7]) : "v"(vo2), "s"(r), "s"((f * 64 + (j >> 3) * 32 + (j & 7)) * (DM * 2)) : "memory");
    }
    SB();
    if (f < 30) get_tag();
    prepB();
    auto transp = [&](u32x4(&w)[8]) {
      u32x4 t[8];
#pragma unroll
      for (int d = 0; d < 4; ++d)
#pragma unroll
        for (int p = 0; p < 4; ++p) {
          t[2 * d][p] = perm_lo(w[2 * p + 1][d], w[2 * p][d]);
          t[2 * d + 1][p] = perm_hi(w[2 * p + 1][d], w[2 * p][d]);
        }
#pragma unroll
      for (int j = 0; j < 8; ++j) {
        w[j] = t[j];
        PIN(w[j]);
      }
    };
    if (do1) {
      transp(r1[0]);
      transp(r1[1]);
      if (wave < 4) transp(r1[2]);
    }
    __builtin_amdgcn_s_waitcnt(0x0F70);
    if (wave == 0 && !lead) {
      res_wait(ws, smem, lane, tag, 0);
      if (do1) {
        WAITL0();
        const u32x2 kq = *(const u32x2*)(smem + L_VAR + 32);
#pragma unroll
        for (int t = 0; t < 2; ++t) {
          const u16* xr = x + (2047 - (int)(__builtin_amdgcn_readfirstlane(kq[t]) & 2047u)) * DM;
          dma16(lane * 16, xr, lds0 + 36864 + t * 1296);
          if (lane < 16) dma16(lane * 16, xr + 512, lds0 + 36864 + t * 1296 + 1024);
        }
      }
    }
    if (wave == 1) touch_line((const char*)out + tile_off((lane & 31) + (nt2 & 1) * 32) + ((nt2 & 1) ? 0 : 128), lds0 + L_TOUCH + 512);
    if (wave == 1 && f != 30 && !do1) res_wait(ws, smem, lane, tag, 1);
    if (wave < 2) WAITV(0);
    WAITL0();
    BARRIER();
    const unsigned r0_ = now(), c0_ = (unsigned)__builtin_amdgcn_s_memtime();
    if (tid == 128) __hip_atomic_store((gu32*)(ws + WS_A + f * 128), tag, RLX_AGENT);
    const __amdgpu_buffer_rsrc_t ryp = __builtin_amdgcn_make_buffer_rsrc((void*)(ws + WS_YP), 0, 27 * 10240, 0x00020000);
    auto pick = [&](const f32x4(&a)[8]) {
      f32x4 v;
#pragma unroll
      for (int q = 0; q < 4; ++q) {
        const float lo = (g4 & 1) ? a[2 + (q >> 1)][q & 1] : a[q >> 1][q & 1], hi = (g4 & 1) ? a[6 + (q >> 1)][q & 1] : a[4 + (q >> 1)][q & 1];
        v[q] = (g4 & 2) ? hi : lo;
      }
      return v;
    };
    if (do1) {
      u32x4 xf[3];
#pragma unroll
      for (int k = 0; k < 3; ++k) xf[k] = *(const u32x4*)(smem + 36864 + (c15 & 1) * 1296 + ((g0 + (k < ng ? k : 0)) * 32 + g4 * 8) * 2);
      f32x4 acc[8];
#pragma unroll
      for (int ff = 0; ff < 8; ++ff) acc[ff] = (f32x4){0.f, 0.f, 0.f, 0.f};
#pragma unroll
      for (int k = 0; k < 3; ++k)
        if (k < ng) {
#pragma unroll
          for (int j = 0; j < 8; ++j) acc[j] = mfma16(xf[k], r1[k][j], acc[j]);
        }
      *(f32x4*)(red + wave * 256 + c15 * 16 + g4 * 4) = pick(acc);
      WAITL0();
      BARRIER();
      u32x4 wt[2][8];
      if (wave < 2) {
        float gs = 0.f, us = 0.f;
#pragma unroll
        for (int w = 0; w < 8; ++w) {
          gs += red[w * 256 + tid];
          us += red[w * 256 + 128 + tid];
        }
        const float gv = rbf(gs), uv = rbf(us);
        const float hv = rbf(gv * __builtin_amdgcn_rcpf(1.0f + __expf(-gv))) * uv;
        hs[(tid & 1) * 64 + (tid >> 1)] = (u16)pk2(hv, hv);
      } else if (wave >= 3) {
        WAITV(0);
#pragma unroll
        for (int j = 0; j < 16; ++j) {
          wt[j >> 3][j & 7] = wd[j >> 3][j & 7];
          PIN(wt[j >> 3][j & 7]);
        }
        transp(wt[0]);
        transp(wt[1]);
      }
      WAITL0();
      BARRIER();
      if (wave == 2) res_wait(ws, smem, lane, tag, 1);
      if (wave >= 3) {
        const u32x4 h0 = *(const u32x4*)(hs + (c15 & 1) * 64 + g4 * 8), h1 = *(const u32x4*)(hs + (c15 & 1) * 64 + 32 + g4 * 8);
#pragma unroll
        for (int j = 0; j < 8; ++j) acc[j] = mfma16(h0, wt[0][j], (f32x4){0.f, 0.f, 0.f, 0.f});
#pragma unroll
        for (int j = 0; j < 8; ++j) acc[j] = mfma16(h1, wt[1][j], acc[j]);
        *(f32x4*)(red + 2048 + wave * 256 + c15 * 16 + g4 * 4) = pick(acc);
        const f32x4 y = *(const f32x4*)(red + 2048 + wave * 256 + lane * 4);
#pragma unroll
        for (int t = 0; t < 2; ++t)
          __builtin_amdgcn_raw_buffer_store_b128((u32x4){__float_as_uint(y[t]), tag, __float_as_uint(y[2 + t]), tag}, ryp,
                                                 ((f * 2 + t) * 640 + (wave - 3) * 128 + lane * 2) * 8, 0, 16);
      }
      WAITL0();
      BARRIER();
    }
    wo_dma(IC<3>{}, wave, 8, 25);
    if (wave == 0) wo_dma(IC<1>{}, 24, 8, 25);
    if (wave == 1) gate_dma(gate_off(lane));
    if (tid < 8) *(unsigned*)(smem + L_VAR + tid * 4) = 0u;
    if (wave == 2 || (f == 30 && wave == 3)) {
      const unsigned tp = __builtin_amdgcn_readfirstlane(*(const unsigned*)(smem + L_CAND2)), d = wave == 2 ? D1 : DK;
#pragma unroll 1
      for (unsigned spins = 0; spins < 128; ++spins) {
        const unsigned r = now(), c = (unsigned)__builtin_amdgcn_s_memtime();
        if ((r - tp) * (c - c0_) > d * (r - r0_ + 1)) break;
        __builtin_amdgcn_s_sleep(2);
      }
    }
    if (f == 30 && wave == 3) {
      const __amdgpu_buffer_rsrc_t ra = __builtin_amdgcn_make_buffer_rsrc((void*)(ws + WS_A), 0, 4096, 0x00020000);
#pragma unroll 1
      for (unsigned spins = 0; spins < SPIN_CAP; ++spins) {
        asm volatile("" ::: "memory");
        if (__all(__builtin_amdgcn_raw_buffer_load_b32(ra, (lane & 31) * 128, 0, 16) == tag)) break;
        __builtin_amdgcn_s_sleep(8);
      }
      if (lane == 0) __hip_atomic_store((gu32*)(ws + WS_E), tag, RLX_AGENT);
    }
    if (wave == 2) {
      const unsigned t_ = now() + D2;
      const u32x4 v = __builtin_amdgcn_raw_buffer_load_b128(rfl, flag_off(), 0, 16);
      *(u32x4*)(smem + L_FS + lane * 16) = (u32x4){(v[1] == tag ? v[0] : t_) + (ETA_F - ETA_G), tag, 0u, tag};
    }
    if (wave == 0) rows_job();
    WAITV(0);
    WAITL0();
    BARRIER();
  }

  {
    const int m = wave & 3, kh = wave >> 2;
    const bool snd = kh != 0;
    const int oo = (rt2 * 4 + m) * 20480 + kh * 8192, ot = (rt2 * 4 + m) * 20480 + 16384 + kh * 2048;
    const int l16 = lane * 16;
    u32x4 fs = *(const u32x4*)(smem + L_FS + l16);
    const u32x4 kk = *(const u32x4*)(smem + L_VAR + 32);
    u32x2 g0_ = *(const u32x2*)(smem + glo);
    u32x4 yw[2][7];
    int yo, y6, yp, yx;
    auto y_prep = [&]() {
      int ly = lane;
      PIN(ly);
      const int cp = (ly & 15) < 10 ? ly & 15 : 0, col = m * 20 + cp * 2 + (ly >> 5);
      yo = (ly >> 4) * 71680 + (nt2 * 80 + m * 20 + cp * 2) * 8;
      y6 = ly >= 48 ? 0x7FFFFF00 : yo;
      yp = (((col >> 4) + 5 - (nt2 & 1)) % 5) * 1024 + (col & 12) * 64 + (col & 3) * 4;
      yx = L_XT + col * 2;
      PIN(yo); PIN(y6); PIN(yp); PIN(yx);
    };
    auto y_srd = [&](int tok) {
      char* wy = ws;
      asm volatile("" : "+s"(wy));
      return make_srd(wy + WS_YP + tok * 5120, 27 * 10240);
    };
    auto y_issue = [&](u32x4(&yv)[7], u32x4 srd, bool again, int j0 = 0, int j1 = 7) {
#pragma unroll
      for (int j = 0; j < 7; ++j) {
        if (j < j0 || j >= j1) continue;
        if (again) asm volatile("buffer_load_dwordx4 %0, %1, %2, %3 offen sc1" : "+v"(yv[j]) : "v"(j < 6 ? yo : y6), "s"(srd), "s"(j * 10240) : "memory");
        else asm volatile("buffer_load_dwordx4 %0, %1, %2, %3 offen sc1" : "=v"(yv[j]) : "v"(j < 6 ? yo : y6), "s"(srd), "s"(j * 10240) : "memory");
      }
    };
    auto y_done = [&](u32x4(&yv)[7], int tok, int row) {
      volatile LAS unsigned* fl = (volatile LAS unsigned*)(LAS char*)(smem + L_VAR + 16 + (row >> 4) * 4);
      volatile LAS float* pe = (volatile LAS float*)(LAS char*)(smem + yp + (row >> 4) * 5120 + (row & 15) * 16);
      unsigned f_, xv_;
      float omg, pv;
      auto rd = [&]() {
        f_ = *fl;
        omg = *(volatile LAS float*)(LAS char*)(smem + L_JUNK + row * 4);
        xv_ = *(volatile LAS u16*)(LAS char*)(smem + yx + row * 160);
        pv = *pe;
      };
      rd();
      const float rsel = *(const float*)(smem + L_JUNK + 256 + tok * 4);
      SB();
      unsigned lo = tag, hi = tag;
      float y[2] = {0.f, 0.f};
#pragma unroll
      for (int j = 0; j < 7; ++j) {
        PIN(yv[j]);
        if (j == 6 && g4 == 3) yv[j][1] = yv[j][3] = tag;
        lo = min(min(lo, yv[j][1]), yv[j][3]);
        hi = max(max(hi, yv[j][1]), yv[j][3]);
        y[0] += __uint_as_float(yv[j][0]);
        PIN(y[0]);
        y[1] += __uint_as_float(yv[j][2]);
        PIN(y[1]);
      }
      if (!__all(lo == hi)) return false;
      const auto a = __builtin_amdgcn_permlane32_swap(__float_as_uint(y[0]), __float_as_uint(y[1]), false, false);
      const float h = __uint_as_float(a[0]) + __uint_as_float(a[1]);
      const auto c = __builtin_amdgcn_permlane16_swap(__float_as_uint(h), __float_as_uint(h), false, false);
      const float r = rbf(rbf(__uint_as_float(c[0]) + __uint_as_float(c[1])) * rsel);
#pragma unroll 1
      for (unsigned spins = 0; spins < (SPIN_CAP << 6) && !__builtin_amdgcn_readfirstlane(f_); ++spins) rd();
      const float xv = bflo(xv_);
      *pe = pv + (rbf(xv + r) - xv) * omg;
      asm volatile("ds_add_u32 %0, %1" ::"v"(fl), "v"(1u) : "memory");
      return true;
    };
    const int l0 = __builtin_amdgcn_readfirstlane(kk[2]), l1 = __builtin_amdgcn_readfirstlane(kk[3]);
    unsigned ry = min((unsigned)l0, (unsigned)l1);
    unsigned need = 1u + ((l0 >> 4) == m ? 256u : 0u) + ((l1 >> 4) == m ? 256u : 0u);
    asm volatile("" : "+s"(ry), "+s"(need));
#define yjob (ry < 64u)
    if (wave == 7) {
      if (__builtin_expect((~__builtin_amdgcn_ballot_w64((fs[1] == tag) & (fs[3] == tag)) & 0xFFull) != 0, 0)) {
        flag_wait(lane >= 8 ? 0x7FFFFF00 : flag_off());
        WAITL0();
        if (lane == 0) *(volatile LAS unsigned*)(LAS char*)(smem + L_VAR + 8) = 1u;
      }
    }
    if (!snd) {
      dma16(adr[0], x, lds0 + L_XT + m * 2560);
      dma16(adr[1], x, lds0 + L_XT + m * 2560 + 1024);
      if (lane < 32) dma16(adr[2], x, lds0 + L_XT + m * 2560 + 2048);
    }
    SB();
    const char* wb = smem + L_WO + kh * 51200 + l16;
    u32x4 wq[WPD][5];
    auto wrd = [&](int k) {
#pragma unroll
      for (int f = 0; f < 5; ++f) wq[k % WPD][f] = *(const u32x4*)(wb + (k * 5 + f) * 1024);
    };
#pragma unroll
    for (int k = 0; k < NPRE; ++k) wrd(k);
    if (snd) {
#pragma unroll
      for (int k = NPRE; k < WPD; ++k) wrd(k);
    }
    if (__builtin_expect(!__all(g0_[1] == tag), 0)) {
      const __amdgpu_buffer_rsrc_t rg_ = __builtin_amdgcn_make_buffer_rsrc((void*)(ws + WS_K), 0, 36864, 0x00020000);
#pragma unroll 1
      for (unsigned spins = 0; spins < SPIN_CAP; ++spins) {
        asm volatile("" ::: "memory");
        g0_ = __builtin_amdgcn_raw_buffer_load_b64(rg_, gate_off(m * 16 + c15), 0, 16);
        if (__all(g0_[1] == tag)) break;
      }
    }
    const float gt = __uint_as_float(g0_[0]);
    SB();
    {
      const u64 inv = ~__builtin_amdgcn_ballot_w64((fs[1] == tag) & (fs[3] == tag));
      if (__builtin_expect((inv & rel) != 0, 0)) {
#pragma unroll 1
        for (unsigned spins = 0; spins < (SPIN_CAP << 6); ++spins) {
          if (__builtin_amdgcn_readfirstlane(*(volatile LAS unsigned*)(LAS char*)(smem + L_VAR + 8))) break;
          __builtin_amdgcn_s_sleep(1);
        }
        fs[0] = *(volatile LAS unsigned*)(LAS char*)(smem + L_FS + l16);
      }
      const unsigned tg = fs[0] + ETA_G;
      unsigned spins = 128;
#pragma unroll 1
      while ((__builtin_amdgcn_ballot_w64((int)(now() - tg) < 0) & rel) != 0 && --spins) {}
    }
    u32x4 of[8];
    u32x2 ofl[2], ofh[2];
    const u32x4 sro = make_srd(ws + WS_OF, S_LEN * DM * 2);
    int l8 = lane * 8;
    asm volatile("buffer_load_dwordx4 %0, %8, %9, %10 offen sc1\n\tbuffer_load_dwordx4 %1, %8, %9, %10 offen offset:1024 sc1\n\t"
                 "buffer_load_dwordx4 %2, %8, %9, %10 offen offset:2048 sc1\n\tbuffer_load_dwordx4 %3, %8, %9, %10 offen offset:3072 sc1\n\t"
                 "buffer_load_dwordx4 %4, %8, %9, %11 offen sc1\n\tbuffer_load_dwordx4 %5, %8, %9, %11 offen offset:1024 sc1\n\t"
                 "buffer_load_dwordx4 %6, %8, %9, %11 offen offset:2048 sc1\n\tbuffer_load_dwordx4 %7, %8, %9, %11 offen offset:3072 sc1"
                 : "=&v"(of[0]), "=&v"(of[1]), "=&v"(of[2]), "=&v"(of[3]), "=&v"(of[4]), "=&v"(of[5]), "=&v"(of[6]), "=&v"(of[7])
                 : "v"(l16), "s"(sro), "s"(oo), "s"(oo + 4096)
                 : "memory");
    asm volatile("buffer_load_dwordx2 %0, %4, %5, %6 offen sc1\n\tbuffer_load_dwordx2 %1, %4, %5, %6 offen offset:512 sc1\n\t"
                 "buffer_load_dwordx2 %2, %4, %5, %6 offen offset:1024 sc1\n\tbuffer_load_dwordx2 %3, %4, %5, %6 offen offset:1536 sc1"
                 : "=&v"(ofl[0]), "=&v"(ofh[0]), "=&v"(ofl[1]), "=&v"(ofh[1])
                 : "v"(l8), "s"(sro), "s"(ot)
                 : "memory");
    if (!snd) {
#pragma unroll
      for (int k = NPRE; k < WPD; ++k) wrd(k);
    }
    auto o_again = [&](int k) {
      if (k < 8)
        asm volatile("buffer_load_dwordx4 %0, %1, %2, %3 offen sc1" : "+v"(of[k]) : "v"(l16), "s"(sro), "s"(oo + k * 1024) : "memory");
      else
        asm volatile("buffer_load_dwordx2 %0, %2, %3, %4 offen sc1\n\tbuffer_load_dwordx2 %1, %2, %3, %4 offen offset:512 sc1"
                     : "+v"(ofl[k - 8]), "+v"(ofh[k - 8])
                     : "v"(l8), "s"(sro), "s"(ot + (k - 8) * 1024)
                     : "memory");
    };
    auto o_pin = [&](int k) {
      if (k < 8) PIN(of[k]);
      else asm volatile("" : "+v"(ofl[k - 8]), "+v"(ofh[k - 8]));
    };
    auto o_get = [&](int k) { return k < 8 ? of[k] : (u32x4){ofl[k - 8][0], ofl[k - 8][1], ofh[k - 8][0], ofh[k - 8][1]}; };
    auto o_bad = [&](int k) {
      const u32x4 v = o_get(k);
      return __any(max(max(v[0], v[1]), max(v[2], v[3])) == SENT);
    };
    int pa_ = m * 5120 + l16;
    PIN(pa_);
    volatile LAS unsigned* pfl = (volatile LAS unsigned*)(LAS char*)(smem + L_VAR + 16 + m * 4);
    u32x4 rout = make_srd(out, S_LEN * DM * 2);
    asm volatile("" : "+s"(rout));
    f32x4 ac2[5], pp[5];
    unsigned pf_ = 0;
    if (!snd) {
      u32x2 xb[5];
      if (__builtin_expect(yjob, 0)) y_prep();
      WAITV(12);
#pragma unroll
      for (int f = 0; f < 5; ++f) xb[f] = *(const u32x2*)(smem + L_XT + m * 2560 + c15 * 160 + g4 * 8 + ((nt2 & 1) ? (f + 1) % 5 : f) * 32);
      const float omg = 1.0f - gt;
      *(float*)(smem + L_JUNK + (m * 16 + c15) * 4) = omg;
#pragma unroll
      for (int f = 0; f < 5; ++f) {
        pp[f] = (f32x4){bflo(xb[f][0]), bfhi(xb[f][0]), bflo(xb[f][1]), bfhi(xb[f][1])} * omg;
        PIN(pp[f]);
      }
    }
#pragma unroll
    for (int f = 0; f < 5; ++f) ac2[f] = (f32x4){0.f, 0.f, 0.f, 0.f};
    __builtin_amdgcn_sched_barrier(0);
    constexpr int GS[5] = {0, 2, 5, 8, 10};
    auto vleft = [](int k) { return k < 8 ? 11 - k : k == 8 ? 2 : 0; };
    WAITV(10);
    o_pin(0);
    o_pin(1);
    bool bad = o_bad(0) | o_bad(1);
#pragma unroll
    for (int g = 0; g < 4; ++g) {
      if (__builtin_expect(bad, 0)) {
        WAITV(0);
#pragma unroll 1
        for (unsigned spins = 0; spins < SPIN_CAP; ++spins) {
          bool any = false;
#pragma unroll
          for (int k2 = GS[g]; k2 < 10; ++k2) {
            o_pin(k2);
            if (o_bad(k2)) {
              o_again(k2);
              if (k2 < GS[g + 1]) any = true;
            }
          }
          WAITV(0);
          if (!any) break;
        }
      }
      if (g == 3) break;
      unsigned mxv = 0;
      const int nm = (GS[g + 1] - GS[g]) * 5, nn = g < 3 ? GS[g + 2] - GS[g + 1] : 0;
#pragma unroll
      for (int i = 0; i < nm; ++i) {
        const int k = GS[g] + i / 5, f = i % 5;
        const int j = i - (nm - 2 - 2 * nn);
        const int kn = GS[g + 1] + j / 2;
        if (j >= 0 && j < 2 * nn && !(j & 1)) {
          WAITV_(vleft(kn));
          o_pin(kn);
          SB();
        }
        ac2[f] = mfma16(wq[k % WPD][f], o_get(k), ac2[f]);
        SB();
        if (k + WPD < 10) wq[k % WPD][f] = *(const u32x4*)(wb + ((k + WPD) * 5 + f) * 1024);
        if (j >= 0 && j < 2 * nn) {
          const u32x4 nv = o_get(kn);
          mxv = max(max(mxv, nv[(j & 1) * 2]), nv[(j & 1) * 2 + 1]);
        }
        if (nn && j == 2 * nn) bad = __any(mxv == SENT);
        if (g == 2 && i == PRD - 1) __builtin_amdgcn_s_waitcnt(0xC07F);
        if (g == 2 && i == PRD && snd) {
          pf_ = *pfl;
          asm volatile("" ::: "memory");
#pragma unroll
          for (int f = 0; f < 5; ++f) pp[f] = *(const f32x4*)(smem + pa_ + f * 1024);
        }
        SB();
      }
    }
    u32x4 o8 = o_get(8), o9 = o_get(9);
    PIN(o8); PIN(o9);
    auto mm = [&](int f) {
      ac2[f] = mfma16(wq[8 % WPD][f], o8, ac2[f]);
      SB();
      ac2[f] = mfma16(wq[9 % WPD][f], o9, ac2[f]);
      SB();
    };
    auto fin = [&](int f, int q) {
      pp[f][q] = __builtin_fmaf(ac2[f][q], gt, pp[f][q]);
      PIN(pp[f][q]);
    };
    if (!snd) {
      SB();
      auto wr = [&](int f) {
#pragma unroll
        for (int q = 0; q < 4; ++q) fin(f, q);
        *(f32x4*)(smem + pa_ + f * 1024) = pp[f];
        SB();
      };
      auto done = [&]() {
        asm volatile("" ::: "memory");
        *pfl = 1u;
      };
      if (__builtin_expect(yjob, 0)) {
        int tok = (unsigned)l0 >= 64u;
        const u32x4 sy = y_srd(tok);
        mm(0); y_issue(yw[0], sy, false, 0, 2); SB(); mm(1); y_issue(yw[0], sy, false, 2, 4); SB(); mm(2); y_issue(yw[0], sy, false, 4, 7); SB();
        if (max((unsigned)l0, (unsigned)l1) < 64u) y_issue(yw[1], y_srd(1), false);
        wr(0); mm(3); wr(1); mm(4); wr(2); wr(3); wr(4);
        done();
        WAITV(0);
#pragma unroll
        for (int j = 0; j < 7; ++j) PIN(yw[1][j]);
#pragma unroll 1
        for (unsigned spins = 0; spins < SPIN_CAP; ++spins) {
          if (y_done(yw[0], tok, tok ? l1 : l0)) {
            if (tok || (unsigned)l1 >= 64u) break;
            tok = 1;
#pragma unroll
            for (int j = 0; j < 7; ++j) yw[0][j] = yw[1][j];
          } else {
            y_issue(yw[0], y_srd(tok), true);
            WAITV(0);
          }
        }
      } else {
        mm(0); mm(1); mm(2);
        wr(0); mm(3); wr(1); mm(4);
        wr(2); wr(3); wr(4);
        done();
      }
    } else {
      SB();
      u32x2 r[5];
      auto cv = [&](int f) {
#pragma unroll
        for (int q = 0; q < 4; ++q) fin(f, q);
        r[f] = (u32x2){pk2(pp[f][0], pp[f][1]), pk2(pp[f][2], pp[f][3])};
        SB();
      };
      auto st = [&](int f) {
        const auto a = __builtin_amdgcn_permlane16_swap(r[f][0], r[f + 1][0], false, false);
        const auto c = __builtin_amdgcn_permlane16_swap(r[f][1], r[f + 1][1], false, false);
        asm volatile("buffer_store_dwordx4 %0, %1, %2, 0 offen offset:%3 sc1\n\ts_nop 1" ::"v"((u32x4){a[0], c[0], a[1], c[1]}), "v"(adr[0]), "s"(rout), "i"(f * 32) : "memory");
        SB();
      };
      if (__builtin_expect(__builtin_amdgcn_readfirstlane(pf_) != need, 0)) {
        mm(0); mm(1); mm(2); mm(3); mm(4);
#pragma unroll 1
        for (unsigned spins = 0; spins < (SPIN_CAP << 6); ++spins) {
          pf_ = *pfl;
          asm volatile("" ::: "memory");
#pragma unroll
          for (int f = 0; f < 5; ++f) pp[f] = *(const f32x4*)(smem + pa_ + f * 1024);
          asm volatile("" ::: "memory");
          if (__builtin_amdgcn_readfirstlane(pf_) == need) break;
        }
        cv(0); cv(1);
      } else {
        mm(0); mm(1); mm(2);
        cv(0); mm(3); cv(1); mm(4);
      }
      st(0); cv(2); cv(3); st(2); cv(4);
      asm volatile("buffer_store_dwordx2 %0, %1, %2, 0 offen sc1" ::"v"(r[4]), "v"(adr[1]), "s"(rout) : "memory");
    }
  }
}

extern "C" void kernel_launch(void* const* d_in, const int* in_sizes, int n_in, void* d_out, int out_size, void* d_ws,
                              size_t ws_size, hipStream_t stream) {
  kf<<<256, 512, 0, stream>>>((const u16*)d_in[0], (const u16*)d_in[1], (const u16*)d_in[2], (const u16*)d_in[4],
                              (const u16*)d_in[8], (const u16*)d_in[5], (const u16*)d_in[6], (const u16*)d_in[7],
                              (char*)d_ws, (u16*)d_out);
}
```

```cpp
#include <hip/hip_runtime.h>
#include <stdint.h>

typedef unsigned short u16;
typedef unsigned long long u64;
typedef __attribute__((ext_vector_type(8))) __bf16 bf16x8;
typedef __attribute__((ext_vector_type(2))) __bf16 bf16x2;
typedef __attribute__((ext_vector_type(4))) short s16x4;
typedef __attribute__((ext_vector_type(4))) float f32x4;
typedef __attribute__((ext_vector_type(2))) float f32x2;
typedef __attribute__((ext_vector_type(4))) unsigned u32x4;
typedef __attribute__((ext_vector_type(2))) unsigned u32x2;
typedef __attribute__((ext_vector_type(10))) unsigned u32x10;
#define GAS __attribute__((address_space(1)))
#define LAS __attribute__((address_space(3)))
typedef GAS unsigned gu32;

#define S_LEN 2048
#define DM 640
#define NQKV 1920
#define FF 1728

#define WS_OF 0
#define WS_WOF 2621440
#define WS_E (WS_WOF + 819200)
#define WS_F2 (WS_E + 256)
#define WS_F (WS_F2 + 32768)
#define WS_K (WS_F + 28672)
#define WS_R (WS_K + 36864)
#define WS_A (WS_R + 256)
#define WS_YP (WS_A + 4096)
#define TAG_STEP 0x9E3779B9u
#define SENT 0xFFFFFFFFu
#define SPIN_CAP (1u << 18)
#define NPRE 3
#define WPD 7
#define PRD 10
#define WB8 2
#define TCY 2750
#define ETA_G 15
#define ETA_F 20
#define D1 6700
#define D2 75
#define S2F 0
#define PRE1 1
#define GIN -1
#define WC 4
#define EW 0
#define DK 4200
#define RLX_AGENT __ATOMIC_RELAXED, __HIP_MEMORY_SCOPE_AGENT

__device__ __forceinline__ float bflo(unsigned u) { return __uint_as_float(u << 16); }
__device__ __forceinline__ float bfhi(unsigned u) { return __uint_as_float(u & 0xFFFF0000u); }
__device__ __forceinline__ unsigned pk2(float a, float b) {
  f32x2 v = {a, b};
  return __builtin_bit_cast(unsigned, __builtin_convertvector(v, bf16x2));
}
__device__ __forceinline__ float rbf(float f) { return bflo(pk2(f, f)); }
__device__ __forceinline__ unsigned perm_lo(unsigned hi_src, unsigned lo_src) {
  return __builtin_amdgcn_perm(hi_src, lo_src, 0x05040100u);
}
__device__ __forceinline__ unsigned perm_hi(unsigned hi_src, unsigned lo_src) {
  return __builtin_amdgcn_perm(hi_src, lo_src, 0x07060302u);
}
__device__ __forceinline__ f32x4 mfma16(u32x4 a, u32x4 b, f32x4 c) {
  return __builtin_amdgcn_mfma_f32_16x16x32_bf16(__builtin_bit_cast(bf16x8, a), __builtin_bit_cast(bf16x8, b), c, 0, 0, 0);
}
__device__ __forceinline__ u32x2 tr_read(const char* p) {
  return __builtin_bit_cast(u32x2, __builtin_amdgcn_ds_read_tr16_b64_v4i16((LAS s16x4*)(p)));
}
#define BARRIER() asm volatile("s_barrier" ::: "memory")
#define WAITV(n) asm volatile("s_waitcnt vmcnt(" #n ")" ::: "memory")
#define WAITV_(n) asm volatile("s_waitcnt vmcnt(%0)" ::"i"(n) : "memory")
#define WAITL0() asm volatile("s_waitcnt lgkmcnt(0)" ::: "memory")

__device__ __forceinline__ void top2_mrg(unsigned& k1, unsigned& k2, unsigned b1, unsigned b2) {
  k2 = max(max(k2, b2), min(k1, b1));
  k1 = max(k1, b1);
}
template <int CTRL>
__device__ __forceinline__ void top2_dpp(unsigned& k1, unsigned& k2) {
  const unsigned b1 = (unsigned)__builtin_amdgcn_update_dpp(0, (int)k1, CTRL, 0xF, 0xF, true);
  const unsigned b2 = (unsigned)__builtin_amdgcn_update_dpp(0, (int)k2, CTRL, 0xF, 0xF, true);
  top2_mrg(k1, k2, b1, b2);
}
__device__ __forceinline__ void top2_row(unsigned& k1, unsigned& k2) {
  top2_dpp<0xB1>(k1, k2);
  top2_dpp<0x4E>(k1, k2);
  top2_dpp<0x141>(k1, k2);
  top2_dpp<0x140>(k1, k2);
}
__device__ __forceinline__ void top2_all(unsigned& k1, unsigned& k2) {
  top2_row(k1, k2);
  {
    auto a = __builtin_amdgcn_permlane16_swap(k1, k1, false, false);
    auto c = __builtin_amdgcn_permlane16_swap(k2, k2, false, false);
    k1 = a[0];
    k2 = c[0];
    top2_mrg(k1, k2, a[1], c[1]);
  }
  {
    auto a = __builtin_amdgcn_permlane32_swap(k1, k1, false, false);
    auto c = __builtin_amdgcn_permlane32_swap(k2, k2, false, false);
    k1 = a[0];
    k2 = c[0];
    top2_mrg(k1, k2, a[1], c[1]);
  }
}
__device__ __forceinline__ void touch_line(const char* p, unsigned lds_off) {
  asm volatile("s_mov_b32 m0, %1\n\ts_nop 0\n\tglobal_load_lds_dword %0, off" ::"v"(p), "s"(lds_off) : "memory");
}
__device__ __forceinline__ void dma16(unsigned voff, const void* sbase, unsigned lds_off) {
  asm volatile("s_mov_b32 m0, %2\n\ts_nop 0\n\tglobal_load_lds_dwordx4 %0, %1" ::"v"(voff), "s"(sbase), "s"(lds_off) : "memory");
}
__device__ __forceinline__ void dma16_sc1(unsigned voff, const void* sbase, unsigned lds_off) {
  asm volatile("s_mov_b32 m0, %2\n\ts_nop 0\n\tglobal_load_lds_dwordx4 %0, %1 sc1" ::"v"(voff), "s"(sbase), "s"(lds_off) : "memory");
}
__device__ __forceinline__ void bdma16(unsigned voff, u32x4 srd, unsigned soff, unsigned lds_off) {
  asm volatile("s_mov_b32 m0, %3\n\ts_nop 0\n\tbuffer_load_dwordx4 %0, %1, %2 offen lds" ::"v"(voff), "s"(srd), "s"(soff), "s"(lds_off) : "memory");
}
__device__ __forceinline__ u32x4 make_srd(const void* p, unsigned bytes) {
  return (u32x4){(unsigned)(uintptr_t)p, (unsigned)((uintptr_t)p >> 32) & 0xFFFFu, bytes, 0x00020000u};
}
__device__ __forceinline__ float key_val(unsigned key) {
  unsigned u = key >> 16;
  u ^= (u & 0x8000u) ? 0x8000u : 0xFFFFu;
  return bflo(u);
}
__device__ __forceinline__ float fmx(float a, float b) { return __builtin_elementwise_maximum(a, b); }
#define SB() __builtin_amdgcn_sched_barrier(0)
__device__ __forceinline__ f32x4 mfma16h(u32x2 a, u32x2 b, f32x4 c) {
  return __builtin_amdgcn_mfma_f32_16x16x16bf16_1k(__builtin_bit_cast(s16x4, a), __builtin_bit_cast(s16x4, b), c, 0, 0, 0);
}

#define SLOT_B 43008
#define L_WO SLOT_B
#define L_EARLY (3 * SLOT_B)
#define L_PIECE L_EARLY
#define L_CAND (L_VAR + 128)
#define L_TOUCH (L_EARLY + 12288)
#define L_JUNK (L_WO + 102400)
#define L_VAR (L_JUNK + 384)
#define L_CAND2 (L_VAR + 48)
#define L_GT (L_VAR + 256)
#define L_FS (L_GT + 1024)
#define L_XT (L_FS + 1024)
#define L_TOTAL (L_XT + 10240)
#define CODE_LINES 256

__device__ __forceinline__ void res_wait(char* ws, char* smem, int lane, unsigned tag, int which) {
  const __amdgpu_buffer_rsrc_t r = __builtin_amdgcn_make_buffer_rsrc((void*)(ws + WS_R), 0, 256, 0x00020000);
  u32x4 v;
#pragma unroll 1
  for (unsigned spins = 0; spins < SPIN_CAP; ++spins) {
    asm volatile("" ::: "memory");
    v = __builtin_amdgcn_raw_buffer_load_b128(r, which * 128, 0, 16);
    if (__all((v[1] == tag) & (v[3] == tag))) break;
    __builtin_amdgcn_s_sleep(2);
  }
  if (lane == 0 && which == 0) *(u32x2*)(smem + L_VAR + 32) = (u32x2){v[0], v[2]};
  if (lane == 0 && which == 1) *(u32x4*)(smem + L_CAND2) = v;
}
__device__ __forceinline__ float dot2(unsigned a, unsigned b, float c) {
  return __builtin_amdgcn_fdot2_f32_bf16(__builtin_bit_cast(bf16x2, a), __builtin_bit_cast(bf16x2, b), c, false);
}
template <int CTRL>
__device__ __forceinline__ float dpp_add(float v) {
  return v + __int_as_float(__builtin_amdgcn_update_dpp(0, __float_as_int(v), CTRL, 0xF, 0xF, true));
}
__device__ __forceinline__ float row_sum(float v) {
  v = dpp_add<0xB1>(v);
  v = dpp_add<0x4E>(v);
  v = dpp_add<0x141>(v);
  return dpp_add<0x140>(v);
}

template <int N> struct IC { static constexpr int value = N; };
#define PIN(v) asm volatile("" : "+v"(v))
__global__ __launch_bounds__(512, 2) void kf(const u16*, const u16*, const u16*, const u16*, const u16*, const u16*, const u16*, const u16*, char*, u16*) {
  __shared__ __attribute__((aligned(16))) char smem[L_TOTAL];
  typedef const __attribute__((address_space(4))) u64* karg_t;
  const karg_t kp0 = (karg_t)__builtin_amdgcn_kernarg_segment_ptr();
  u64 pc;
  asm volatile("s_load_dword s100, %1, 0x0\n\ts_load_dword s101, %1, 0x40\n\ts_getpc_b64 %0" : "=s"(pc) : "s"(kp0) : "s100", "s101");
  const u16 *x, *wqkv, *wo, *w_r, *gate_w, *w_gate, *w_up, *w_down;
  char* ws;
  u16* out;
  auto args = [&]() {
    SB();
    karg_t kp = kp0;
    asm volatile("" : "+s"(kp));
    x = (const u16*)kp[0]; wqkv = (const u16*)kp[1]; wo = (const u16*)kp[2]; w_r = (const u16*)kp[3]; gate_w = (const u16*)kp[4];
    w_gate = (const u16*)kp[5]; w_up = (const u16*)kp[6]; w_down = (const u16*)kp[7]; ws = (char*)kp[8]; out = (u16*)kp[9];
  };
  const int tid = threadIdx.x, b = blockIdx.x;
  {
    const int ln = (b >> 3) + 32 * tid;
    if (ln < CODE_LINES)
      touch_line((const char*)((pc & ~255ull) + ln * 128), (unsigned)(uintptr_t)(LAS char*)smem + L_TOUCH);
  }
  unsigned tag;
  auto get_tag = [&]() {
    asm volatile("s_load_dword %0, %1, 0x0\n\ts_waitcnt lgkmcnt(0)" : "=s"(tag) : "s"(ws + WS_E) : "memory");
    tag += TAG_STEP;
  };
  const int lane = tid & 63;
  const int wave = __builtin_amdgcn_readfirstlane(tid >> 6);
  const int xcd = b & 7, jj = b >> 3;
  const int c15 = lane & 15, g4 = lane >> 4;
  const bool gemm_blk = jj < 28;
  const int q_ = (xcd & 3) * 28 + jj;
  const int rg = q_ >> 2, hg = (xcd >> 2) * 4 + (q_ & 3);
  const int par = rg >= 14, gi = rg - par * 14;
  const int tb = min(73 * gi, 944);
  const int tlo = gi == 0 ? 0 : 73 * gi + 7;
  const int lg = jj >> 2;
  const int rt2 = gemm_blk ? par * 16 + gi : (xcd & 2) * 8 + 14 + (xcd & 1), nt2 = hg;
  const unsigned lds0 = (unsigned)(uintptr_t)(LAS char*)smem;
  auto wo_dma = [&](auto n_, int i0, int st, int lo) {
    const u32x4 srd = make_srd(ws + WS_WOF + nt2 * 102400, 102400);
    int g = __builtin_amdgcn_readfirstlane((i0 + ((lg * (25 - lo)) >> 3)) * 4096);
#pragma unroll
    for (int i = 0; i < decltype(n_)::value; ++i) {
      if (g >= 102400) g -= (25 - lo) * 4096;
      asm volatile("s_add_u32 m0, %3, %2\n\ts_nop 0\n\tbuffer_load_dwordx4 %0, %1, %2 offen sc1 lds\n\t"
                   "buffer_load_dwordx4 %0, %1, %2 offen offset:1024 sc1 lds\n\tbuffer_load_dwordx4 %0, %1, %2 offen offset:2048 sc1 lds\n\t"
                   "buffer_load_dwordx4 %0, %1, %2 offen offset:3072 sc1 lds" ::"v"(lane * 16), "s"(srd), "s"(g), "s"(lds0 + L_WO)
                   : "memory", "scc");
      g += st * 4096;
    }
  };

  int od[5];
  const int pa = tb + c15 - 64 * gi;
  const bool pv = tb + c15 >= tlo;
  auto prepO = [&]() {
    const int h = hg * 4 + (wave & 3);
    const int b0 = (par * 64 + 56 + (pa >> 4)) * 20480, b1 = (par * 64 + gi * 4) * 20480;
    od[0] = (pv && lane < 32) ? b0 + (2 * h + g4) * 256 + (pa & 15) * 16 : 0x7FFFFF00;
    od[1] = b1 + (lane >> 5) * 20480 + (2 * h + (g4 & 1)) * 256 + c15 * 16;
    od[2] = od[1] + 2 * 20480;
    od[3] = b1 + g4 * 20480 + 16384 + h * 128 + c15 * 8;
    od[4] = (pv && g4 == 3) ? b0 + 16384 + h * 128 + (pa & 15) * 8 : 0x7FFFFF00;
#pragma unroll
    for (int e = 0; e < 5; ++e) PIN(od[e]);
  };
  auto row_t = [&](int row) {
    const int a = (xcd & 1) * 64 + row;
    return gemm_blk ? tb + 16 + row : a + (a < 16 ? 0 : (a - 7) / 9) * 64;
  };
  int adr[3], glo;
  u64 rel;
  auto tile_off = [&](int row) { return ((2 * row_t(row) + (rt2 >> 4)) * DM + nt2 * 80) * 2; };
  auto prepB = [&]() {
    const int row = (wave & 3) * 16 + c15;
    if (wave < 4) {
#pragma unroll
      for (int e = 0; e < 3; ++e) {
        const int c = min(e * 64 + lane, 159), r = c / 10;
        adr[e] = tile_off((wave & 3) * 16 + r) + (c - r * 10) * 16;
      }
    } else {
      adr[2] = tile_off(row);
      adr[0] = adr[2] + (nt2 & 1) * 32 + (g4 & 1) * 32 + (g4 >> 1) * 16;
      adr[1] = adr[2] + ((nt2 & 1) ? 0 : 128) + g4 * 8;
    }
    PIN(adr[0]); PIN(adr[1]); PIN(adr[2]);
    glo = L_GT + row * 16 + (((gemm_blk ? tb : 0) + row) & 1) * 8;
    PIN(glo);
    {
      const int a0 = (xcd & 1) * 64 + (wave & 3) * 16, gl = (xcd & 1) ? 6 + (lane >> 3) : min(lane >> 3, 6);
      rel = __builtin_amdgcn_ballot_w64(((lane >> 2) & 1) == (wave >> 2) && (gemm_blk ? lane < 8 : gl >= (a0 < 16 ? 0 : (a0 - 7) / 9) && gl <= (a0 + 8) / 9));
      asm volatile("" : "+s"(rel));
    }
  };
  auto lrow_of = [&](unsigned key) {
    const int i = 2047 - (int)(key & 0xFFFFu);
    const int t = i >> 1, g = max(t - 7, 0) / 73, rho = t - min(73 * g, 944);
    return (i & 1) == (rt2 >> 4) ? (rho >= 16 ? g * 64 + rho - 16 : 896 + t - 64 * g) - (rt2 & 15) * 64 : -1;
  };
  auto rows_job = [&]() {
    if (lane < 2) {
      const unsigned key = *(const unsigned*)(smem + L_VAR + 32 + lane * 4);
      *(int*)(smem + L_VAR + 40 + lane * 4) = lrow_of(key);
      *(float*)(smem + L_JUNK + 256 + lane * 4) = rbf(__builtin_amdgcn_rcpf(1.0f + __expf(-key_val(key))));
    }
  };
  auto gate_off = [&](int row) {
    const int p2 = rt2 >> 4, tg = row_t(row);
    const int q2 = p2 * 2 + (tg >> 9), u = tg & 511;
    return (u < 448 ? (q2 * 56 + (u >> 3)) * 128 + (u & 7) * 8 : (224 + q2 * 16 + ((u - 448) >> 2)) * 128 + (u & 3) * 8) + 64;
  };
  auto flag_off = [&]() {
    const int g = gemm_blk ? gi : (xcd & 1) ? 6 + (lane >> 3) : min(lane >> 3, 6);
    return (((rt2 >> 4) * 14 + g) * 8 + (lane & 7)) * 128 + 56;
  };
#define rfl __builtin_amdgcn_make_buffer_rsrc((void*)(ws + WS_F), 0, 28672, 0x00020000)
  auto gate_dma = [&](int go) { dma16_sc1(go & ~15, ws + WS_K, lds0 + L_GT); };
  auto now = []() { return (unsigned)__builtin_amdgcn_s_memrealtime(); };
  auto flag_wait = [&](int foff) {
#pragma unroll 1
    for (unsigned spins = 0; spins < SPIN_CAP; ++spins) {
      asm volatile("" ::: "memory");
      const u32x4 v = __builtin_amdgcn_raw_buffer_load_b128(rfl, foff, 0, 16);
      if ((v[1] == tag) & (v[3] == tag) & (foff != 0x7FFFFF00)) {
        *(u32x4*)(smem + L_FS + lane * 16) = v;
        foff = 0x7FFFFF00;
      }
      if (__all(foff == 0x7FFFFF00)) break;
      __builtin_amdgcn_s_sleep(8);
    }
  };
#define ro __builtin_amdgcn_make_buffer_rsrc((void*)(ws + WS_OF), 0, S_LEN * DM * 2, 0x00020000)

  if (__builtin_expect(gemm_blk, 1)) {
    if (__builtin_expect(wave >= 4, 1)) {
      const int hw = wave - 4;
      const int rot = (lg % 5) * 2 + (xcd & 1);
      int voffA = (lane >> 3) * (DM * 4) + (((lane & 7) ^ (((hw & 1) * 4 + g4) & 7)) * 16);
      int sA[3];
#pragma unroll
      for (int v = 0; v < 3; ++v) {
        sA[v] = ((tb + 8 * (hw + 4 * v)) * 2 + par) * (DM * 2);
      }
      const int hi = lane >> 5;
      int pair = ((lane & 31) >> 1) ^ (hi | ((hw & 1) << 1) | ((hw >> 1) << 2));
      const int sec = (pair >= 5) + (pair >= 10);
      int voffW = pair == 15 ? 0x7FFFFF00 : hi * (NQKV * 2) + sec * (1280 - 160) + hg * 160 + pair * 32 + ((lane & 1) ^ (sec == 2)) * 16;
      const int n0 = (hw & 1) + 4 * (hw >> 1);
      PIN(voffA); PIN(voffW);
      args();
      const u32x4 rsx = make_srd(x, S_LEN * DM * 2), rsw = make_srd(wqkv, DM * NQKV * 2);
      auto issue = [&](int i, int buf) {
        int kc = rot + (i < 5 ? 2 * i : 2 * i - 9);
        if (kc >= 10) kc -= 10;
        const unsigned dst = lds0 + buf * SLOT_B;
#pragma unroll
        for (int v = 0; v < 3; ++v)
          if (v < 2 || hw < 2) bdma16(voffA, rsx, sA[v] + kc * 128, dst + (hw + 4 * v) * 1024);
        const int so = (kc * 64 + 2 * n0) * (NQKV * 2);
#pragma unroll
        for (int u = 0; u < 8; ++u) {
          const int dn = 2 * (u & 1) + 8 * (u >> 1);
          bdma16(voffW, rsw, so + dn * 2 * (NQKV * 2), dst + 10240 + (n0 + dn) * 1024);
        }
      };
      const int qp = rg * 8 + hg;
      issue(0, 0);
      SB();
      int md = (par * 64 + gi * 4 + 2 * (hw & 1) + (lane >> 5)) * 20480 + 16384 + hg * 512 + (lane & 31) * 16;
      PIN(md);
      int so_ = hw == 0 ? flag_off() : gate_off(lane);
      PIN(so_);
      prepB();
      __builtin_amdgcn_sched_barrier(0);
      issue(1, 1);
      get_tag();
      if (S2F) {
        issue(2, 2);
        WAITV(20);
        BARRIER();
      } else {
        WAITV(10);
        BARRIER();
        issue(2, 2);
      }
      WAITV(10);
      BARRIER();
      const unsigned r0_ = now(), c0_ = (unsigned)__builtin_amdgcn_s_memtime();
      {
        int buf = 0;
#pragma unroll 1
        for (int i = 3; i < 9; ++i) {
          const bool ex = i == 4 && hw == 2;
          if (ex && lane == 0) __hip_atomic_store((gu32*)(ws + WS_F2 + b * 128), tag, RLX_AGENT);
          issue(i, buf);
          if (i == 3)
            asm volatile("s_load_dword s100, %0, 0x0\n\ts_load_dword s100, %0, 0xa00\n\ts_load_dword s100, %0, 0x1400\n\ts_load_dword s100, %0, 0x1e00\n\t"
                         "s_load_dword s100, %0, 0x2800\n\ts_load_dword s100, %0, 0x3200\n\ts_load_dword s100, %0, 0x3c00\n\ts_load_dword s100, %0, 0x4600" ::"s"(
                             (const char*)out + tile_off(hw * 8 + (nt2 & 1) * 32) + ((nt2 & 1) ? 0 : 128))
                         : "s100");
          if (ex) WAITV(11); else WAITV(10);
          buf = buf == 2 ? 0 : buf + 1;
          BARRIER();
        }
      }
      if (hw == 3) {
        if (lane < 2) dma16_sc1(lane * 128, ws + WS_R, lds0 + L_CAND);
        if (lane < 8) *(unsigned*)(smem + L_VAR + lane * 4) = 0u;
      }
      issue(9, 0);
      auto mark = [&]() {
        const u32x4 sent4 = {SENT, SENT, SENT, SENT};
        const int mo_ = (par * 64 + gi * 4 + (hw & 1) * 2) * 20480 + hg * 2048;
#pragma unroll
        for (int e = 0; e < 5; ++e) {
          if (e == 0) __builtin_amdgcn_raw_buffer_store_b128(sent4, ro, md, 0, 16);
          else __builtin_amdgcn_raw_buffer_store_b128(sent4, ro, lane * 16 + ((e - 1) & 1) * 1024, mo_ + ((e - 1) >> 1) * 20480, 16);
        }
        SB();
      };
      auto words = [&]() {
        const u32x4 rv = *(const u32x4*)(smem + L_CAND + (lane & 1) * 16);
        if (__all((rv[1] == tag) & (rv[3] == tag))) {
          if (tid == 256) *(u32x2*)(smem + L_VAR + 32) = (u32x2){rv[0], rv[2]};
        } else {
          res_wait(ws, smem, lane, tag, 0);
          res_wait(ws, smem, lane, tag, 1);
        }
      };
      if (hw >= 2) {
        mark();
        WAITV(15);
        BARRIER();
        WAITV(5);
        BARRIER();
        unsigned t0_ = now();
        t0_ += (unsigned)((float)(t0_ - r0_) * (float)TCY * __builtin_amdgcn_rcpf((float)((unsigned)__builtin_amdgcn_s_memtime() - c0_)));
        WAITV(0);
        __builtin_amdgcn_raw_buffer_store_b128((u32x4){t0_, tag, t0_, tag}, rfl, lane < 4 ? qp * 128 + (hw & 1) * 64 + lane * 16 : 0x7FFFFF00, 0, 16);
        if (hw == 2) rows_job();
      } else {
        WAITV(10);
        BARRIER();
        words();
        wo_dma(IC<WB8>{}, hw, 2, 25);
        WAITV_(4 * WB8);
        WAITL0();
        BARRIER();
        if (hw == 1) gate_dma(so_);
        wo_dma(IC<12 - WB8>{}, 2 * WB8 + hw, 2, 2 * WB8);
        if (hw == 0 && lane < 8) dma16_sc1(so_, ws + WS_F, lds0 + L_FS);
        if (hw == 0) wo_dma(IC<1>{}, 24, 2, 2 * WB8);
        WAITV(0);
      }
      WAITL0();
      BARRIER();
    } else {
      const int xb_ = b - 160;
      const bool wjob = wave < 2 || (wave == 2 && (unsigned)xb_ < 64u);
      const int xi = (xb_ >> 3) * 2 + (xcd >> 2);
      const int tj0 = (xcd & 1) * 512 + (wave < 2 ? lg * 64 + hg * 8 + wave * 4 : 448 + xi * 4), pj = par;
      u32x4 xs[5], wv[4];
      int jo0 = lane * 16, jo1 = g4 * 2560 + c15 * 16, jo2 = c15 * 16;
      PIN(jo0); PIN(jo1); PIN(jo2);
      args();
      if (wjob) {
        const u16* xr = x + (2 * tj0 + pj) * DM;
        asm volatile("global_load_dwordx4 %0, %9, %12\n\tglobal_load_dwordx4 %1, %9, %12 offset:2560\n\t"
                     "global_load_dwordx4 %2, %9, %13\n\tglobal_load_dwordx4 %3, %9, %13 offset:2560\n\t"
                     "global_load_dwordx4 %4, %10, %12 offset:1024\n\tglobal_load_dwordx4 %5, %9, %14\n\t"
                     "global_load_dwordx4 %6, %11, %14 offset:1024\n\tglobal_load_dwordx4 %7, %9, %15\n\tglobal_load_dwordx4 %8, %11, %15 offset:1024"
                     : "=&v"(xs[0]), "=&v"(xs[1]), "=&v"(xs[2]), "=&v"(xs[3]), "=&v"(xs[4]), "=&v"(wv[0]), "=&v"(wv[1]), "=&v"(wv[2]), "=&v"(wv[3])
                     : "v"(jo0), "v"(jo1), "v"(jo2), "s"(xr), "s"(xr + 4 * DM), "s"(w_r), "s"(gate_w)
                     : "memory");
      }
      f32x4 acc[5][4];
#pragma unroll
      for (int m = 0; m < 5; ++m)
#pragma unroll
        for (int f = 0; f < 4; ++f) acc[m][f] = (f32x4){0.f, 0.f, 0.f, 0.f};
      const int wks = b % 20;
      if (b < 200) {
        const int kh = wks >= 10, s = wks - kh * 10, pr = lane >> 3, g = wave, hi = pr >> 2;
        const int row = (s < 8 ? (2 * (kh * 8 + s) + (g >> 1)) * 20 + 4 * (g & 1) + 8 * hi : (kh * 16 + (s - 8) * 8 + hi * 4 + g) * 20 + 16) + (pr & 3);
        asm volatile("s_mov_b32 m0, %2\n\ts_nop 0\n\tglobal_load_lds_dwordx4 %0, %1 nt" ::"v"((row * DM + (b / 20) * 64 + (lane & 7) * 8) * 2), "s"(wo), "s"(lds0 + L_PIECE + wave * 1024) : "memory");
      }
      const __amdgpu_buffer_rsrc_t rk = __builtin_amdgcn_make_buffer_rsrc((void*)(ws + WS_K), 0, 36864, 0x00020000);
      const int ko = c15 != 0 ? 0x7FFFFF00 : wave < 2 ? (rg * 8 + hg) * 128 + (wave * 4 + g4) * 8 : (224 + (xcd & 3) * 16 + xi) * 128 + g4 * 8;
      auto rows4 = [&](float (&p)[5]) {
        const auto a = __builtin_amdgcn_permlane32_swap(__float_as_uint(p[0]), __float_as_uint(p[2]), false, false);
        const auto c = __builtin_amdgcn_permlane32_swap(__float_as_uint(p[1]), __float_as_uint(p[3]), false, false);
        const auto e = __builtin_amdgcn_permlane16_swap(__float_as_uint(__uint_as_float(a[0]) + __uint_as_float(a[1])),
                                                        __float_as_uint(__uint_as_float(c[0]) + __uint_as_float(c[1])), false, false);
        return row_sum(__uint_as_float(e[0]) + __uint_as_float(e[1]) + p[4]);
      };
      const int p4 = lane & 3;
      f32x4 limA, limB;
      unsigned mA[2];
      u32x4 ones;
      u32x2 sel[4], kz[5];
      auto prepM = [&]() {
        int ln = lane;
        PIN(ln);
        const int e0 = (ln & 15) - 4 * (ln >> 4);
#pragma unroll
        for (int i = 0; i < 4; ++i) {
          limA[i] = e0 - i <= -9 ? 0.f : -__builtin_inff();
          limB[i] = (e0 - i >= 0 && e0 - i <= 7) ? 0.f : -__builtin_inff();
        }
        mA[0] = (e0 <= -9 ? 0xFFFFu : 0u) | (e0 - 1 <= -9 ? 0xFFFF0000u : 0u);
        mA[1] = (e0 - 2 <= -9 ? 0xFFFFu : 0u) | (e0 - 3 <= -9 ? 0xFFFF0000u : 0u);
        PIN(limA); PIN(limB); PIN(mA[0]); PIN(mA[1]);
      };
      auto prepS = [&]() {
        int ln = lane;
        PIN(ln);
        ones = (u32x4){0x3F803F80u, 0x3F803F80u, 0x3F803F80u, 0x3F803F80u};
        PIN(ones);
#pragma unroll
        for (int t = 0; t < 4; ++t) {
          const bool on = (ln >> 4) == 2 && ((((ln & 15) >> 2) - t + 1) & 3) < 2;
          sel[t] = (u32x2){on && (ln & 3) < 2 ? 0x3F80u << (16 * (ln & 3)) : 0u, on && (ln & 3) >= 2 ? 0x3F80u << (16 * ((ln & 3) - 2)) : 0u};
          PIN(sel[t]);
        }
#pragma unroll
        for (int t = 0; t < 5; ++t) {
          kz[t] = (u32x2){0u, 0u};
          PIN(kz[t]);
        }
      };
      const int swz = (c15 >> 1) & 7;
      const int aoff0 = c15 * 128 + ((g4 ^ swz) * 16);
      const int aoff1 = c15 * 128 + (((4 + g4) ^ swz) * 16);
      int woff[4];
      {
        const int q4 = c15 >> 2, sw = q4 | ((g4 & 1) << 2);
#pragma unroll
        for (int t = 0; t < 4; ++t) {
          const int sc = t < 3 ? t : p4;
          const int by = wave * 40 + (t < 3 ? p4 : 4) * 8;
          woff[t] = 10240 + (8 * g4 + q4) * 512 + (((sc == 3 ? 15 : sc * 5 + (by >> 5)) ^ sw) * 32) + ((by & 31) ^ (sc == 2 ? 16 : sc == 3 ? 8 : 0));
          PIN(woff[t]);
        }
      }
      int wo3[2][4], ao3[2][2];
#pragma unroll
      for (int q = 0; q < 2; ++q) {
#pragma unroll
        for (int j = 0; j < 4; ++j) {
          wo3[q][j] = woff[j] + (q + 1) * SLOT_B;
          PIN(wo3[q][j]);
        }
        ao3[q][0] = aoff0 + (q + 1) * SLOT_B;
        ao3[q][1] = aoff1 + (q + 1) * SLOT_B;
        PIN(ao3[q][0]); PIN(ao3[q][1]);
      }
#define GM(m, f, wf, af) acc[m][f] = (f) == 2 ? mfma16(af[m], wf[f], acc[m][f]) : mfma16(wf[f], af[m], acc[m][f])
      u32x4 af0[5], wf0[4], af1[5], wf1[4];
      auto rd1 = [&](u32x4(&an)[5], u32x4(&wn)[4], int q, int h, int r) {
        const int ao = q ? ao3[q - 1][h] : h ? aoff1 : aoff0;
        if (r == 2) {
          an[0] = *(const u32x4*)(smem + ao);
        } else if (r >= 9) {
          an[r - 8] = *(const u32x4*)(smem + ao + (r - 8) * 2048);
        } else {
          const int k = r < 2 ? r : r - 1;
          const u32x2 t = tr_read(smem + (q ? wo3[q - 1][k >> 1] : woff[k >> 1]) + h * 16384 + (k & 1) * 2048);
          wn[k >> 1][(k & 1) * 2] = t[0];
          wn[k >> 1][(k & 1) * 2 + 1] = t[1];
        }
      };
      auto half = [&](const u32x4(&a)[5], const u32x4(&w)[4], u32x4(&an)[5], u32x4(&wn)[4], int q, int h) {
#pragma unroll
        for (int i = 0; i < 20; ++i) {
          const int m_ = i < 15 ? i / 3 : 19 - i, j_ = ((i / 3) & 1) ? 2 - i % 3 : i % 3, f_ = i < 15 ? j_ + (j_ == 2) : 2;
          GM(m_, f_, w, a);
          SB();
          if (i < 13) rd1(an, wn, q, h, i);
          SB();
        }
      };
      SB();
      get_tag();
      if (wjob) {
        if (b < 200) WAITV(3); else WAITV(2);
#pragma unroll
        for (int i = 0; i < 5; ++i) PIN(xs[i]);
        PIN(wv[0]); PIN(wv[1]);
        float p[5];
#pragma unroll
        for (int i = 0; i < 5; ++i) {
          p[i] = 0.f;
#pragma unroll
          for (int q = 0; q < 4; ++q) p[i] = dot2(xs[i][q], wv[i == 4][q], p[i]);
        }
        const float r_ = rows4(p);
        unsigned u = pk2(r_, r_) & 0xFFFFu;
        u ^= (u & 0x8000u) ? 0xFFFFu : 0x8000u;
        const unsigned key = (u << 16) | (unsigned)(2047 - (2 * (tj0 + g4) + pj));
        __builtin_amdgcn_raw_buffer_store_b64((u32x2){key, tag}, rk, ko, 0, 16);
        SB();
      }
      u32x2 gv = {0u, 0u};
      int ga = 0x7FFFFF00;
      auto gate_job = [&]() {
      if (wjob) {
        PIN(wv[2]); PIN(wv[3]);
        float p[5];
#pragma unroll
        for (int i = 0; i < 5; ++i) {
          p[i] = 0.f;
#pragma unroll
          for (int q = 0; q < 4; ++q) {
            const unsigned gw = wv[2 + (i == 4)][q];
            p[i] = dot2(pk2(bflo(xs[i][q]) * bflo(gw), bfhi(xs[i][q]) * bfhi(gw)), 0x3F803F80u, p[i]);
          }
        }
        const float g_ = rbf(rows4(p));
        gv = (u32x2){__float_as_uint(rbf(1.0f / (1.0f + __expf(-g_)))), tag};
        ga = c15 != 0 ? ko : ko + 64;
        SB();
      }
        PIN(gv); PIN(ga);
      };
      if (b < 200) WAITV(2); else WAITV(1);
      if (GIN < 0) gate_job();
      u32x4 wfr;
      int wfa;
      {
        if (wjob) WAITV(1); else WAITV(0);
        const char* tp = smem + L_PIECE + wave * 1024 + (c15 >> 2) * 128 + g4 * 32 + (lane & 3) * 8;
        const u32x2 lo = tr_read(tp), hi = tr_read(tp + 512);
        const int T = (b / 20) * 4 + g4;
        wfr = (u32x4){lo[0], lo[1], hi[0], hi[1]};
        wfa = b < 200 ? (((T / 5) * 20 + wks) * 5 + (T + 5 - ((T / 5) & 1)) % 5) * 1024 + (wave * 16 + c15) * 16 : 0x7FFFFF00;
        PIN(wfr); PIN(wfa);
      }
      WAITL0();
      BARRIER();
#pragma unroll
      for (int r = 0; r < 13; ++r) rd1(af0, wf0, 0, 0, r);
      SB();
#pragma unroll
      for (int i = 0; i < 9; ++i) {
        half(af0, wf0, af1, wf1, i % 3, 1);
        if (i == 0) {
          __builtin_amdgcn_raw_buffer_store_b128(wfr, __builtin_amdgcn_make_buffer_rsrc((void*)(ws + WS_WOF), 0, 819200, 0x00020000), wfa, 0, 16);
          if (GIN < 0) __builtin_amdgcn_raw_buffer_store_b64(gv, rk, ga, 0, 16);
        }
        if (i == GIN) {
          gate_job();
          __builtin_amdgcn_raw_buffer_store_b64(gv, rk, ga, 0, 16);
        }
        if (i == 1) WAITV(0);
        if (i == 3) prepO();
        if (i == 4) prepM();
        if (i == 5) prepS();
        if (i == 6) prepB();
        SB();
        WAITL0();
        BARRIER();
        if (i < 8) half(af1, wf1, af0, wf0, (i + 1) % 3, 0);
      }
      u32x4 wf2[4], af2[5];
      u32x4 qf[5], kf[5];
      u32x10 vb, tb_;
      u32x4 pf[5], os[3];
      u32x2 pp[5], ob[6], obT, obU;
      f32x4 l[5], sA[5], oA[5], sm[5], tx[5], oT[3];
      f32x2 ar[5][2], nn0[2], nn1[2], nn2[2], nn3[2], nn4[2], nnT[2], nnU[2];
      float mx[6], inv[5], it, iu;
      constexpr u64 mo = 0xFFFF0000FFFF0000ull, mh = 0xFFFFFFFF00000000ull;
      auto selm = [](u64 m_, float a, float c) { return __builtin_amdgcn_inverse_ballot_w64(m_) ? a : c; };
      const float cs = 0.22360679774997896f * 1.4426950408889634f;
      const f32x2 c2 = {cs, cs};
      const f32x4 zero4 = {0.f, 0.f, 0.f, 0.f};
      auto cpy = [](float v) { PIN(v); return v; };
      auto cpyu = [](unsigned v) { PIN(v); return v; };
      auto mulo = [](float v, float c) { PIN(v); return v * c; };
#define XBAR() BARRIER()
      GM(0, 3, wf1, af1); SB();
      af0[0] = *(const u32x4*)(smem + aoff0 + 0); SB();
      { const u32x2 t_ = tr_read(smem + woff[3] + 0); wf0[3][0] = t_[0]; wf0[3][1] = t_[1]; } SB();
      GM(0, 1, wf1, af1); SB();
      { const u32x2 t_ = tr_read(smem + woff[3] + 2048); wf0[3][2] = t_[0]; wf0[3][3] = t_[1]; } SB();
      { const u32x2 t_ = tr_read(smem + woff[1] + 0); wf0[1][0] = t_[0]; wf0[1][1] = t_[1]; } SB();
      GM(0, 0, wf1, af1); SB();
      { const u32x2 t_ = tr_read(smem + woff[1] + 2048); wf0[1][2] = t_[0]; wf0[1][3] = t_[1]; } SB();
      { const u32x2 t_ = tr_read(smem + woff[0] + 0); wf0[0][0] = t_[0]; wf0[0][1] = t_[1]; } SB();
      GM(0, 2, wf1, af1); SB();
      { const u32x2 t_ = tr_read(smem + woff[0] + 2048); wf0[0][2] = t_[0]; wf0[0][3] = t_[1]; } SB();
      af2[0] = *(const u32x4*)(smem + aoff1 + 0); SB();
      GM(1, 3, wf1, af1); SB();
      { const u32x2 t_ = tr_read(smem + woff[2] + 0); wf0[2][0] = t_[0]; wf0[2][1] = t_[1]; } SB();
      { const u32x2 t_ = tr_read(smem + woff[3] + 16384); wf2[3][0] = t_[0]; wf2[3][1] = t_[1]; } SB();
      GM(1, 1, wf1, af1); SB();
      { const u32x2 t_ = tr_read(smem + woff[2] + 2048); wf0[2][2] = t_[0]; wf0[2][3] = t_[1]; } SB();
      { const u32x2 t_ = tr_read(smem + woff[3] + 18432); wf2[3][2] = t_[0]; wf2[3][3] = t_[1]; } SB();
      GM(1, 0, wf1, af1); SB();
      { const u32x2 t_ = tr_read(smem + woff[1] + 16384); wf2[1][0] = t_[0]; wf2[1][1] = t_[1]; } SB();
      { const u32x2 t_ = tr_read(smem + woff[1] + 18432); wf2[1][2] = t_[0]; wf2[1][3] = t_[1]; } SB();
      GM(1, 2, wf1, af1); SB();
      { const u32x2 t_ = tr_read(smem + woff[0] + 16384); wf2[0][0] = t_[0]; wf2[0][1] = t_[1]; } SB();
      { const u32x2 t_ = tr_read(smem + woff[0] + 18432); wf2[0][2] = t_[0]; wf2[0][3] = t_[1]; } SB();
      GM(2, 3, wf1, af1); SB();
      af0[1] = *(const u32x4*)(smem + aoff0 + 2048); SB();
      { const u32x2 t_ = tr_read(smem + woff[2] + 16384); wf2[2][0] = t_[0]; wf2[2][1] = t_[1]; } SB();
      GM(2, 1, wf1, af1); SB();
      af2[1] = *(const u32x4*)(smem + aoff1 + 2048); SB();
      { const u32x2 t_ = tr_read(smem + woff[2] + 18432); wf2[2][2] = t_[0]; wf2[2][3] = t_[1]; } SB();
      GM(2, 0, wf1, af1); SB();
      af0[2] = *(const u32x4*)(smem + aoff0 + 4096); SB();
      af2[2] = *(const u32x4*)(smem + aoff1 + 4096); SB();
      GM(2, 2, wf1, af1); SB();
      af0[3] = *(const u32x4*)(smem + aoff0 + 6144); SB();
      af2[3] = *(const u32x4*)(smem + aoff1 + 6144); SB();
      GM(0, 3, wf0, af0); SB();
      af0[4] = *(const u32x4*)(smem + aoff0 + 8192); SB();
      af2[4] = *(const u32x4*)(smem + aoff1 + 8192); SB();
      GM(0, 1, wf0, af0); SB();
      GM(0, 0, wf0, af0); SB();
      GM(0, 2, wf0, af0); SB();
      GM(0, 3, wf2, af2); SB();
      GM(0, 1, wf2, af2); SB();
      GM(0, 0, wf2, af2); SB();
      GM(0, 2, wf2, af2); SB();
      GM(1, 3, wf0, af0); SB();
      GM(1, 1, wf0, af0); SB();
      GM(1, 0, wf0, af0); SB();
      GM(1, 2, wf0, af0); SB();
      qf[0][2] = pk2(acc[0][3][0], acc[0][3][1]); SB();
      GM(1, 3, wf2, af2); SB();
      qf[0][3] = pk2(acc[0][3][2], acc[0][3][3]); SB();
      { const auto r_ = __builtin_amdgcn_permlane16_swap(qf[0][2], kz[0][0], false, false); qf[0][2] = r_[0]; kf[0][2] = r_[1]; } SB();
      GM(1, 1, wf2, af2); SB();
      kf[0][0] = pk2(acc[0][1][0], acc[0][1][1]); SB();
      { const auto r_ = __builtin_amdgcn_permlane16_swap(qf[0][3], kz[0][1], false, false); qf[0][3] = r_[0]; kf[0][3] = r_[1]; } SB();
      GM(1, 0, wf2, af2); SB();
      kf[0][1] = pk2(acc[0][1][2], acc[0][1][3]); SB();
      qf[0][0] = pk2(acc[0][0][0], acc[0][0][1]); SB();
      tx[0] = mfma16h((u32x2){qf[0][2], qf[0][3]}, sel[0], zero4); SB();
      qf[1][2] = pk2(acc[1][3][0], acc[1][3][1]); SB();
      qf[1][3] = pk2(acc[1][3][2], acc[1][3][3]); SB();
      GM(1, 2, wf2, af2); SB();
      { const auto r_ = __builtin_amdgcn_permlane16_swap(qf[1][2], kz[1][0], false, false); qf[1][2] = r_[0]; kf[1][2] = r_[1]; } SB();
      kf[1][0] = pk2(acc[1][1][0], acc[1][1][1]); SB();
      GM(2, 3, wf0, af0); SB();
      qf[1][0] = pk2(acc[1][0][0], acc[1][0][1]); SB();
      { const auto r_ = __builtin_amdgcn_permlane16_swap(qf[1][3], kz[1][1], false, false); qf[1][3] = r_[0]; kf[1][3] = r_[1]; } SB();
      GM(2, 1, wf0, af0); SB();
      kf[1][1] = pk2(acc[1][1][2], acc[1][1][3]); SB();
      qf[1][1] = pk2(acc[1][0][2], acc[1][0][3]); SB();
      tx[1] = mfma16h((u32x2){qf[1][2], qf[1][3]}, sel[1], zero4); SB();
      qf[0][1] = pk2(acc[0][0][2], acc[0][0][3]); SB();
      tb_[0] = pk2(tx[0][0], tx[0][1]); SB();
      l[1] = mfma16(kf[1], qf[1], limB); SB();
      tb_[1] = pk2(tx[0][2], tx[0][3]); SB();
      vb[0] = pk2(acc[0][2][0], acc[0][2][1]); SB();
      sA[1] = mfma16(kf[0], qf[1], limA); SB();
      vb[1] = pk2(acc[0][2][2], acc[0][2][3]); SB();
      vb[2] = pk2(acc[1][2][0], acc[1][2][1]); SB();
      l[0] = mfma16(kf[0], qf[0], limB); SB();
      vb[3] = pk2(acc[1][2][2], acc[1][2][3]); SB();
      tb_[2] = pk2(tx[1][0], tx[1][1]); SB();
      GM(2, 0, wf0, af0); SB();
      tb_[3] = pk2(tx[1][2], tx[1][3]); SB();
      GM(2, 2, wf0, af0); SB();
      l[1][0] = fmx(l[1][0], sA[1][0]); SB();
      l[1][1] = fmx(l[1][1], sA[1][1]); SB();
      GM(2, 3, wf2, af2); SB();
      l[1][2] = fmx(l[1][2], sA[1][2]); SB();
      mx[0] = fmx(fmx(l[0][0], l[0][1]), l[0][2]); SB();
      GM(2, 1, wf2, af2); SB();
      l[1][3] = fmx(l[1][3], sA[1][3]); SB();
      mx[1] = fmx(fmx(l[1][0], l[1][1]), l[1][2]); SB();
      GM(2, 0, wf2, af2); SB();
      mx[0] = fmx(mx[0], l[0][3]); SB();
      mx[1] = fmx(mx[1], l[1][3]); SB();
      GM(2, 2, wf2, af2); SB();
      qf[2][2] = pk2(acc[2][3][0], acc[2][3][1]); SB();
      qf[2][3] = pk2(acc[2][3][2], acc[2][3][3]); SB();
      GM(3, 3, wf1, af1); SB();
      { const auto r_ = __builtin_amdgcn_permlane16_swap(qf[2][2], kz[2][0], false, false); qf[2][2] = r_[0]; kf[2][2] = r_[1]; } SB();
      kf[2][0] = pk2(acc[2][1][0], acc[2][1][1]); SB();
      GM(3, 1, wf1, af1); SB();
      qf[2][0] = pk2(acc[2][0][0], acc[2][0][1]); SB();
      { const auto r_ = __builtin_amdgcn_permlane16_swap(qf[2][3], kz[2][1], false, false); qf[2][3] = r_[0]; kf[2][3] = r_[1]; } SB();
      GM(3, 0, wf1, af1); SB();
      kf[2][1] = pk2(acc[2][1][2], acc[2][1][3]); SB();
      qf[2][1] = pk2(acc[2][0][2], acc[2][0][3]); SB();
      tx[2] = mfma16h((u32x2){qf[2][2], qf[2][3]}, sel[2], zero4); SB();
      { const auto r_ = __builtin_amdgcn_permlane32_swap(__float_as_uint(mx[0]), __float_as_uint(mx[1]), false, false); mx[0] = __uint_as_float(r_[0]); mx[1] = __uint_as_float(r_[1]); } SB();
      mx[0] = fmx(mx[0], mx[1]); SB();
      l[2] = mfma16(kf[2], qf[2], limB); SB();
      mx[1] = cpy(mx[0]); SB();
      vb[4] = pk2(acc[2][2][0], acc[2][2][1]); SB();
      sA[2] = mfma16(kf[1], qf[2], limA); SB();
      { const auto r_ = __builtin_amdgcn_permlane16_swap(__float_as_uint(mx[0]), __float_as_uint(mx[1]), false, false); mx[0] = __uint_as_float(r_[0]); mx[1] = __uint_as_float(r_[1]); } SB();
      mx[0] = fmx(mx[0], mx[1]); SB();
      GM(3, 2, wf1, af1); SB();
      mx[1] = mx[0] * -cs; SB();
      mx[0] = mulo(mx[0], -cs); SB();
      GM(3, 3, wf0, af0); SB();
      vb[5] = pk2(acc[2][2][2], acc[2][2][3]); SB();
      { const auto r_ = __builtin_amdgcn_permlane32_swap(__float_as_uint(mx[0]), __float_as_uint(mx[1]), false, false); mx[0] = __uint_as_float(r_[0]); mx[1] = __uint_as_float(r_[1]); } SB();
      GM(3, 1, wf0, af0); SB();
      l[2][0] = fmx(l[2][0], sA[2][0]); SB();
      l[2][1] = fmx(l[2][1], sA[2][1]); SB();
      GM(3, 0, wf0, af0); SB();
      l[2][2] = fmx(l[2][2], sA[2][2]); SB();
      l[2][3] = fmx(l[2][3], sA[2][3]); SB();
      GM(3, 2, wf0, af0); SB();
      mx[2] = fmx(fmx(l[2][0], l[2][1]), l[2][2]); SB();
      mx[2] = fmx(mx[2], l[2][3]); SB();
      GM(3, 3, wf2, af2); SB();
      ar[1][0] = __builtin_elementwise_fma((f32x2){l[1][0], l[1][1]}, c2, (f32x2){mx[1], mx[1]}); SB();
      ar[1][1] = __builtin_elementwise_fma((f32x2){l[1][2], l[1][3]}, c2, (f32x2){mx[1], mx[1]}); SB();
      GM(3, 1, wf2, af2); SB();
      ar[0][0] = __builtin_elementwise_fma((f32x2){l[0][0], l[0][1]}, c2, (f32x2){mx[0], mx[0]}); SB();
      ar[0][1] = __builtin_elementwise_fma((f32x2){l[0][2], l[0][3]}, c2, (f32x2){mx[0], mx[0]}); SB();
      GM(3, 0, wf2, af2); SB();
      ar[1][0][0] = __builtin_amdgcn_exp2f(ar[1][0][0]); SB();
      GM(3, 2, wf2, af2); SB();
      qf[3][2] = pk2(acc[3][3][0], acc[3][3][1]); SB();
      qf[3][3] = pk2(acc[3][3][2], acc[3][3][3]); SB();
      GM(4, 3, wf1, af1); SB();
      { const auto r_ = __builtin_amdgcn_permlane16_swap(qf[3][2], kz[3][0], false, false); qf[3][2] = r_[0]; kf[3][2] = r_[1]; } SB();
      kf[3][0] = pk2(acc[3][1][0], acc[3][1][1]); SB();
      GM(4, 1, wf1, af1); SB();
      qf[3][0] = pk2(acc[3][0][0], acc[3][0][1]); SB();
      { const auto r_ = __builtin_amdgcn_permlane16_swap(qf[3][3], kz[3][1], false, false); qf[3][3] = r_[0]; kf[3][3] = r_[1]; } SB();
      GM(4, 0, wf1, af1); SB();
      kf[3][1] = pk2(acc[3][1][2], acc[3][1][3]); SB();
      qf[3][1] = pk2(acc[3][0][2], acc[3][0][3]); SB();
      tx[3] = mfma16h((u32x2){qf[3][2], qf[3][3]}, sel[3], zero4); SB();
      ar[1][0][1] = __builtin_amdgcn_exp2f(ar[1][0][1]); SB();
      l[3] = mfma16(kf[3], qf[3], limB); SB();
      ar[1][1][0] = __builtin_amdgcn_exp2f(ar[1][1][0]); SB();
      sA[3] = mfma16(kf[2], qf[3], limA); SB();
      ar[1][1][1] = __builtin_amdgcn_exp2f(ar[1][1][1]); SB();
      GM(4, 2, wf1, af1); SB();
      ar[0][0][0] = __builtin_amdgcn_exp2f(ar[0][0][0]); SB();
      GM(4, 3, wf0, af0); SB();
      ar[0][0][1] = __builtin_amdgcn_exp2f(ar[0][0][1]); SB();
      GM(4, 1, wf0, af0); SB();
      ar[0][1][0] = __builtin_amdgcn_exp2f(ar[0][1][0]); SB();
      GM(4, 0, wf0, af0); SB();
      l[3][0] = fmx(l[3][0], sA[3][0]); SB();
      l[3][1] = fmx(l[3][1], sA[3][1]); SB();
      GM(4, 2, wf0, af0); SB();
      l[3][2] = fmx(l[3][2], sA[3][2]); SB();
      l[3][3] = fmx(l[3][3], sA[3][3]); SB();
      GM(4, 3, wf2, af2); SB();
      mx[3] = fmx(fmx(l[3][0], l[3][1]), l[3][2]); SB();
      mx[3] = fmx(mx[3], l[3][3]); SB();
      GM(4, 1, wf2, af2); SB();
      ar[0][1][1] = __builtin_amdgcn_exp2f(ar[0][1][1]); SB();
      GM(4, 0, wf2, af2); SB();
      { const auto r_ = __builtin_amdgcn_permlane32_swap(__float_as_uint(mx[2]), __float_as_uint(mx[3]), false, false); mx[2] = __uint_as_float(r_[0]); mx[3] = __uint_as_float(r_[1]); } SB();
      mx[2] = fmx(mx[2], mx[3]); SB();
      GM(4, 2, wf2, af2); SB();
      qf[4][2] = pk2(acc[4][3][0], acc[4][3][1]); SB();
      qf[4][3] = pk2(acc[4][3][2], acc[4][3][3]); SB();
      mx[3] = cpy(mx[2]); SB();
      { const auto r_ = __builtin_amdgcn_permlane16_swap(qf[4][2], kz[4][0], false, false); qf[4][2] = r_[0]; kf[4][2] = r_[1]; } SB();
      kf[4][0] = pk2(acc[4][1][0], acc[4][1][1]); SB();
      qf[4][0] = pk2(acc[4][0][0], acc[4][0][1]); SB();
      { const auto r_ = __builtin_amdgcn_permlane16_swap(qf[4][3], kz[4][1], false, false); qf[4][3] = r_[0]; kf[4][3] = r_[1]; } SB();
      kf[4][1] = pk2(acc[4][1][2], acc[4][1][3]); SB();
      qf[4][1] = pk2(acc[4][0][2], acc[4][0][3]); SB();
      tx[4] = mfma16h((u32x2){qf[4][2], qf[4][3]}, sel[0], zero4); SB();
      { const auto r_ = __builtin_amdgcn_permlane16_swap(__float_as_uint(mx[2]), __float_as_uint(mx[3]), false, false); mx[2] = __uint_as_float(r_[0]); mx[3] = __uint_as_float(r_[1]); } SB();
      mx[2] = fmx(mx[2], mx[3]); SB();
      l[4] = mfma16(kf[4], qf[4], limB); SB();
      mx[3] = mx[2] * -cs; SB();
      mx[2] = mulo(mx[2], -cs); SB();
      sA[4] = mfma16(kf[3], qf[4], limA); SB();
      pp[1][0] = pk2(ar[1][0][0], ar[1][0][1]); SB();
      { const auto r_ = __builtin_amdgcn_permlane32_swap(__float_as_uint(mx[2]), __float_as_uint(mx[3]), false, false); mx[2] = __uint_as_float(r_[0]); mx[3] = __uint_as_float(r_[1]); } SB();
      ar[2][0] = __builtin_elementwise_fma((f32x2){l[2][0], l[2][1]}, c2, (f32x2){mx[2], mx[2]}); SB();
      ar[2][1] = __builtin_elementwise_fma((f32x2){l[2][2], l[2][3]}, c2, (f32x2){mx[2], mx[2]}); SB();
      ar[2][0][0] = __builtin_amdgcn_exp2f(ar[2][0][0]); SB();
      ar[2][0][1] = __builtin_amdgcn_exp2f(ar[2][0][1]); SB();
      ar[2][1][0] = __builtin_amdgcn_exp2f(ar[2][1][0]); SB();
      ar[2][1][1] = __builtin_amdgcn_exp2f(ar[2][1][1]); SB();
      l[4][0] = fmx(l[4][0], sA[4][0]); SB();
      l[4][1] = fmx(l[4][1], sA[4][1]); SB();
      l[4][2] = fmx(l[4][2], sA[4][2]); SB();
      l[4][3] = fmx(l[4][3], sA[4][3]); SB();
      mx[4] = fmx(fmx(l[4][0], l[4][1]), l[4][2]); SB();
      mx[4] = fmx(mx[4], l[4][3]); SB();
      mx[5] = cpy(mx[4]); SB();
      pp[1][1] = pk2(ar[1][1][0], ar[1][1][1]); SB();
      pp[2][0] = pk2(ar[2][0][0], ar[2][0][1]); SB();
      { const auto r_ = __builtin_amdgcn_permlane32_swap(__float_as_uint(mx[4]), __float_as_uint(mx[5]), false, false); mx[4] = __uint_as_float(r_[0]); mx[5] = __uint_as_float(r_[1]); } SB();
      mx[4] = fmx(mx[4], mx[5]); SB();
      mx[5] = cpy(mx[4]); SB();
      pp[2][1] = pk2(ar[2][1][0], ar[2][1][1]); SB();
      pp[0][0] = pk2(ar[0][0][0], ar[0][0][1]); SB();
      { const auto r_ = __builtin_amdgcn_permlane16_swap(__float_as_uint(mx[4]), __float_as_uint(mx[5]), false, false); mx[4] = __uint_as_float(r_[0]); mx[5] = __uint_as_float(r_[1]); } SB();
      pp[0][1] = pk2(ar[0][1][0], ar[0][1][1]); SB();
      mx[4] = fmx(mx[4], mx[5]); SB();
      pf[1][0] = pp[1][0] & mA[0]; SB();
      sm[0] = mfma16h((u32x2){ones[0], ones[1]}, pp[0], zero4); SB();
      pf[1][2] = pp[1][0] & ~mA[0]; SB();
      pf[1][1] = pp[1][1] & mA[1]; SB();
      oA[0] = mfma16h((u32x2){vb[0], vb[1]}, pp[0], zero4); SB();
      pf[1][3] = pp[1][1] & ~mA[1]; SB();
      pf[2][0] = pp[2][0] & mA[0]; SB();
      oT[2] = mfma16h((u32x2){tb_[0], tb_[1]}, pp[0], zero4); SB();
      pf[2][2] = pp[2][0] & ~mA[0]; SB();
      pf[2][1] = pp[2][1] & mA[1]; SB();
      sm[1] = mfma16(ones, pf[1], zero4); SB();
      pf[2][3] = pp[2][1] & ~mA[1]; SB();
      mx[4] = mx[4] * -cs; SB();
      PIN(tb_); oT[0] = mfma16(__builtin_shufflevector(tb_, tb_, 0, 1, 2, 3), pf[1], zero4); SB();
      ar[3][0] = __builtin_elementwise_fma((f32x2){l[3][0], l[3][1]}, c2, (f32x2){mx[3], mx[3]}); SB();
      ar[3][1] = __builtin_elementwise_fma((f32x2){l[3][2], l[3][3]}, c2, (f32x2){mx[3], mx[3]}); SB();
      sm[2] = mfma16(ones, pf[2], zero4); SB();
      ar[4][0] = __builtin_elementwise_fma((f32x2){l[4][0], l[4][1]}, c2, (f32x2){mx[4], mx[4]}); SB();
      ar[4][1] = __builtin_elementwise_fma((f32x2){l[4][2], l[4][3]}, c2, (f32x2){mx[4], mx[4]}); SB();
      PIN(vb); oA[1] = mfma16(__builtin_shufflevector(vb, vb, 0, 1, 2, 3), pf[1], zero4); SB();
      ar[3][0][0] = __builtin_amdgcn_exp2f(ar[3][0][0]); SB();
      PIN(vb); oA[2] = mfma16(__builtin_shufflevector(vb, vb, 2, 3, 4, 5), pf[2], zero4); SB();
      ar[3][0][1] = __builtin_amdgcn_exp2f(ar[3][0][1]); SB();
      ar[3][1][0] = __builtin_amdgcn_exp2f(ar[3][1][0]); SB();
      ar[3][1][1] = __builtin_amdgcn_exp2f(ar[3][1][1]); SB();
      ar[4][0][0] = __builtin_amdgcn_exp2f(ar[4][0][0]); SB();
      ar[4][0][1] = __builtin_amdgcn_exp2f(ar[4][0][1]); SB();
      ar[4][1][0] = __builtin_amdgcn_exp2f(ar[4][1][0]); SB();
      ar[4][1][1] = __builtin_amdgcn_exp2f(ar[4][1][1]); SB();
      pp[3][0] = pk2(ar[3][0][0], ar[3][0][1]); SB();
      pp[3][1] = pk2(ar[3][1][0], ar[3][1][1]); SB();
      pp[4][0] = pk2(ar[4][0][0], ar[4][0][1]); SB();
      pp[4][1] = pk2(ar[4][1][0], ar[4][1][1]); SB();
      inv[0] = __builtin_amdgcn_rcpf(sm[0][0]); PIN(sm[0]); SB();
      inv[1] = __builtin_amdgcn_rcpf(sm[1][0]); PIN(sm[1]); SB();
      inv[2] = __builtin_amdgcn_rcpf(sm[2][0]); PIN(sm[2]); SB();
      pf[3][0] = pp[3][0] & mA[0]; SB();
      pf[3][2] = pp[3][0] & ~mA[0]; SB();
      pf[3][1] = pp[3][1] & mA[1]; SB();
      pf[3][3] = pp[3][1] & ~mA[1]; SB();
      pf[4][0] = pp[4][0] & mA[0]; SB();
      pf[4][2] = pp[4][0] & ~mA[0]; SB();
      sm[3] = mfma16(ones, pf[3], zero4); SB();
      pf[4][1] = pp[4][1] & mA[1]; SB();
      pf[4][3] = pp[4][1] & ~mA[1]; SB();
      tb_[4] = pk2(tx[2][0], tx[2][1]); SB();
      tb_[5] = pk2(tx[2][2], tx[2][3]); SB();
      sm[4] = mfma16(ones, pf[4], zero4); SB();
      nn0[0] = (f32x2){oA[0][0], oA[0][1]} * (f32x2){inv[0], inv[0]}; SB();
      nn0[1] = (f32x2){oA[0][2], oA[0][3]} * (f32x2){inv[0], inv[0]}; SB();
      PIN(tb_); oT[1] = mfma16(__builtin_shufflevector(tb_, tb_, 2, 3, 4, 5), pf[2], zero4); SB();
      ob[0][0] = pk2(nn0[0][0], nn0[0][1]); SB();
      ob[0][1] = pk2(nn0[1][0], nn0[1][1]); SB();
      nn1[0] = (f32x2){oA[1][0], oA[1][1]} * (f32x2){inv[1], inv[1]}; SB();
      nn1[1] = (f32x2){oA[1][2], oA[1][3]} * (f32x2){inv[1], inv[1]}; SB();
      nn2[0] = (f32x2){oA[2][0], oA[2][1]} * (f32x2){inv[2], inv[2]}; SB();
      nn2[1] = (f32x2){oA[2][2], oA[2][3]} * (f32x2){inv[2], inv[2]}; SB();
      vb[6] = pk2(acc[3][2][0], acc[3][2][1]); SB();
      vb[7] = pk2(acc[3][2][2], acc[3][2][3]); SB();
      vb[8] = pk2(acc[4][2][0], acc[4][2][1]); SB();
      vb[9] = pk2(acc[4][2][2], acc[4][2][3]); SB();
      PIN(vb); oA[3] = mfma16(__builtin_shufflevector(vb, vb, 4, 5, 6, 7), pf[3], zero4); SB();
      tb_[6] = pk2(tx[3][0], tx[3][1]); SB();
      tb_[7] = pk2(tx[3][2], tx[3][3]); SB();
      PIN(vb); oA[4] = mfma16(__builtin_shufflevector(vb, vb, 6, 7, 8, 9), pf[4], zero4); SB();
      tb_[8] = pk2(tx[4][0], tx[4][1]); SB();
      tb_[9] = pk2(tx[4][2], tx[4][3]); SB();
      PIN(tb_); oT[0] = mfma16(__builtin_shufflevector(tb_, tb_, 4, 5, 6, 7), pf[3], oT[0]); SB();
      ob[1][0] = pk2(nn1[0][0], nn1[0][1]); SB();
      ob[1][1] = pk2(nn1[1][0], nn1[1][1]); SB();
      PIN(tb_); oT[1] = mfma16(__builtin_shufflevector(tb_, tb_, 6, 7, 8, 9), pf[4], oT[1]); SB();
      ob[2][0] = pk2(nn2[0][0], nn2[0][1]); SB();
      ob[2][1] = pk2(nn2[1][0], nn2[1][1]); SB();
      ob[5][0] = cpyu(ob[0][0]); SB();
      ob[5][1] = cpyu(ob[0][1]); SB();
      nnU[0] = (f32x2){oT[2][0], oT[2][1]} * (f32x2){inv[0], inv[0]}; SB();
      nnU[1] = (f32x2){oT[2][2], oT[2][3]} * (f32x2){inv[0], inv[0]}; SB();
      obU[0] = pk2(nnU[0][0], nnU[0][1]); SB();
      obU[1] = pk2(nnU[1][0], nnU[1][1]); SB();
      { const auto r_ = __builtin_amdgcn_permlane32_swap(ob[0][0], ob[5][0], false, false); os[0][0] = r_[0]; os[0][2] = r_[1]; } SB();
      { const auto r_ = __builtin_amdgcn_permlane32_swap(ob[0][1], ob[5][1], false, false); os[0][1] = r_[0]; os[0][3] = r_[1]; } SB();
      { const auto r_ = __builtin_amdgcn_permlane32_swap(ob[1][0], ob[2][0], false, false); os[1][0] = r_[0]; os[1][2] = r_[1]; } SB();
      { const auto r_ = __builtin_amdgcn_permlane32_swap(ob[1][1], ob[2][1], false, false); os[1][1] = r_[0]; os[1][3] = r_[1]; } SB();
      inv[3] = __builtin_amdgcn_rcpf(sm[3][0]); PIN(sm[3]); SB();
      inv[4] = __builtin_amdgcn_rcpf(sm[4][0]); PIN(sm[4]); SB();
      it = selm(mo, inv[2], inv[1]); SB();
      iu = selm(mo, inv[4], inv[3]); SB();
      nn3[0] = (f32x2){oA[3][0], oA[3][1]} * (f32x2){inv[3], inv[3]}; SB();
      nn3[1] = (f32x2){oA[3][2], oA[3][3]} * (f32x2){inv[3], inv[3]}; SB();
      nn4[0] = (f32x2){oA[4][0], oA[4][1]} * (f32x2){inv[4], inv[4]}; SB();
      nn4[1] = (f32x2){oA[4][2], oA[4][3]} * (f32x2){inv[4], inv[4]}; SB();
      it = selm(mh, iu, it); SB();
      oT[0][0] = selm(mo, oT[1][0], oT[0][0]); SB();
      oT[0][1] = selm(mo, oT[1][1], oT[0][1]); SB();
      oT[0][2] = selm(mo, oT[1][2], oT[0][2]); SB();
      oT[0][3] = selm(mo, oT[1][3], oT[0][3]); SB();
      ob[3][0] = pk2(nn3[0][0], nn3[0][1]); SB();
      ob[3][1] = pk2(nn3[1][0], nn3[1][1]); SB();
      ob[4][0] = pk2(nn4[0][0], nn4[0][1]); SB();
      ob[4][1] = pk2(nn4[1][0], nn4[1][1]); SB();
      nnT[0] = (f32x2){oT[0][0], oT[0][1]} * (f32x2){it, it}; SB();
      nnT[1] = (f32x2){oT[0][2], oT[0][3]} * (f32x2){it, it}; SB();
      obT[0] = pk2(nnT[0][0], nnT[0][1]); SB();
      obT[1] = pk2(nnT[1][0], nnT[1][1]); SB();
      { const auto r_ = __builtin_amdgcn_permlane32_swap(ob[3][0], ob[4][0], false, false); os[2][0] = r_[0]; os[2][2] = r_[1]; } SB();
      { const auto r_ = __builtin_amdgcn_permlane32_swap(ob[3][1], ob[4][1], false, false); os[2][1] = r_[0]; os[2][3] = r_[1]; } SB();
      XBAR(); SB();
      __builtin_amdgcn_raw_buffer_store_b128(os[1], ro, od[1], 0, 16); SB();
      __builtin_amdgcn_raw_buffer_store_b128(os[2], ro, od[2], 0, 16); SB();
      __builtin_amdgcn_raw_buffer_store_b64(obT, ro, od[3], 0, 16); SB();
      __builtin_amdgcn_raw_buffer_store_b128(os[0], ro, od[0], 0, 16); SB();
      __builtin_amdgcn_raw_buffer_store_b64(obU, ro, od[4], 0, 16); SB();
    }
  } else {
    const int f = b - 224;
    args();
    float* red = (float*)smem;
    u16* hs = (u16*)(smem + 32768);
    const bool do1 = f < 27;
    const bool lead = f == 31;
    if (__builtin_expect(lead, 0)) {
      get_tag();
      const __amdgpu_buffer_rsrc_t rk = __builtin_amdgcn_make_buffer_rsrc((void*)(ws + WS_K), 0, 36864, 0x00020000);
      u32x2 kv[2];
      int ko[2];
      ko[0] = (tid >> 2) * 128 + (tid & 3) * 16;
      ko[1] = tid < 384 ? (128 + (tid >> 2)) * 128 + (tid & 3) * 16 : (224 + ((tid - 384) >> 1)) * 128 + (tid & 1) * 16;
      kv[0] = kv[1] = (u32x2){0u, 0u};
#pragma unroll 1
      for (unsigned spins = 0; spins < SPIN_CAP; ++spins) {
        asm volatile("" ::: "memory");
        u32x4 t_[2];
#pragma unroll
        for (int q = 0; q < 2; ++q) t_[q] = __builtin_amdgcn_raw_buffer_load_b128(rk, ko[q], 0, 16);
        bool ok = true;
#pragma unroll
        for (int q = 0; q < 2; ++q)
          if (ko[q] != 0x7FFFFF00) {
            if ((t_[q][1] == tag) & (t_[q][3] == tag)) {
              kv[q] = (u32x2){t_[q][0], t_[q][2]};
              ko[q] = 0x7FFFFF00;
            } else {
              ok = false;
            }
          }
        if (__all(ok)) break;
        __builtin_amdgcn_s_sleep(4);
      }
      unsigned k1 = max(kv[0][0], kv[0][1]), k2 = min(kv[0][0], kv[0][1]);
      top2_mrg(k1, k2, max(kv[1][0], kv[1][1]), min(kv[1][0], kv[1][1]));
      top2_all(k1, k2);
      unsigned* lk = (unsigned*)(smem + L_VAR + 64);
      if (lane == 0) *(u32x2*)(lk + wave * 2) = (u32x2){k1, k2};
      WAITL0();
      BARRIER();
      k1 = 0u;
      k2 = 0u;
#pragma unroll
      for (int q = 0; q < 4; ++q) {
        const u32x4 a_ = *(const u32x4*)(lk + q * 4);
        top2_mrg(k1, k2, a_[0], a_[1]);
        top2_mrg(k1, k2, a_[2], a_[3]);
      }
      if (tid == 0) {
        const __amdgpu_buffer_rsrc_t rr = __builtin_amdgcn_make_buffer_rsrc((void*)(ws + WS_R), 0, 128, 0x00020000);
        __builtin_amdgcn_raw_buffer_store_b128((u32x4){k1, tag, k2, tag}, rr, 0, 0, 16);
        *(u32x2*)(smem + L_VAR + 32) = (u32x2){k1, k2};
      }
    }
    if (__builtin_expect(f == 30, 0)) {
      get_tag();
      if (wave < 4) {
        const __amdgpu_buffer_rsrc_t rf = __builtin_amdgcn_make_buffer_rsrc((void*)(ws + WS_F2), 0, 32768, 0x00020000);
        int fo = tid < 227 ? tid * 128 : 0x7FFFFF00;
#pragma unroll 1
        for (unsigned spins = 0; spins < SPIN_CAP; ++spins) {
          asm volatile("" ::: "memory");
          if (__builtin_amdgcn_raw_buffer_load_b32(rf, fo, 0, 16) == tag) fo = 0x7FFFFF00;
          if (__all(fo == 0x7FFFFF00)) break;
          __builtin_amdgcn_s_sleep(4);
        }
      }
      BARRIER();
      if (tid == 0) {
        const __amdgpu_buffer_rsrc_t rr = __builtin_amdgcn_make_buffer_rsrc((void*)(ws + WS_R), 0, 256, 0x00020000);
        const unsigned t_ = now();
        __builtin_amdgcn_raw_buffer_store_b128((u32x4){t_, tag, t_, tag}, rr, 128, 0, 16);
        *(u32x4*)(smem + L_CAND2) = (u32x4){t_, tag, t_, tag};
      }
    }
    if (__builtin_expect(f >= 27 && f < 30, 0)) {
      get_tag();
#pragma unroll 1
      for (int c = wave * 3 + f - 27; c < 320; c += 24)
        __builtin_amdgcn_raw_buffer_store_b128((u32x4){SENT, SENT, SENT, SENT}, ro, lane * 16, (c >= 160 ? 112 * 20480 : 56 * 20480) + c * 1024, 16);
      WAITV(0);
      BARRIER();
      if (tid == 0) __hip_atomic_store((gu32*)(ws + WS_F2 + (197 + f) * 128), tag, RLX_AGENT);
    }
    const int g0 = wave < 4 ? wave * 3 : 4 + wave * 2, ng = wave < 4 ? 3 : 2;
    u32x4 r1[3][8], wd[2][8];
    int vo2 = (wave - 3) * 256 + c15 * 16 + g4 * 8 * (DM * 2);
    PIN(vo2);
    if (do1) {
      const char* pa = (const char*)(c15 < 8 ? w_gate : w_up) + f * 128 + (c15 & 7) * 16 + (g0 * 32 + g4 * 8) * (FF * 2);
      const GAS char* pp[12];
#pragma unroll
      for (int j = 0; j < 12; ++j) {
        pp[j] = (const GAS char*)pa + ((j >> 2) * 32 + (j & 3) * 2) * (FF * 2) + FF;
        PIN(pp[j]);
      }
      SB();
#pragma unroll
      for (int j = 0; j < 24; ++j)
        if (j < 16 || wave < 4) r1[j >> 3][j & 7] = __builtin_nontemporal_load((const GAS u32x4*)(pp[j >> 1] + ((j & 1) ? FF : -FF)));
    }
    SB();
    BARRIER();
    if (do1 && wave >= 3) {
      const u32x4 r = make_srd(w_down, DM * FF * 2);
#pragma unroll
      for (int j = 0; j < 16; ++j)
        asm volatile("buffer_load_dwordx4 %0, %1, %2, %3 offen nt" : "=v"(wd[j >> 3][j & 7]) : "v"(vo2), "s"(r), "s"((f * 64 + (j >> 3) * 32 + (j & 7)) * (DM * 2)) : "memory");
    }
    SB();
    if (f < 30) get_tag();
    prepB();
    auto transp = [&](u32x4(&w)[8]) {
      u32x4 t[8];
#pragma unroll
      for (int d = 0; d < 4; ++d)
#pragma unroll
        for (int p = 0; p < 4; ++p) {
          t[2 * d][p] = perm_lo(w[2 * p + 1][d], w[2 * p][d]);
          t[2 * d + 1][p] = perm_hi(w[2 * p + 1][d], w[2 * p][d]);
        }
#pragma unroll
      for (int j = 0; j < 8; ++j) {
        w[j] = t[j];
        PIN(w[j]);
      }
    };
    if (do1) {
      transp(r1[0]);
      transp(r1[1]);
      if (wave < 4) transp(r1[2]);
    }
    __builtin_amdgcn_s_waitcnt(0x0F70);
    if (wave == 0 && !lead) {
      res_wait(ws, smem, lane, tag, 0);
      if (do1) {
        WAITL0();
        const u32x2 kq = *(const u32x2*)(smem + L_VAR + 32);
#pragma unroll
        for (int t = 0; t < 2; ++t) {
          const u16* xr = x + (2047 - (int)(__builtin_amdgcn_readfirstlane(kq[t]) & 2047u)) * DM;
          dma16(lane * 16, xr, lds0 + 36864 + t * 1296);
          if (lane < 16) dma16(lane * 16, xr + 512, lds0 + 36864 + t * 1296 + 1024);
        }
        if (EW && lane == 0) dma16_sc1(0, ws + WS_R + 128, lds0 + L_CAND2);
      }
    }
    if (wave == 1) touch_line((const char*)out + tile_off((lane & 31) + (nt2 & 1) * 32) + ((nt2 & 1) ? 0 : 128), lds0 + L_TOUCH + 512);
    if (wave == 1 && f != 30 && !do1) res_wait(ws, smem, lane, tag, 1);
    if (wave < 2) WAITV(0);
    WAITL0();
    BARRIER();
    const unsigned r0_ = now(), c0_ = (unsigned)__builtin_amdgcn_s_memtime();
    if (tid == 128) __hip_atomic_store((gu32*)(ws + WS_A + f * 128), tag, RLX_AGENT);
    bool early = false;
    const __amdgpu_buffer_rsrc_t ryp = __builtin_amdgcn_make_buffer_rsrc((void*)(ws + WS_YP), 0, 27 * 10240, 0x00020000);
    auto pick = [&](const f32x4(&a)[8]) {
      f32x4 v;
#pragma unroll
      for (int q = 0; q < 4; ++q) {
        const float lo = (g4 & 1) ? a[2 + (q >> 1)][q & 1] : a[q >> 1][q & 1], hi = (g4 & 1) ? a[6 + (q >> 1)][q & 1] : a[4 + (q >> 1)][q & 1];
        v[q] = (g4 & 2) ? hi : lo;
      }
      return v;
    };
    if (do1) {
      u32x4 xf[3];
#pragma unroll
      for (int k = 0; k < 3; ++k) xf[k] = *(const u32x4*)(smem + 36864 + (c15 & 1) * 1296 + ((g0 + (k < ng ? k : 0)) * 32 + g4 * 8) * 2);
      const u32x4 rv = *(const u32x4*)(smem + L_CAND2);
      f32x4 acc[8];
#pragma unroll
      for (int ff = 0; ff < 8; ++ff) acc[ff] = (f32x4){0.f, 0.f, 0.f, 0.f};
#pragma unroll
      for (int k = 0; k < 3; ++k)
        if (k < ng) {
#pragma unroll
          for (int j = 0; j < 8; ++j) acc[j] = mfma16(xf[k], r1[k][j], acc[j]);
        }
      *(f32x4*)(red + wave * 256 + c15 * 16 + g4 * 4) = pick(acc);
      WAITL0();
      BARRIER();
      early = EW && __all((rv[1] == tag) & (rv[3] == tag));
      u32x4 wt[2][8];
      if (wave < 2) {
        float gs = 0.f, us = 0.f;
#pragma unroll
        for (int w = 0; w < 8; ++w) {
          gs += red[w * 256 + tid];
          us += red[w * 256 + 128 + tid];
        }
        const float gv = rbf(gs), uv = rbf(us);
        const float hv = rbf(gv * __builtin_amdgcn_rcpf(1.0f + __expf(-gv))) * uv;
        hs[(tid & 1) * 64 + (tid >> 1)] = (u16)pk2(hv, hv);
      } else if (wave >= 3) {
        WAITV(0);
#pragma unroll
        for (int j = 0; j < 16; ++j) {
          wt[j >> 3][j & 7] = wd[j >> 3][j & 7];
          PIN(wt[j >> 3][j & 7]);
        }
        transp(wt[0]);
        transp(wt[1]);
      } else if (early) {
        wo_dma(IC<WC>{}, 25 - WC, 1, 25);
      }
      WAITL0();
      BARRIER();
      if (wave < 3) {
        if (early) wo_dma(IC<(25 - WC) / 3>{}, wave, 3, 25);
        else if (wave == 2) res_wait(ws, smem, lane, tag, 1);
      }
      if (wave >= 3) {
        const u32x4 h0 = *(const u32x4*)(hs + (c15 & 1) * 64 + g4 * 8), h1 = *(const u32x4*)(hs + (c15 & 1) * 64 + 32 + g4 * 8);
#pragma unroll
        for (int j = 0; j < 8; ++j) acc[j] = mfma16(h0, wt[0][j], (f32x4){0.f, 0.f, 0.f, 0.f});
#pragma unroll
        for (int j = 0; j < 8; ++j) acc[j] = mfma16(h1, wt[1][j], acc[j]);
        *(f32x4*)(red + 2048 + wave * 256 + c15 * 16 + g4 * 4) = pick(acc);
        const f32x4 y = *(const f32x4*)(red + 2048 + wave * 256 + lane * 4);
#pragma unroll
        for (int t = 0; t < 2; ++t)
          __builtin_amdgcn_raw_buffer_store_b128((u32x4){__float_as_uint(y[t]), tag, __float_as_uint(y[2 + t]), tag}, ryp,
                                                 ((f * 2 + t) * 640 + (wave - 3) * 128 + lane * 2) * 8, 0, 16);
      }
      WAITL0();
      if (!early) BARRIER();
    }
    if (!early) {
      wo_dma(IC<3>{}, wave, 8, 25);
      if (wave == 0) wo_dma(IC<1>{}, 24, 8, 25);
    }
    if (wave == 1) gate_dma(gate_off(lane));
    if (tid < 8) *(unsigned*)(smem + L_VAR + tid * 4) = 0u;
    if (wave == 2 || (f == 30 && wave == 3)) {
      const unsigned tp = __builtin_amdgcn_readfirstlane(*(const unsigned*)(smem + L_CAND2)), d = wave == 2 ? D1 : DK;
#pragma unroll 1
      for (unsigned spins = 0; spins < 128; ++spins) {
        const unsigned r = now(), c = (unsigned)__builtin_amdgcn_s_memtime();
        if ((r - tp) * (c - c0_) > d * (r - r0_ + 1)) break;
        __builtin_amdgcn_s_sleep(2);
      }
    }
    if (f == 30 && wave == 3) {
      const __amdgpu_buffer_rsrc_t ra = __builtin_amdgcn_make_buffer_rsrc((void*)(ws + WS_A), 0, 4096, 0x00020000);
#pragma unroll 1
      for (unsigned spins = 0; spins < SPIN_CAP; ++spins) {
        asm volatile("" ::: "memory");
        if (__all(__builtin_amdgcn_raw_buffer_load_b32(ra, (lane & 31) * 128, 0, 16) == tag)) break;
        __builtin_amdgcn_s_sleep(8);
      }
      if (lane == 0) __hip_atomic_store((gu32*)(ws + WS_E), tag, RLX_AGENT);
    }
    if (wave == 2) {
      const unsigned t_ = now() + D2;
      const u32x4 v = __builtin_amdgcn_raw_buffer_load_b128(rfl, flag_off(), 0, 16);
      *(u32x4*)(smem + L_FS + lane * 16) = (u32x4){(v[1] == tag ? v[0] : t_) + (ETA_F - ETA_G), tag, 0u, tag};
    }
    if (wave == 0) rows_job();
    if (!early || wave < 3) WAITV(0);
    WAITL0();
    BARRIER();
  }

  {
    const int m = wave & 3, kh = wave >> 2;
    const bool snd = kh != 0;
    const int oo = (rt2 * 4 + m) * 20480 + kh * 8192, ot = (rt2 * 4 + m) * 20480 + 16384 + kh * 2048;
    const int l16 = lane * 16;
    u32x4 fs = *(const u32x4*)(smem + L_FS + l16);
    const u32x4 kk = *(const u32x4*)(smem + L_VAR + 32);
    u32x2 g0_ = *(const u32x2*)(smem + glo);
    u32x4 yw[2][7];
    int yo, y6, yp, yx;
    auto y_prep = [&]() {
      int ly = lane;
      PIN(ly);
      const int cp = (ly & 15) < 10 ? ly & 15 : 0, col = m * 20 + cp * 2 + (ly >> 5);
      yo = (ly >> 4) * 71680 + (nt2 * 80 + m * 20 + cp * 2) * 8;
      y6 = ly >= 48 ? 0x7FFFFF00 : yo;
      yp = (((col >> 4) + 5 - (nt2 & 1)) % 5) * 1024 + (col & 12) * 64 + (col & 3) * 4;
      yx = L_XT + col * 2;
      PIN(yo); PIN(y6); PIN(yp); PIN(yx);
    };
    auto y_srd = [&](int tok) {
      char* wy = ws;
      asm volatile("" : "+s"(wy));
      return make_srd(wy + WS_YP + tok * 5120, 27 * 10240);
    };
    auto y_issue = [&](u32x4(&yv)[7], u32x4 srd, bool again, int j0 = 0, int j1 = 7) {
#pragma unroll
      for (int j = 0; j < 7; ++j) {
        if (j < j0 || j >= j1) continue;
        if (again) asm volatile("buffer_load_dwordx4 %0, %1, %2, %3 offen sc1" : "+v"(yv[j]) : "v"(j < 6 ? yo : y6), "s"(srd), "s"(j * 10240) : "memory");
        else asm volatile("buffer_load_dwordx4 %0, %1, %2, %3 offen sc1" : "=v"(yv[j]) : "v"(j < 6 ? yo : y6), "s"(srd), "s"(j * 10240) : "memory");
      }
    };
    auto y_done = [&](u32x4(&yv)[7], int tok, int row) {
      volatile LAS unsigned* fl = (volatile LAS unsigned*)(LAS char*)(smem + L_VAR + 16 + (row >> 4) * 4);
      volatile LAS float* pe = (volatile LAS float*)(LAS char*)(smem + yp + (row >> 4) * 5120 + (row & 15) * 16);
      unsigned f_, xv_;
      float omg, pv;
      auto rd = [&]() {
        f_ = *fl;
        omg = *(volatile LAS float*)(LAS char*)(smem + L_JUNK + row * 4);
        xv_ = *(volatile LAS u16*)(LAS char*)(smem + yx + row * 160);
        pv = *pe;
      };
      rd();
      const float rsel = *(const float*)(smem + L_JUNK + 256 + tok * 4);
      SB();
      unsigned lo = tag, hi = tag;
      float y[2] = {0.f, 0.f};
#pragma unroll
      for (int j = 0; j < 7; ++j) {
        PIN(yv[j]);
        if (j == 6 && g4 == 3) yv[j][1] = yv[j][3] = tag;
        lo = min(min(lo, yv[j][1]), yv[j][3]);
        hi = max(max(hi, yv[j][1]), yv[j][3]);
        y[0] += __uint_as_float(yv[j][0]);
        PIN(y[0]);
        y[1] += __uint_as_float(yv[j][2]);
        PIN(y[1]);
      }
      if (!__all(lo == hi)) return false;
      const auto a = __builtin_amdgcn_permlane32_swap(__float_as_uint(y[0]), __float_as_uint(y[1]), false, false);
      const float h = __uint_as_float(a[0]) + __uint_as_float(a[1]);
      const auto c = __builtin_amdgcn_permlane16_swap(__float_as_uint(h), __float_as_uint(h), false, false);
      const float r = rbf(rbf(__uint_as_float(c[0]) + __uint_as_float(c[1])) * rsel);
#pragma unroll 1
      for (unsigned spins = 0; spins < (SPIN_CAP << 6) && !__builtin_amdgcn_readfirstlane(f_); ++spins) rd();
      const float xv = bflo(xv_);
      *pe = pv + (rbf(xv + r) - xv) * omg;
      asm volatile("ds_add_u32 %0, %1" ::"v"(fl), "v"(1u) : "memory");
      return true;
    };
    const int l0 = __builtin_amdgcn_readfirstlane(kk[2]), l1 = __builtin_amdgcn_readfirstlane(kk[3]);
    unsigned ry = min((unsigned)l0, (unsigned)l1);
    unsigned need = 1u + ((l0 >> 4) == m ? 256u : 0u) + ((l1 >> 4) == m ? 256u : 0u);
    asm volatile("" : "+s"(ry), "+s"(need));
#define yjob (ry < 64u)
    if (wave == 7) {
      if (__builtin_expect((~__builtin_amdgcn_ballot_w64((fs[1] == tag) & (fs[3] == tag)) & 0xFFull) != 0, 0)) {
        flag_wait(lane >= 8 ? 0x7FFFFF00 : flag_off());
        WAITL0();
        if (lane == 0) *(volatile LAS unsigned*)(LAS char*)(smem + L_VAR + 8) = 1u;
      }
    }
    if (!snd) {
      dma16(adr[0], x, lds0 + L_XT + m * 2560);
      dma16(adr[1], x, lds0 + L_XT + m * 2560 + 1024);
      if (lane < 32) dma16(adr[2], x, lds0 + L_XT + m * 2560 + 2048);
    }
    SB();
    const char* wb = smem + L_WO + kh * 51200 + l16;
    u32x4 wq[WPD][5];
    auto wrd = [&](int k) {
#pragma unroll
      for (int f = 0; f < 5; ++f) wq[k % WPD][f] = *(const u32x4*)(wb + (k * 5 + f) * 1024);
    };
#pragma unroll
    for (int k = 0; k < NPRE; ++k) wrd(k);
    if (snd) {
#pragma unroll
      for (int k = NPRE; k < WPD; ++k) wrd(k);
    }
    if (__builtin_expect(!__all(g0_[1] == tag), 0)) {
      const __amdgpu_buffer_rsrc_t rg_ = __builtin_amdgcn_make_buffer_rsrc((void*)(ws + WS_K), 0, 36864, 0x00020000);
#pragma unroll 1
      for (unsigned spins = 0; spins < SPIN_CAP; ++spins) {
        asm volatile("" ::: "memory");
        g0_ = __builtin_amdgcn_raw_buffer_load_b64(rg_, gate_off(m * 16 + c15), 0, 16);
        if (__all(g0_[1] == tag)) break;
      }
    }
    const float gt = __uint_as_float(g0_[0]);
    SB();
    {
      const u64 inv = ~__builtin_amdgcn_ballot_w64((fs[1] == tag) & (fs[3] == tag));
      if (__builtin_expect((inv & rel) != 0, 0)) {
#pragma unroll 1
        for (unsigned spins = 0; spins < (SPIN_CAP << 6); ++spins) {
          if (__builtin_amdgcn_readfirstlane(*(volatile LAS unsigned*)(LAS char*)(smem + L_VAR + 8))) break;
          __builtin_amdgcn_s_sleep(1);
        }
        fs[0] = *(volatile LAS unsigned*)(LAS char*)(smem + L_FS + l16);
      }
      const unsigned tg = fs[0] + ETA_G;
      unsigned spins = 128;
#pragma unroll 1
      while ((__builtin_amdgcn_ballot_w64((int)(now() - tg) < 0) & rel) != 0 && --spins) {}
    }
    u32x4 of[8];
    u32x2 ofl[2], ofh[2];
    const u32x4 sro = make_srd(ws + WS_OF, S_LEN * DM * 2);
    int l8 = lane * 8;
    asm volatile("buffer_load_dwordx4 %0, %8, %9, %10 offen sc1\n\tbuffer_load_dwordx4 %1, %8, %9, %10 offen offset:1024 sc1\n\t"
                 "buffer_load_dwordx4 %2, %8, %9, %10 offen offset:2048 sc1\n\tbuffer_load_dwordx4 %3, %8, %9, %10 offen offset:3072 sc1\n\t"
                 "buffer_load_dwordx4 %4, %8, %9, %11 offen sc1\n\tbuffer_load_dwordx4 %5, %8, %9, %11 offen offset:1024 sc1\n\t"
                 "buffer_load_dwordx4 %6, %8, %9, %11 offen offset:2048 sc1\n\tbuffer_load_dwordx4 %7, %8, %9, %11 offen offset:3072 sc1"
                 : "=&v"(of[0]), "=&v"(of[1]), "=&v"(of[2]), "=&v"(of[3]), "=&v"(of[4]), "=&v"(of[5]), "=&v"(of[6]), "=&v"(of[7])
                 : "v"(l16), "s"(sro), "s"(oo), "s"(oo + 4096)
                 : "memory");
    asm volatile("buffer_load_dwordx2 %0, %4, %5, %6 offen sc1\n\tbuffer_load_dwordx2 %1, %4, %5, %6 offen offset:512 sc1\n\t"
                 "buffer_load_dwordx2 %2, %4, %5, %6 offen offset:1024 sc1\n\tbuffer_load_dwordx2 %3, %4, %5, %6 offen offset:1536 sc1"
                 : "=&v"(ofl[0]), "=&v"(ofh[0]), "=&v"(ofl[1]), "=&v"(ofh[1])
                 : "v"(l8), "s"(sro), "s"(ot)
                 : "memory");
    if (!snd) {
#pragma unroll
      for (int k = NPRE; k < WPD; ++k) wrd(k);
    }
    auto o_again = [&](int k) {
      if (k < 8)
        asm volatile("buffer_load_dwordx4 %0, %1, %2, %3 offen sc1" : "+v"(of[k]) : "v"(l16), "s"(sro), "s"(oo + k * 1024) : "memory");
      else
        asm volatile("buffer_load_dwordx2 %0, %2, %3, %4 offen sc1\n\tbuffer_load_dwordx2 %1, %2, %3, %4 offen offset:512 sc1"
                     : "+v"(ofl[k - 8]), "+v"(ofh[k - 8])
                     : "v"(l8), "s"(sro), "s"(ot + (k - 8) * 1024)
                     : "memory");
    };
    auto o_pin = [&](int k) {
      if (k < 8) PIN(of[k]);
      else asm volatile("" : "+v"(ofl[k - 8]), "+v"(ofh[k - 8]));
    };
    auto o_get = [&](int k) { return k < 8 ? of[k] : (u32x4){ofl[k - 8][0], ofl[k - 8][1], ofh[k - 8][0], ofh[k - 8][1]}; };
    auto o_bad = [&](int k) {
      const u32x4 v = o_get(k);
      return __any(max(max(v[0], v[1]), max(v[2], v[3])) == SENT);
    };
    int pa_ = m * 5120 + l16;
    PIN(pa_);
    volatile LAS unsigned* pfl = (volatile LAS unsigned*)(LAS char*)(smem + L_VAR + 16 + m * 4);
    u32x4 rout = make_srd(out, S_LEN * DM * 2);
    asm volatile("" : "+s"(rout));
    f32x4 ac2[5], pp[5];
    unsigned pf_ = 0;
    if (!snd) {
      u32x2 xb[5];
      if (__builtin_expect(yjob, 0)) y_prep();
      WAITV(12);
#pragma unroll
      for (int f = 0; f < 5; ++f) xb[f] = *(const u32x2*)(smem + L_XT + m * 2560 + c15 * 160 + g4 * 8 + ((nt2 & 1) ? (f + 1) % 5 : f) * 32);
      const float omg = 1.0f - gt;
      *(float*)(smem + L_JUNK + (m * 16 + c15) * 4) = omg;
#pragma unroll
      for (int f = 0; f < 5; ++f) {
        pp[f] = (f32x4){bflo(xb[f][0]), bfhi(xb[f][0]), bflo(xb[f][1]), bfhi(xb[f][1])} * omg;
        PIN(pp[f]);
      }
    }
#pragma unroll
    for (int f = 0; f < 5; ++f) ac2[f] = (f32x4){0.f, 0.f, 0.f, 0.f};
    __builtin_amdgcn_sched_barrier(0);
    constexpr int GS[5] = {0, 2, 5, 8, 10};
    auto vleft = [](int k) { return k < 8 ? 11 - k : k == 8 ? 2 : 0; };
    WAITV(10);
    o_pin(0);
    o_pin(1);
    bool bad = o_bad(0) | o_bad(1);
#pragma unroll
    for (int g = 0; g < 4; ++g) {
      if (__builtin_expect(bad, 0)) {
        WAITV(0);
#pragma unroll 1
        for (unsigned spins = 0; spins < SPIN_CAP; ++spins) {
          bool any = false;
#pragma unroll
          for (int k2 = GS[g]; k2 < 10; ++k2) {
            o_pin(k2);
            if (o_bad(k2)) {
              o_again(k2);
              if (k2 < GS[g + 1]) any = true;
            }
          }
          WAITV(0);
          if (!any) break;
        }
      }
      if (g == 3) break;
      unsigned mxv = 0;
      const int nm = (GS[g + 1] - GS[g]) * 5, nn = g < 3 ? GS[g + 2] - GS[g + 1] : 0;
#pragma unroll
      for (int i = 0; i < nm; ++i) {
        const int k = GS[g] + i / 5, f = i % 5;
        const int j = i - (nm - 2 - 2 * nn);
        const int kn = GS[g + 1] + j / 2;
        if (j >= 0 && j < 2 * nn && !(j & 1)) {
          WAITV_(vleft(kn));
          o_pin(kn);
          SB();
        }
        ac2[f] = mfma16(wq[k % WPD][f], o_get(k), ac2[f]);
        SB();
        if (k + WPD < 10) wq[k % WPD][f] = *(const u32x4*)(wb + ((k + WPD) * 5 + f) * 1024);
        if (j >= 0 && j < 2 * nn) {
          const u32x4 nv = o_get(kn);
          mxv = max(max(mxv, nv[(j & 1) * 2]), nv[(j & 1) * 2 + 1]);
        }
        if (nn && j == 2 * nn) bad = __any(mxv == SENT);
        if (g == 2 && i == PRD - 1) __builtin_amdgcn_s_waitcnt(0xC07F);
        if (g == 2 && i == PRD && snd) {
          pf_ = *pfl;
          asm volatile("" ::: "memory");
#pragma unroll
          for (int f = 0; f < 5; ++f) pp[f] = *(const f32x4*)(smem + pa_ + f * 1024);
        }
        SB();
      }
    }
    u32x4 o8 = o_get(8), o9 = o_get(9);
    PIN(o8); PIN(o9);
    auto mm = [&](int f) {
      ac2[f] = mfma16(wq[8 % WPD][f], o8, ac2[f]);
      SB();
      ac2[f] = mfma16(wq[9 % WPD][f], o9, ac2[f]);
      SB();
    };
    auto fin = [&](int f, int q) {
      pp[f][q] = __builtin_fmaf(ac2[f][q], gt, pp[f][q]);
      PIN(pp[f][q]);
    };
    if (!snd) {
      SB();
      auto wr = [&](int f) {
#pragma unroll
        for (int q = 0; q < 4; ++q) fin(f, q);
        *(f32x4*)(smem + pa_ + f * 1024) = pp[f];
        SB();
      };
      auto done = [&]() {
        asm volatile("" ::: "memory");
        *pfl = 1u;
      };
      if (__builtin_expect(yjob, 0)) {
        int tok = (unsigned)l0 >= 64u;
        const u32x4 sy = y_srd(tok);
        mm(0); y_issue(yw[0], sy, false, 0, 2); SB(); mm(1); y_issue(yw[0], sy, false, 2, 4); SB(); mm(2); y_issue(yw[0], sy, false, 4, 7); SB();
        if (max((unsigned)l0, (unsigned)l1) < 64u) y_issue(yw[1], y_srd(1), false);
        wr(0); mm(3); wr(1); mm(4); wr(2); wr(3); wr(4);
        done();
        WAITV(0);
#pragma unroll
        for (int j = 0; j < 7; ++j) PIN(yw[1][j]);
#pragma unroll 1
        for (unsigned spins = 0; spins < SPIN_CAP; ++spins) {
          if (y_done(yw[0], tok, tok ? l1 : l0)) {
            if (tok || (unsigned)l1 >= 64u) break;
            tok = 1;
#pragma unroll
            for (int j = 0; j < 7; ++j) yw[0][j] = yw[1][j];
          } else {
            y_issue(yw[0], y_srd(tok), true);
            WAITV(0);
          }
        }
      } else {
        mm(0); mm(1); mm(2);
        wr(0); mm(3); wr(1); mm(4);
        wr(2); wr(3); wr(4);
        done();
      }
    } else {
      SB();
      u32x2 r[5];
      auto cv = [&](int f) {
#pragma unroll
        for (int q = 0; q < 4; ++q) fin(f, q);
        r[f] = (u32x2){pk2(pp[f][0], pp[f][1]), pk2(pp[f][2], pp[f][3])};
        SB();
      };
      auto st = [&](int f) {
        const auto a = __builtin_amdgcn_permlane16_swap(r[f][0], r[f + 1][0], false, false);
        const auto c = __builtin_amdgcn_permlane16_swap(r[f][1], r[f + 1][1], false, false);
        asm volatile("buffer_store_dwordx4 %0, %1, %2, 0 offen offset:%3 sc1\n\ts_nop 1" ::"v"((u32x4){a[0], c[0], a[1], c[1]}), "v"(adr[0]), "s"(rout), "i"(f * 32) : "memory");
        SB();
      };
      if (__builtin_expect(__builtin_amdgcn_readfirstlane(pf_) != need, 0)) {
        mm(0); mm(1); mm(2); mm(3); mm(4);
#pragma unroll 1
        for (unsigned spins = 0; spins < (SPIN_CAP << 6); ++spins) {
          pf_ = *pfl;
          asm volatile("" ::: "memory");
#pragma unroll
          for (int f = 0; f < 5; ++f) pp[f] = *(const f32x4*)(smem + pa_ + f * 1024);
          asm volatile("" ::: "memory");
          if (__builtin_amdgcn_readfirstlane(pf_) == need) break;
        }
        cv(0); cv(1);
      } else {
        mm(0); mm(1); mm(2);
        cv(0); mm(3); cv(1); mm(4);
      }
      st(0); cv(2); cv(3); st(2); cv(4);
      asm volatile("buffer_store_dwordx2 %0, %1, %2, 0 offen sc1" ::"v"(r[4]), "v"(adr[1]), "s"(rout) : "memory");
    }
  }
}

extern "C" void kernel_launch(void* const* d_in, const int* in_sizes, int n_in, void* d_out, int out_size, void* d_ws,
                              size_t ws_size, hipStream_t stream) {
  kf<<<256, 512, 0, stream>>>((const u16*)d_in[0], (const u16*)d_in[1], (const u16*)d_in[2], (const u16*)d_in[4],
                              (const u16*)d_in[8], (const u16*)d_in[5], (const u16*)d_in[6], (const u16*)d_in[7],
                              (char*)d_ws, (u16*)d_out);
}
```

```cpp
#include <hip/hip_runtime.h>
#include <stdint.h>

typedef unsigned short u16;
typedef unsigned long long u64;
typedef __attribute__((ext_vector_type(8))) __bf16 bf16x8;
typedef __attribute__((ext_vector_type(2))) __bf16 bf16x2;
typedef __attribute__((ext_vector_type(4))) short s16x4;
typedef __attribute__((ext_vector_type(4))) float f32x4;
typedef __attribute__((ext_vector_type(2))) float f32x2;
typedef __attribute__((ext_vector_type(4))) unsigned u32x4;
typedef __attribute__((ext_vector_type(2))) unsigned u32x2;
typedef __attribute__((ext_vector_type(10))) unsigned u32x10;
#define GAS __attribute__((address_space(1)))
#define LAS __attribute__((address_space(3)))
typedef GAS unsigned gu32;

#define S_LEN 2048
#define DM 640
#define NQKV 1920
#define FF 1728

#define WS_OF 0
#define WS_WOF 2621440
#define WS_E (WS_WOF + 819200)
#define WS_F2 (WS_E + 256)
#define WS_F (WS_F2 + 32768)
#define WS_K (WS_F + 28672)
#define WS_R (WS_K + 36864)
#define WS_A (WS_R + 256)
#define WS_YP (WS_A + 4096)
#define TAG_STEP 0x9E3779B9u
#define SENT 0xFFFFFFFFu
#define SPIN_CAP (1u << 18)
#define NPRE 3
#define WPD 7
#define PRD 10
#define WB8 2
#define TCY 2750
#define ETA_G 10
#define ETA_F 15
#define D1 6700
#define D2 75
#define DK 4200
#define RLX_AGENT __ATOMIC_RELAXED, __HIP_MEMORY_SCOPE_AGENT

__device__ __forceinline__ float bflo(unsigned u) { return __uint_as_float(u << 16); }
__device__ __forceinline__ float bfhi(unsigned u) { return __uint_as_float(u & 0xFFFF0000u); }
__device__ __forceinline__ unsigned pk2(float a, float b) {
  f32x2 v = {a, b};
  return __builtin_bit_cast(unsigned, __builtin_convertvector(v, bf16x2));
}
__device__ __forceinline__ float rbf(float f) { return bflo(pk2(f, f)); }
__device__ __forceinline__ unsigned perm_lo(unsigned hi_src, unsigned lo_src) {
  return __builtin_amdgcn_perm(hi_src, lo_src, 0x05040100u);
}
__device__ __forceinline__ unsigned perm_hi(unsigned hi_src, unsigned lo_src) {
  return __builtin_amdgcn_perm(hi_src, lo_src, 0x07060302u);
}
__device__ __forceinline__ f32x4 mfma16(u32x4 a, u32x4 b, f32x4 c) {
  return __builtin_amdgcn_mfma_f32_16x16x32_bf16(__builtin_bit_cast(bf16x8, a), __builtin_bit_cast(bf16x8, b), c, 0, 0, 0);
}
__device__ __forceinline__ u32x2 tr_read(const char* p) {
  return __builtin_bit_cast(u32x2, __builtin_amdgcn_ds_read_tr16_b64_v4i16((LAS s16x4*)(p)));
}
#define BARRIER() asm volatile("s_barrier" ::: "memory")
#define WAITV(n) asm volatile("s_waitcnt vmcnt(" #n ")" ::: "memory")
#define WAITV_(n) asm volatile("s_waitcnt vmcnt(%0)" ::"i"(n) : "memory")
#define WAITL0() asm volatile("s_waitcnt lgkmcnt(0)" ::: "memory")

__device__ __forceinline__ void top2_mrg(unsigned& k1, unsigned& k2, unsigned b1, unsigned b2) {
  k2 = max(max(k2, b2), min(k1, b1));
  k1 = max(k1, b1);
}
template <int CTRL>
__device__ __forceinline__ void top2_dpp(unsigned& k1, unsigned& k2) {
  const unsigned b1 = (unsigned)__builtin_amdgcn_update_dpp(0, (int)k1, CTRL, 0xF, 0xF, true);
  const unsigned b2 = (unsigned)__builtin_amdgcn_update_dpp(0, (int)k2, CTRL, 0xF, 0xF, true);
  top2_mrg(k1, k2, b1, b2);
}
__device__ __forceinline__ void top2_row(unsigned& k1, unsigned& k2) {
  top2_dpp<0xB1>(k1, k2);
  top2_dpp<0x4E>(k1, k2);
  top2_dpp<0x141>(k1, k2);
  top2_dpp<0x140>(k1, k2);
}
__device__ __forceinline__ void top2_all(unsigned& k1, unsigned& k2) {
  top2_row(k1, k2);
  {
    auto a = __builtin_amdgcn_permlane16_swap(k1, k1, false, false);
    auto c = __builtin_amdgcn_permlane16_swap(k2, k2, false, false);
    k1 = a[0];
    k2 = c[0];
    top2_mrg(k1, k2, a[1], c[1]);
  }
  {
    auto a = __builtin_amdgcn_permlane32_swap(k1, k1, false, false);
    auto c = __builtin_amdgcn_permlane32_swap(k2, k2, false, false);
    k1 = a[0];
    k2 = c[0];
    top2_mrg(k1, k2, a[1], c[1]);
  }
}
__device__ __forceinline__ void touch_line(const char* p, unsigned lds_off) {
  asm volatile("s_mov_b32 m0, %1\n\ts_nop 0\n\tglobal_load_lds_dword %0, off" ::"v"(p), "s"(lds_off) : "memory");
}
__device__ __forceinline__ void dma16(unsigned voff, const void* sbase, unsigned lds_off) {
  asm volatile("s_mov_b32 m0, %2\n\ts_nop 0\n\tglobal_load_lds_dwordx4 %0, %1" ::"v"(voff), "s"(sbase), "s"(lds_off) : "memory");
}
__device__ __forceinline__ void dma16_sc1(unsigned voff, const void* sbase, unsigned lds_off) {
  asm volatile("s_mov_b32 m0, %2\n\ts_nop 0\n\tglobal_load_lds_dwordx4 %0, %1 sc1" ::"v"(voff), "s"(sbase), "s"(lds_off) : "memory");
}
__device__ __forceinline__ void bdma16(unsigned voff, u32x4 srd, unsigned soff, unsigned lds_off) {
  asm volatile("s_mov_b32 m0, %3\n\ts_nop 0\n\tbuffer_load_dwordx4 %0, %1, %2 offen lds" ::"v"(voff), "s"(srd), "s"(soff), "s"(lds_off) : "memory");
}
__device__ __forceinline__ u32x4 make_srd(const void* p, unsigned bytes) {
  return (u32x4){(unsigned)(uintptr_t)p, (unsigned)((uintptr_t)p >> 32) & 0xFFFFu, bytes, 0x00020000u};
}
__device__ __forceinline__ float key_val(unsigned key) {
  unsigned u = key >> 16;
  u ^= (u & 0x8000u) ? 0x8000u : 0xFFFFu;
  return bflo(u);
}
__device__ __forceinline__ float fmx(float a, float b) { return __builtin_elementwise_maximum(a, b); }
#define SB() __builtin_amdgcn_sched_barrier(0)
__device__ __forceinline__ f32x4 mfma16h(u32x2 a, u32x2 b, f32x4 c) {
  return __builtin_amdgcn_mfma_f32_16x16x16bf16_1k(__builtin_bit_cast(s16x4, a), __builtin_bit_cast(s16x4, b), c, 0, 0, 0);
}

#define SLOT_B 43008
#define L_WO SLOT_B
#define L_EARLY (3 * SLOT_B)
#define L_PIECE L_EARLY
#define L_CAND (L_VAR + 128)
#define L_TOUCH (L_EARLY + 12288)
#define L_JUNK (L_WO + 102400)
#define L_VAR (L_JUNK + 384)
#define L_CAND2 (L_VAR + 48)
#define L_GT (L_VAR + 256)
#define L_FS (L_GT + 1024)
#define L_XT (L_FS + 1024)
#define L_TOTAL (L_XT + 10240)
#define CODE_LINES 255

__device__ __forceinline__ void res_wait(char* ws, char* smem, int lane, unsigned tag, int which) {
  const __amdgpu_buffer_rsrc_t r = __builtin_amdgcn_make_buffer_rsrc((void*)(ws + WS_R), 0, 256, 0x00020000);
  u32x4 v;
#pragma unroll 1
  for (unsigned spins = 0; spins < SPIN_CAP; ++spins) {
    asm volatile("" ::: "memory");
    v = __builtin_amdgcn_raw_buffer_load_b128(r, which * 128, 0, 16);
    if (__all((v[1] == tag) & (v[3] == tag))) break;
    __builtin_amdgcn_s_sleep(2);
  }
  if (lane == 0 && which == 0) *(u32x2*)(smem + L_VAR + 32) = (u32x2){v[0], v[2]};
  if (lane == 0 && which == 1) *(u32x4*)(smem + L_CAND2) = v;
}
__device__ __forceinline__ float dot2(unsigned a, unsigned b, float c) {
  return __builtin_amdgcn_fdot2_f32_bf16(__builtin_bit_cast(bf16x2, a), __builtin_bit_cast(bf16x2, b), c, false);
}
template <int CTRL>
__device__ __forceinline__ float dpp_add(float v) {
  return v + __int_as_float(__builtin_amdgcn_update_dpp(0, __float_as_int(v), CTRL, 0xF, 0xF, true));
}
__device__ __forceinline__ float row_sum(float v) {
  v = dpp_add<0xB1>(v);
  v = dpp_add<0x4E>(v);
  v = dpp_add<0x141>(v);
  return dpp_add<0x140>(v);
}

template <int N> struct IC { static constexpr int value = N; };
#define PIN(v) asm volatile("" : "+v"(v))
__global__ __launch_bounds__(512, 2) void kf(const u16*, const u16*, const u16*, const u16*, const u16*, const u16*, const u16*, const u16*, char*, u16*) {
  __shared__ __attribute__((aligned(16))) char smem[L_TOTAL];
  typedef const __attribute__((address_space(4))) u64* karg_t;
  const karg_t kp0 = (karg_t)__builtin_amdgcn_kernarg_segment_ptr();
  u64 pc;
  asm volatile("s_load_dword s100, %1, 0x0\n\ts_load_dword s101, %1, 0x40\n\ts_getpc_b64 %0" : "=s"(pc) : "s"(kp0) : "s100", "s101");
  const u16 *x, *wqkv, *wo, *w_r, *gate_w, *w_gate, *w_up, *w_down;
  char* ws;
  u16* out;
  auto args = [&]() {
    SB();
    karg_t kp = kp0;
    asm volatile("" : "+s"(kp));
    x = (const u16*)kp[0]; wqkv = (const u16*)kp[1]; wo = (const u16*)kp[2]; w_r = (const u16*)kp[3]; gate_w = (const u16*)kp[4];
    w_gate = (const u16*)kp[5]; w_up = (const u16*)kp[6]; w_down = (const u16*)kp[7]; ws = (char*)kp[8]; out = (u16*)kp[9];
  };
  const int tid = threadIdx.x, b = blockIdx.x;
  {
    const int ln = (b >> 3) + 32 * tid;
    if (ln < CODE_LINES)
      touch_line((const char*)((pc & ~255ull) + ln * 128), (unsigned)(uintptr_t)(LAS char*)smem + L_TOUCH);
  }
  unsigned tag;
  auto get_tag = [&]() {
    asm volatile("s_load_dword %0, %1, 0x0\n\ts_waitcnt lgkmcnt(0)" : "=s"(tag) : "s"(ws + WS_E) : "memory");
    tag += TAG_STEP;
  };
  const int lane = tid & 63;
  const int wave = __builtin_amdgcn_readfirstlane(tid >> 6);
  const int xcd = b & 7, jj = b >> 3;
  const int c15 = lane & 15, g4 = lane >> 4;
  const bool gemm_blk = jj < 28;
  const int q_ = (xcd & 3) * 28 + jj;
  const int rg = q_ >> 2, hg = (xcd >> 2) * 4 + (q_ & 3);
  const int par = rg >= 14, gi = rg - par * 14;
  const int tb = min(73 * gi, 944);
  const int tlo = gi == 0 ? 0 : 73 * gi + 7;
  const int lg = jj >> 2;
  const int rt2 = gemm_blk ? par * 16 + gi : (xcd & 2) * 8 + 14 + (xcd & 1), nt2 = hg;
  const unsigned lds0 = (unsigned)(uintptr_t)(LAS char*)smem;
  auto wo_dma = [&](auto n_, int i0, int st, int lo) {
    const u32x4 srd = make_srd(ws + WS_WOF + nt2 * 102400, 102400);
    int g = __builtin_amdgcn_readfirstlane((i0 + ((lg * (25 - lo)) >> 3)) * 4096);
#pragma unroll
    for (int i = 0; i < decltype(n_)::value; ++i) {
      if (g >= 102400) g -= (25 - lo) * 4096;
      asm volatile("s_add_u32 m0, %3, %2\n\ts_nop 0\n\tbuffer_load_dwordx4 %0, %1, %2 offen sc1 lds\n\t"
                   "buffer_load_dwordx4 %0, %1, %2 offen offset:1024 sc1 lds\n\tbuffer_load_dwordx4 %0, %1, %2 offen offset:2048 sc1 lds\n\t"
                   "buffer_load_dwordx4 %0, %1, %2 offen offset:3072 sc1 lds" ::"v"(lane * 16), "s"(srd), "s"(g), "s"(lds0 + L_WO)
                   : "memory", "scc");
      g += st * 4096;
    }
  };

  int od[5];
  const int pa = tb + c15 - 64 * gi;
  const bool pv = tb + c15 >= tlo;
  auto prepO = [&]() {
    const int h = hg * 4 + (wave & 3);
    const int b0 = (par * 64 + 56 + (pa >> 4)) * 20480, b1 = (par * 64 + gi * 4) * 20480;
    od[0] = (pv && lane < 32) ? b0 + (2 * h + g4) * 256 + (pa & 15) * 16 : 0x7FFFFF00;
    od[1] = b1 + (lane >> 5) * 20480 + (2 * h + (g4 & 1)) * 256 + c15 * 16;
    od[2] = od[1] + 2 * 20480;
    od[3] = b1 + g4 * 20480 + 16384 + h * 128 + c15 * 8;
    od[4] = (pv && g4 == 3) ? b0 + 16384 + h * 128 + (pa & 15) * 8 : 0x7FFFFF00;
#pragma unroll
    for (int e = 0; e < 5; ++e) PIN(od[e]);
  };
  auto row_t = [&](int row) {
    const int a = (xcd & 1) * 64 + row;
    return gemm_blk ? tb + 16 + row : a + (a < 16 ? 0 : (a - 7) / 9) * 64;
  };
  int adr[3], glo;
  u64 rel;
  auto tile_off = [&](int row) { return ((2 * row_t(row) + (rt2 >> 4)) * DM + nt2 * 80) * 2; };
  auto prepB = [&]() {
    const int row = (wave & 3) * 16 + c15;
    if (wave < 4) {
#pragma unroll
      for (int e = 0; e < 3; ++e) {
        const int c = min(e * 64 + lane, 159), r = c / 10;
        adr[e] = tile_off((wave & 3) * 16 + r) + (c - r * 10) * 16;
      }
    } else {
      adr[2] = tile_off(row);
      adr[0] = adr[2] + (nt2 & 1) * 32 + (g4 & 1) * 32 + (g4 >> 1) * 16;
      adr[1] = adr[2] + ((nt2 & 1) ? 0 : 128) + g4 * 8;
    }
    PIN(adr[0]); PIN(adr[1]); PIN(adr[2]);
    glo = L_GT + row * 16 + (((gemm_blk ? tb : 0) + row) & 1) * 8;
    PIN(glo);
    {
      const int a0 = (xcd & 1) * 64 + (wave & 3) * 16, gl = (xcd & 1) ? 6 + (lane >> 3) : min(lane >> 3, 6);
      rel = __builtin_amdgcn_ballot_w64(((lane >> 2) & 1) == (wave >> 2) && (gemm_blk ? lane < 8 : gl >= (a0 < 16 ? 0 : (a0 - 7) / 9) && gl <= (a0 + 8) / 9));
      asm volatile("" : "+s"(rel));
    }
  };
  auto lrow_of = [&](unsigned key) {
    const int i = 2047 - (int)(key & 0xFFFFu);
    const int t = i >> 1, g = max(t - 7, 0) / 73, rho = t - min(73 * g, 944);
    return (i & 1) == (rt2 >> 4) ? (rho >= 16 ? g * 64 + rho - 16 : 896 + t - 64 * g) - (rt2 & 15) * 64 : -1;
  };
  auto rows_job = [&]() {
    if (lane < 2) {
      const unsigned key = *(const unsigned*)(smem + L_VAR + 32 + lane * 4);
      *(int*)(smem + L_VAR + 40 + lane * 4) = lrow_of(key);
      *(float*)(smem + L_JUNK + 256 + lane * 4) = rbf(__builtin_amdgcn_rcpf(1.0f + __expf(-key_val(key))));
    }
  };
  auto gate_off = [&](int row) {
    const int p2 = rt2 >> 4, tg = row_t(row);
    const int q2 = p2 * 2 + (tg >> 9), u = tg & 511;
    return (u < 448 ? (q2 * 56 + (u >> 3)) * 128 + (u & 7) * 8 : (224 + q2 * 16 + ((u - 448) >> 2)) * 128 + (u & 3) * 8) + 64;
  };
  auto flag_off = [&]() {
    const int g = gemm_blk ? gi : (xcd & 1) ? 6 + (lane >> 3) : min(lane >> 3, 6);
    return (((rt2 >> 4) * 14 + g) * 8 + (lane & 7)) * 128 + 56;
  };
#define rfl __builtin_amdgcn_make_buffer_rsrc((void*)(ws + WS_F), 0, 28672, 0x00020000)
  auto gate_dma = [&](int go) { dma16_sc1(go & ~15, ws + WS_K, lds0 + L_GT); };
  auto now = []() { return (unsigned)__builtin_amdgcn_s_memrealtime(); };
  auto flag_wait = [&](int foff) {
#pragma unroll 1
    for (unsigned spins = 0; spins < SPIN_CAP; ++spins) {
      asm volatile("" ::: "memory");
      const u32x4 v = __builtin_amdgcn_raw_buffer_load_b128(rfl, foff, 0, 16);
      if ((v[1] == tag) & (v[3] == tag) & (foff != 0x7FFFFF00)) {
        *(u32x4*)(smem + L_FS + lane * 16) = v;
        foff = 0x7FFFFF00;
      }
      if (__all(foff == 0x7FFFFF00)) break;
      __builtin_amdgcn_s_sleep(8);
    }
  };
#define ro __builtin_amdgcn_make_buffer_rsrc((void*)(ws + WS_OF), 0, S_LEN * DM * 2, 0x00020000)

  if (__builtin_expect(gemm_blk, 1)) {
    if (__builtin_expect(wave >= 4, 1)) {
      const int hw = wave - 4;
      const int rot = (lg % 5) * 2 + (xcd & 1);
      int voffA = (lane >> 3) * (DM * 4) + (((lane & 7) ^ (((hw & 1) * 4 + g4) & 7)) * 16);
      int sA[3];
#pragma unroll
      for (int v = 0; v < 3; ++v) {
        sA[v] = ((tb + 8 * (hw + 4 * v)) * 2 + par) * (DM * 2);
      }
      const int hi = lane >> 5;
      int pair = ((lane & 31) >> 1) ^ (hi | ((hw & 1) << 1) | ((hw >> 1) << 2));
      const int sec = (pair >= 5) + (pair >= 10);
      int voffW = pair == 15 ? 0x7FFFFF00 : hi * (NQKV * 2) + sec * (1280 - 160) + hg * 160 + pair * 32 + ((lane & 1) ^ (sec == 2)) * 16;
      const int n0 = (hw & 1) + 4 * (hw >> 1);
      PIN(voffA); PIN(voffW);
      args();
      const u32x4 rsx = make_srd(x, S_LEN * DM * 2), rsw = make_srd(wqkv, DM * NQKV * 2);
      auto issue = [&](int i, int buf) {
        int kc = rot + (i < 5 ? 2 * i : 2 * i - 9);
        if (kc >= 10) kc -= 10;
        const unsigned dst = lds0 + buf * SLOT_B;
#pragma unroll
        for (int v = 0; v < 3; ++v)
          if (v < 2 || hw < 2) bdma16(voffA, rsx, sA[v] + kc * 128, dst + (hw + 4 * v) * 1024);
        const int so = (kc * 64 + 2 * n0) * (NQKV * 2);
#pragma unroll
        for (int u = 0; u < 8; ++u) {
          const int dn = 2 * (u & 1) + 8 * (u >> 1);
          bdma16(voffW, rsw, so + dn * 2 * (NQKV * 2), dst + 10240 + (n0 + dn) * 1024);
        }
      };
      const int qp = rg * 8 + hg;
      issue(0, 0);
      SB();
      int md = (par * 64 + gi * 4 + 2 * (hw & 1) + (lane >> 5)) * 20480 + 16384 + hg * 512 + (lane & 31) * 16;
      PIN(md);
      int so_ = hw == 0 ? flag_off() : gate_off(lane);
      PIN(so_);
      prepB();
      __builtin_amdgcn_sched_barrier(0);
      issue(1, 1);
      get_tag();
      WAITV(10);
      BARRIER();
      issue(2, 2);
      WAITV(10);
      BARRIER();
      const unsigned r0_ = now(), c0_ = (unsigned)__builtin_amdgcn_s_memtime();
      {
        int buf = 0;
#pragma unroll 1
        for (int i = 3; i < 9; ++i) {
          const bool ex = i == 4 && hw == 2;
          if (ex && lane == 0) __hip_atomic_store((gu32*)(ws + WS_F2 + b * 128), tag, RLX_AGENT);
          issue(i, buf);
          if (i == 3)
            asm volatile("s_load_dword s100, %0, 0x0\n\ts_load_dword s100, %0, 0xa00\n\ts_load_dword s100, %0, 0x1400\n\ts_load_dword s100, %0, 0x1e00\n\t"
                         "s_load_dword s100, %0, 0x2800\n\ts_load_dword s100, %0, 0x3200\n\ts_load_dword s100, %0, 0x3c00\n\ts_load_dword s100, %0, 0x4600" ::"s"(
                             (const char*)out + tile_off(hw * 8 + (nt2 & 1) * 32) + ((nt2 & 1) ? 0 : 128))
                         : "s100");
          if (ex) WAITV(11); else WAITV(10);
          buf = buf == 2 ? 0 : buf + 1;
          BARRIER();
        }
      }
      if (hw == 3) {
        if (lane < 2) dma16_sc1(lane * 128, ws + WS_R, lds0 + L_CAND);
        if (lane < 8) *(unsigned*)(smem + L_VAR + lane * 4) = 0u;
      }
      issue(9, 0);
      auto mark = [&]() {
        const u32x4 sent4 = {SENT, SENT, SENT, SENT};
        const int mo_ = (par * 64 + gi * 4 + (hw & 1) * 2) * 20480 + hg * 2048;
#pragma unroll
        for (int e = 0; e < 5; ++e) {
          if (e == 0) __builtin_amdgcn_raw_buffer_store_b128(sent4, ro, md, 0, 16);
          else __builtin_amdgcn_raw_buffer_store_b128(sent4, ro, lane * 16 + ((e - 1) & 1) * 1024, mo_ + ((e - 1) >> 1) * 20480, 16);
        }
        SB();
      };
      auto words = [&]() {
        const u32x4 rv = *(const u32x4*)(smem + L_CAND + (lane & 1) * 16);
        if (__all((rv[1] == tag) & (rv[3] == tag))) {
          if (tid == 256) *(u32x2*)(smem + L_VAR + 32) = (u32x2){rv[0], rv[2]};
        } else {
          res_wait(ws, smem, lane, tag, 0);
          res_wait(ws, smem, lane, tag, 1);
        }
      };
      if (hw >= 2) {
        mark();
        WAITV(15);
        BARRIER();
        WAITV(5);
        BARRIER();
        unsigned t0_ = now();
        t0_ += (unsigned)((float)(t0_ - r0_) * (float)TCY * __builtin_amdgcn_rcpf((float)((unsigned)__builtin_amdgcn_s_memtime() - c0_)));
        WAITV(0);
        __builtin_amdgcn_raw_buffer_store_b128((u32x4){t0_, tag, t0_, tag}, rfl, lane < 4 ? qp * 128 + (hw & 1) * 64 + lane * 16 : 0x7FFFFF00, 0, 16);
        if (hw == 2) rows_job();
      } else {
        WAITV(10);
        BARRIER();
        words();
        wo_dma(IC<WB8>{}, hw, 2, 25);
        WAITV_(4 * WB8);
        WAITL0();
        BARRIER();
        if (hw == 1) gate_dma(so_);
        wo_dma(IC<12 - WB8>{}, 2 * WB8 + hw, 2, 2 * WB8);
        if (hw == 0 && lane < 8) dma16_sc1(so_, ws + WS_F, lds0 + L_FS);
        if (hw == 0) wo_dma(IC<1>{}, 24, 2, 2 * WB8);
        WAITV(0);
      }
      WAITL0();
      BARRIER();
    } else {
      const int xb_ = b - 160;
      const bool wjob = wave < 2 || (wave == 2 && (unsigned)xb_ < 64u);
      const int xi = (xb_ >> 3) * 2 + (xcd >> 2);
      const int tj0 = (xcd & 1) * 512 + (wave < 2 ? lg * 64 + hg * 8 + wave * 4 : 448 + xi * 4), pj = par;
      u32x4 xs[5], wv[4];
      int jo0 = lane * 16, jo1 = g4 * 2560 + c15 * 16, jo2 = c15 * 16;
      PIN(jo0); PIN(jo1); PIN(jo2);
      args();
      if (wjob) {
        const u16* xr = x + (2 * tj0 + pj) * DM;
        asm volatile("global_load_dwordx4 %0, %9, %12\n\tglobal_load_dwordx4 %1, %9, %12 offset:2560\n\t"
                     "global_load_dwordx4 %2, %9, %13\n\tglobal_load_dwordx4 %3, %9, %13 offset:2560\n\t"
                     "global_load_dwordx4 %4, %10, %12 offset:1024\n\tglobal_load_dwordx4 %5, %9, %14\n\t"
                     "global_load_dwordx4 %6, %11, %14 offset:1024\n\tglobal_load_dwordx4 %7, %9, %15\n\tglobal_load_dwordx4 %8, %11, %15 offset:1024"
                     : "=&v"(xs[0]), "=&v"(xs[1]), "=&v"(xs[2]), "=&v"(xs[3]), "=&v"(xs[4]), "=&v"(wv[0]), "=&v"(wv[1]), "=&v"(wv[2]), "=&v"(wv[3])
                     : "v"(jo0), "v"(jo1), "v"(jo2), "s"(xr), "s"(xr + 4 * DM), "s"(w_r), "s"(gate_w)
                     : "memory");
      }
      f32x4 acc[5][4];
#pragma unroll
      for (int m = 0; m < 5; ++m)
#pragma unroll
        for (int f = 0; f < 4; ++f) acc[m][f] = (f32x4){0.f, 0.f, 0.f, 0.f};
      const int wks = b % 20;
      if (b < 200) {
        const int kh = wks >= 10, s = wks - kh * 10, pr = lane >> 3, g = wave, hi = pr >> 2;
        const int row = (s < 8 ? (2 * (kh * 8 + s) + (g >> 1)) * 20 + 4 * (g & 1) + 8 * hi : (kh * 16 + (s - 8) * 8 + hi * 4 + g) * 20 + 16) + (pr & 3);
        asm volatile("s_mov_b32 m0, %2\n\ts_nop 0\n\tglobal_load_lds_dwordx4 %0, %1 nt" ::"v"((row * DM + (b / 20) * 64 + (lane & 7) * 8) * 2), "s"(wo), "s"(lds0 + L_PIECE + wave * 1024) : "memory");
      }
      const __amdgpu_buffer_rsrc_t rk = __builtin_amdgcn_make_buffer_rsrc((void*)(ws + WS_K), 0, 36864, 0x00020000);
      const int ko = c15 != 0 ? 0x7FFFFF00 : wave < 2 ? (rg * 8 + hg) * 128 + (wave * 4 + g4) * 8 : (224 + (xcd & 3) * 16 + xi) * 128 + g4 * 8;
      auto rows4 = [&](float (&p)[5]) {
        const auto a = __builtin_amdgcn_permlane32_swap(__float_as_uint(p[0]), __float_as_uint(p[2]), false, false);
        const auto c = __builtin_amdgcn_permlane32_swap(__float_as_uint(p[1]), __float_as_uint(p[3]), false, false);
        const auto e = __builtin_amdgcn_permlane16_swap(__float_as_uint(__uint_as_float(a[0]) + __uint_as_float(a[1])),
                                                        __float_as_uint(__uint_as_float(c[0]) + __uint_as_float(c[1])), false, false);
        return row_sum(__uint_as_float(e[0]) + __uint_as_float(e[1]) + p[4]);
      };
      const int p4 = lane & 3;
      f32x4 limA, limB;
      unsigned mA[2];
      u32x4 ones;
      u32x2 sel[4], kz[5];
      auto prepM = [&]() {
        int ln = lane;
        PIN(ln);
        const int e0 = (ln & 15) - 4 * (ln >> 4);
#pragma unroll
        for (int i = 0; i < 4; ++i) {
          limA[i] = e0 - i <= -9 ? 0.f : -__builtin_inff();
          limB[i] = (e0 - i >= 0 && e0 - i <= 7) ? 0.f : -__builtin_inff();
        }
        mA[0] = (e0 <= -9 ? 0xFFFFu : 0u) | (e0 - 1 <= -9 ? 0xFFFF0000u : 0u);
        mA[1] = (e0 - 2 <= -9 ? 0xFFFFu : 0u) | (e0 - 3 <= -9 ? 0xFFFF0000u : 0u);
        PIN(limA); PIN(limB); PIN(mA[0]); PIN(mA[1]);
      };
      auto prepS = [&]() {
        int ln = lane;
        PIN(ln);
        ones = (u32x4){0x3F803F80u, 0x3F803F80u, 0x3F803F80u, 0x3F803F80u};
        PIN(ones);
#pragma unroll
        for (int t = 0; t < 4; ++t) {
          const bool on = (ln >> 4) == 2 && ((((ln & 15) >> 2) - t + 1) & 3) < 2;
          sel[t] = (u32x2){on && (ln & 3) < 2 ? 0x3F80u << (16 * (ln & 3)) : 0u, on && (ln & 3) >= 2 ? 0x3F80u << (16 * ((ln & 3) - 2)) : 0u};
          PIN(sel[t]);
        }
#pragma unroll
        for (int t = 0; t < 5; ++t) {
          kz[t] = (u32x2){0u, 0u};
          PIN(kz[t]);
        }
      };
      const int swz = (c15 >> 1) & 7;
      const int aoff0 = c15 * 128 + ((g4 ^ swz) * 16);
      const int aoff1 = c15 * 128 + (((4 + g4) ^ swz) * 16);
      int woff[4];
      {
        const int q4 = c15 >> 2, sw = q4 | ((g4 & 1) << 2);
#pragma unroll
        for (int t = 0; t < 4; ++t) {
          const int sc = t < 3 ? t : p4;
          const int by = wave * 40 + (t < 3 ? p4 : 4) * 8;
          woff[t] = 10240 + (8 * g4 + q4) * 512 + (((sc == 3 ? 15 : sc * 5 + (by >> 5)) ^ sw) * 32) + ((by & 31) ^ (sc == 2 ? 16 : sc == 3 ? 8 : 0));
          PIN(woff[t]);
        }
      }
      int wo3[2][4], ao3[2][2];
#pragma unroll
      for (int q = 0; q < 2; ++q) {
#pragma unroll
        for (int j = 0; j < 4; ++j) {
          wo3[q][j] = woff[j] + (q + 1) * SLOT_B;
          PIN(wo3[q][j]);
        }
        ao3[q][0] = aoff0 + (q + 1) * SLOT_B;
        ao3[q][1] = aoff1 + (q + 1) * SLOT_B;
        PIN(ao3[q][0]); PIN(ao3[q][1]);
      }
#define GM(m, f, wf, af) acc[m][f] = (f) == 2 ? mfma16(af[m], wf[f], acc[m][f]) : mfma16(wf[f], af[m], acc[m][f])
      u32x4 af0[5], wf0[4], af1[5], wf1[4];
      auto rd1 = [&](u32x4(&an)[5], u32x4(&wn)[4], int q, int h, int r) {
        const int ao = q ? ao3[q - 1][h] : h ? aoff1 : aoff0;
        if (r == 2) {
          an[0] = *(const u32x4*)(smem + ao);
        } else if (r >= 9) {
          an[r - 8] = *(const u32x4*)(smem + ao + (r - 8) * 2048);
        } else {
          const int k = r < 2 ? r : r - 1;
          const u32x2 t = tr_read(smem + (q ? wo3[q - 1][k >> 1] : woff[k >> 1]) + h * 16384 + (k & 1) * 2048);
          wn[k >> 1][(k & 1) * 2] = t[0];
          wn[k >> 1][(k & 1) * 2 + 1] = t[1];
        }
      };
      auto half = [&](const u32x4(&a)[5], const u32x4(&w)[4], u32x4(&an)[5], u32x4(&wn)[4], int q, int h) {
#pragma unroll
        for (int i = 0; i < 20; ++i) {
          const int m_ = i < 15 ? i / 3 : 19 - i, j_ = ((i / 3) & 1) ? 2 - i % 3 : i % 3, f_ = i < 15 ? j_ + (j_ == 2) : 2;
          GM(m_, f_, w, a);
          SB();
          if (i < 13) rd1(an, wn, q, h, i);
          SB();
        }
      };
      SB();
      get_tag();
      if (wjob) {
        if (b < 200) WAITV(3); else WAITV(2);
#pragma unroll
        for (int i = 0; i < 5; ++i) PIN(xs[i]);
        PIN(wv[0]); PIN(wv[1]);
        float p[5];
#pragma unroll
        for (int i = 0; i < 5; ++i) {
          p[i] = 0.f;
#pragma unroll
          for (int q = 0; q < 4; ++q) p[i] = dot2(xs[i][q], wv[i == 4][q], p[i]);
        }
        const float r_ = rows4(p);
        unsigned u = pk2(r_, r_) & 0xFFFFu;
        u ^= (u & 0x8000u) ? 0xFFFFu : 0x8000u;
        const unsigned key = (u << 16) | (unsigned)(2047 - (2 * (tj0 + g4) + pj));
        __builtin_amdgcn_raw_buffer_store_b64((u32x2){key, tag}, rk, ko, 0, 16);
        SB();
      }
      u32x2 gv = {0u, 0u};
      int ga = 0x7FFFFF00;
      if (wjob) {
        if (b < 200) WAITV(2); else WAITV(1);
        PIN(wv[2]); PIN(wv[3]);
        float p[5];
#pragma unroll
        for (int i = 0; i < 5; ++i) {
          p[i] = 0.f;
#pragma unroll
          for (int q = 0; q < 4; ++q) {
            const unsigned gw = wv[2 + (i == 4)][q];
            p[i] = dot2(pk2(bflo(xs[i][q]) * bflo(gw), bfhi(xs[i][q]) * bfhi(gw)), 0x3F803F80u, p[i]);
          }
        }
        const float g_ = rbf(rows4(p));
        gv = (u32x2){__float_as_uint(rbf(1.0f / (1.0f + __expf(-g_)))), tag};
        ga = c15 != 0 ? ko : ko + 64;
        SB();
      }
      PIN(gv); PIN(ga);
      u32x4 wfr;
      int wfa;
      {
        if (wjob) WAITV(1); else WAITV(0);
        const char* tp = smem + L_PIECE + wave * 1024 + (c15 >> 2) * 128 + g4 * 32 + (lane & 3) * 8;
        const u32x2 lo = tr_read(tp), hi = tr_read(tp + 512);
        const int T = (b / 20) * 4 + g4;
        wfr = (u32x4){lo[0], lo[1], hi[0], hi[1]};
        wfa = b < 200 ? (((T / 5) * 20 + wks) * 5 + (T + 5 - ((T / 5) & 1)) % 5) * 1024 + (wave * 16 + c15) * 16 : 0x7FFFFF00;
        PIN(wfr); PIN(wfa);
      }
      WAITL0();
      BARRIER();
#pragma unroll
      for (int r = 0; r < 13; ++r) rd1(af0, wf0, 0, 0, r);
      SB();
#pragma unroll
      for (int i = 0; i < 9; ++i) {
        half(af0, wf0, af1, wf1, i % 3, 1);
        if (i == 0) {
          __builtin_amdgcn_raw_buffer_store_b128(wfr, __builtin_amdgcn_make_buffer_rsrc((void*)(ws + WS_WOF), 0, 819200, 0x00020000), wfa, 0, 16);
          __builtin_amdgcn_raw_buffer_store_b64(gv, rk, ga, 0, 16);
        }
        if (i == 1) WAITV(0);
        if (i == 3) prepO();
        if (i == 4) prepM();
        if (i == 5) prepS();
        if (i == 6) prepB();
        SB();
        WAITL0();
        BARRIER();
        if (i < 8) half(af1, wf1, af0, wf0, (i + 1) % 3, 0);
      }
      u32x4 wf2[4], af2[5];
      u32x4 qf[5], kf[5];
      u32x10 vb, tb_;
      u32x4 pf[5], os[3];
      u32x2 pp[5], ob[6], obT, obU;
      f32x4 l[5], sA[5], oA[5], sm[5], tx[5], oT[3];
      f32x2 ar[5][2], nn0[2], nn1[2], nn2[2], nn3[2], nn4[2], nnT[2], nnU[2];
      float mx[6], inv[5], it, iu;
      constexpr u64 mo = 0xFFFF0000FFFF0000ull, mh = 0xFFFFFFFF00000000ull;
      auto selm = [](u64 m_, float a, float c) { return __builtin_amdgcn_inverse_ballot_w64(m_) ? a : c; };
      const float cs = 0.22360679774997896f * 1.4426950408889634f;
      const f32x2 c2 = {cs, cs};
      const f32x4 zero4 = {0.f, 0.f, 0.f, 0.f};
      auto cpy = [](float v) { PIN(v); return v; };
      auto cpyu = [](unsigned v) { PIN(v); return v; };
      auto mulo = [](float v, float c) { PIN(v); return v * c; };
#define XBAR() BARRIER()
      GM(0, 3, wf1, af1); SB();
      af0[0] = *(const u32x4*)(smem + aoff0 + 0); SB();
      { const u32x2 t_ = tr_read(smem + woff[3] + 0); wf0[3][0] = t_[0]; wf0[3][1] = t_[1]; } SB();
      GM(0, 1, wf1, af1); SB();
      { const u32x2 t_ = tr_read(smem + woff[3] + 2048); wf0[3][2] = t_[0]; wf0[3][3] = t_[1]; } SB();
      { const u32x2 t_ = tr_read(smem + woff[1] + 0); wf0[1][0] = t_[0]; wf0[1][1] = t_[1]; } SB();
      GM(0, 0, wf1, af1); SB();
      { const u32x2 t_ = tr_read(smem + woff[1] + 2048); wf0[1][2] = t_[0]; wf0[1][3] = t_[1]; } SB();
      { const u32x2 t_ = tr_read(smem + woff[0] + 0); wf0[0][0] = t_[0]; wf0[0][1] = t_[1]; } SB();
      GM(0, 2, wf1, af1); SB();
      { const u32x2 t_ = tr_read(smem + woff[0] + 2048); wf0[0][2] = t_[0]; wf0[0][3] = t_[1]; } SB();
      af2[0] = *(const u32x4*)(smem + aoff1 + 0); SB();
      GM(1, 3, wf1, af1); SB();
      { const u32x2 t_ = tr_read(smem + woff[2] + 0); wf0[2][0] = t_[0]; wf0[2][1] = t_[1]; } SB();
      { const u32x2 t_ = tr_read(smem + woff[3] + 16384); wf2[3][0] = t_[0]; wf2[3][1] = t_[1]; } SB();
      GM(1, 1, wf1, af1); SB();
      { const u32x2 t_ = tr_read(smem + woff[2] + 2048); wf0[2][2] = t_[0]; wf0[2][3] = t_[1]; } SB();
      { const u32x2 t_ = tr_read(smem + woff[3] + 18432); wf2[3][2] = t_[0]; wf2[3][3] = t_[1]; } SB();
      GM(1, 0, wf1, af1); SB();
      { const u32x2 t_ = tr_read(smem + woff[1] + 16384); wf2[1][0] = t_[0]; wf2[1][1] = t_[1]; } SB();
      { const u32x2 t_ = tr_read(smem + woff[1] + 18432); wf2[1][2] = t_[0]; wf2[1][3] = t_[1]; } SB();
      GM(1, 2, wf1, af1); SB();
      { const u32x2 t_ = tr_read(smem + woff[0] + 16384); wf2[0][0] = t_[0]; wf2[0][1] = t_[1]; } SB();
      { const u32x2 t_ = tr_read(smem + woff[0] + 18432); wf2[0][2] = t_[0]; wf2[0][3] = t_[1]; } SB();
      GM(2, 3, wf1, af1); SB();
      af0[1] = *(const u32x4*)(smem + aoff0 + 2048); SB();
      { const u32x2 t_ = tr_read(smem + woff[2] + 16384); wf2[2][0] = t_[0]; wf2[2][1] = t_[1]; } SB();
      GM(2, 1, wf1, af1); SB();
      af2[1] = *(const u32x4*)(smem + aoff1 + 2048); SB();
      { const u32x2 t_ = tr_read(smem + woff[2] + 18432); wf2[2][2] = t_[0]; wf2[2][3] = t_[1]; } SB();
      GM(2, 0, wf1, af1); SB();
      af0[2] = *(const u32x4*)(smem + aoff0 + 4096); SB();
      af2[2] = *(const u32x4*)(smem + aoff1 + 4096); SB();
      GM(2, 2, wf1, af1); SB();
      af0[3] = *(const u32x4*)(smem + aoff0 + 6144); SB();
      af2[3] = *(const u32x4*)(smem + aoff1 + 6144); SB();
      GM(0, 3, wf0, af0); SB();
      af0[4] = *(const u32x4*)(smem + aoff0 + 8192); SB();
      af2[4] = *(const u32x4*)(smem + aoff1 + 8192); SB();
      GM(0, 1, wf0, af0); SB();
      GM(0, 0, wf0, af0); SB();
      GM(0, 2, wf0, af0); SB();
      GM(0, 3, wf2, af2); SB();
      GM(0, 1, wf2, af2); SB();
      GM(0, 0, wf2, af2); SB();
      GM(0, 2, wf2, af2); SB();
      GM(1, 3, wf0, af0); SB();
      GM(1, 1, wf0, af0); SB();
      GM(1, 0, wf0, af0); SB();
      GM(1, 2, wf0, af0); SB();
      qf[0][2] = pk2(acc[0][3][0], acc[0][3][1]); SB();
      GM(1, 3, wf2, af2); SB();
      qf[0][3] = pk2(acc[0][3][2], acc[0][3][3]); SB();
      { const auto r_ = __builtin_amdgcn_permlane16_swap(qf[0][2], kz[0][0], false, false); qf[0][2] = r_[0]; kf[0][2] = r_[1]; } SB();
      GM(1, 1, wf2, af2); SB();
      kf[0][0] = pk2(acc[0][1][0], acc[0][1][1]); SB();
      { const auto r_ = __builtin_amdgcn_permlane16_swap(qf[0][3], kz[0][1], false, false); qf[0][3] = r_[0]; kf[0][3] = r_[1]; } SB();
      GM(1, 0, wf2, af2); SB();
      kf[0][1] = pk2(acc[0][1][2], acc[0][1][3]); SB();
      qf[0][0] = pk2(acc[0][0][0], acc[0][0][1]); SB();
      tx[0] = mfma16h((u32x2){qf[0][2], qf[0][3]}, sel[0], zero4); SB();
      qf[1][2] = pk2(acc[1][3][0], acc[1][3][1]); SB();
      qf[1][3] = pk2(acc[1][3][2], acc[1][3][3]); SB();
      GM(1, 2, wf2, af2); SB();
      { const auto r_ = __builtin_amdgcn_permlane16_swap(qf[1][2], kz[1][0], false, false); qf[1][2] = r_[0]; kf[1][2] = r_[1]; } SB();
      kf[1][0] = pk2(acc[1][1][0], acc[1][1][1]); SB();
      GM(2, 3, wf0, af0); SB();
      qf[1][0] = pk2(acc[1][0][0], acc[1][0][1]); SB();
      { const auto r_ = __builtin_amdgcn_permlane16_swap(qf[1][3], kz[1][1], false, false); qf[1][3] = r_[0]; kf[1][3] = r_[1]; } SB();
      GM(2, 1, wf0, af0); SB();
      kf[1][1] = pk2(acc[1][1][2], acc[1][1][3]); SB();
      qf[1][1] = pk2(acc[1][0][2], acc[1][0][3]); SB();
      tx[1] = mfma16h((u32x2){qf[1][2], qf[1][3]}, sel[1], zero4); SB();
      qf[0][1] = pk2(acc[0][0][2], acc[0][0][3]); SB();
      tb_[0] = pk2(tx[0][0], tx[0][1]); SB();
      l[1] = mfma16(kf[1], qf[1], limB); SB();
      tb_[1] = pk2(tx[0][2], tx[0][3]); SB();
      vb[0] = pk2(acc[0][2][0], acc[0][2][1]); SB();
      sA[1] = mfma16(kf[0], qf[1], limA); SB();
      vb[1] = pk2(acc[0][2][2], acc[0][2][3]); SB();
      vb[2] = pk2(acc[1][2][0], acc[1][2][1]); SB();
      l[0] = mfma16(kf[0], qf[0], limB); SB();
      vb[3] = pk2(acc[1][2][2], acc[1][2][3]); SB();
      tb_[2] = pk2(tx[1][0], tx[1][1]); SB();
      GM(2, 0, wf0, af0); SB();
      tb_[3] = pk2(tx[1][2], tx[1][3]); SB();
      GM(2, 2, wf0, af0); SB();
      l[1][0] = fmx(l[1][0], sA[1][0]); SB();
      l[1][1] = fmx(l[1][1], sA[1][1]); SB();
      GM(2, 3, wf2, af2); SB();
      l[1][2] = fmx(l[1][2], sA[1][2]); SB();
      mx[0] = fmx(fmx(l[0][0], l[0][1]), l[0][2]); SB();
      GM(2, 1, wf2, af2); SB();
      l[1][3] = fmx(l[1][3], sA[1][3]); SB();
      mx[1] = fmx(fmx(l[1][0], l[1][1]), l[1][2]); SB();
      GM(2, 0, wf2, af2); SB();
      mx[0] = fmx(mx[0], l[0][3]); SB();
      mx[1] = fmx(mx[1], l[1][3]); SB();
      GM(2, 2, wf2, af2); SB();
      qf[2][2] = pk2(acc[2][3][0], acc[2][3][1]); SB();
      qf[2][3] = pk2(acc[2][3][2], acc[2][3][3]); SB();
      GM(3, 3, wf1, af1); SB();
      { const auto r_ = __builtin_amdgcn_permlane16_swap(qf[2][2], kz[2][0], false, false); qf[2][2] = r_[0]; kf[2][2] = r_[1]; } SB();
      kf[2][0] = pk2(acc[2][1][0], acc[2][1][1]); SB();
      GM(3, 1, wf1, af1); SB();
      qf[2][0] = pk2(acc[2][0][0], acc[2][0][1]); SB();
      { const auto r_ = __builtin_amdgcn_permlane16_swap(qf[2][3], kz[2][1], false, false); qf[2][3] = r_[0]; kf[2][3] = r_[1]; } SB();
      GM(3, 0, wf1, af1); SB();
      kf[2][1] = pk2(acc[2][1][2], acc[2][1][3]); SB();
      qf[2][1] = pk2(acc[2][0][2], acc[2][0][3]); SB();
      tx[2] = mfma16h((u32x2){qf[2][2], qf[2][3]}, sel[2], zero4); SB();
      { const auto r_ = __builtin_amdgcn_permlane32_swap(__float_as_uint(mx[0]), __float_as_uint(mx[1]), false, false); mx[0] = __uint_as_float(r_[0]); mx[1] = __uint_as_float(r_[1]); } SB();
      mx[0] = fmx(mx[0], mx[1]); SB();
      l[2] = mfma16(kf[2], qf[2], limB); SB();
      mx[1] = cpy(mx[0]); SB();
      vb[4] = pk2(acc[2][2][0], acc[2][2][1]); SB();
      sA[2] = mfma16(kf[1], qf[2], limA); SB();
      { const auto r_ = __builtin_amdgcn_permlane16_swap(__float_as_uint(mx[0]), __float_as_uint(mx[1]), false, false); mx[0] = __uint_as_float(r_[0]); mx[1] = __uint_as_float(r_[1]); } SB();
      mx[0] = fmx(mx[0], mx[1]); SB();
      GM(3, 2, wf1, af1); SB();
      mx[1] = mx[0] * -cs; SB();
      mx[0] = mulo(mx[0], -cs); SB();
      GM(3, 3, wf0, af0); SB();
      vb[5] = pk2(acc[2][2][2], acc[2][2][3]); SB();
      { const auto r_ = __builtin_amdgcn_permlane32_swap(__float_as_uint(mx[0]), __float_as_uint(mx[1]), false, false); mx[0] = __uint_as_float(r_[0]); mx[1] = __uint_as_float(r_[1]); } SB();
      GM(3, 1, wf0, af0); SB();
      l[2][0] = fmx(l[2][0], sA[2][0]); SB();
      l[2][1] = fmx(l[2][1], sA[2][1]); SB();
      GM(3, 0, wf0, af0); SB();
      l[2][2] = fmx(l[2][2], sA[2][2]); SB();
      l[2][3] = fmx(l[2][3], sA[2][3]); SB();
      GM(3, 2, wf0, af0); SB();
      mx[2] = fmx(fmx(l[2][0], l[2][1]), l[2][2]); SB();
      mx[2] = fmx(mx[2], l[2][3]); SB();
      GM(3, 3, wf2, af2); SB();
      ar[1][0] = __builtin_elementwise_fma((f32x2){l[1][0], l[1][1]}, c2, (f32x2){mx[1], mx[1]}); SB();
      ar[1][1] = __builtin_elementwise_fma((f32x2){l[1][2], l[1][3]}, c2, (f32x2){mx[1], mx[1]}); SB();
      GM(3, 1, wf2, af2); SB();
      ar[0][0] = __builtin_elementwise_fma((f32x2){l[0][0], l[0][1]}, c2, (f32x2){mx[0], mx[0]}); SB();
      ar[0][1] = __builtin_elementwise_fma((f32x2){l[0][2], l[0][3]}, c2, (f32x2){mx[0], mx[0]}); SB();
      GM(3, 0, wf2, af2); SB();
      ar[1][0][0] = __builtin_amdgcn_exp2f(ar[1][0][0]); SB();
      GM(3, 2, wf2, af2); SB();
      qf[3][2] = pk2(acc[3][3][0], acc[3][3][1]); SB();
      qf[3][3] = pk2(acc[3][3][2], acc[3][3][3]); SB();
      GM(4, 3, wf1, af1); SB();
      { const auto r_ = __builtin_amdgcn_permlane16_swap(qf[3][2], kz[3][0], false, false); qf[3][2] = r_[0]; kf[3][2] = r_[1]; } SB();
      kf[3][0] = pk2(acc[3][1][0], acc[3][1][1]); SB();
      GM(4, 1, wf1, af1); SB();
      qf[3][0] = pk2(acc[3][0][0], acc[3][0][1]); SB();
      { const auto r_ = __builtin_amdgcn_permlane16_swap(qf[3][3], kz[3][1], false, false); qf[3][3] = r_[0]; kf[3][3] = r_[1]; } SB();
      GM(4, 0, wf1, af1); SB();
      kf[3][1] = pk2(acc[3][1][2], acc[3][1][3]); SB();
      qf[3][1] = pk2(acc[3][0][2], acc[3][0][3]); SB();
      tx[3] = mfma16h((u32x2){qf[3][2], qf[3][3]}, sel[3], zero4); SB();
      ar[1][0][1] = __builtin_amdgcn_exp2f(ar[1][0][1]); SB();
      l[3] = mfma16(kf[3], qf[3], limB); SB();
      ar[1][1][0] = __builtin_amdgcn_exp2f(ar[1][1][0]); SB();
      sA[3] = mfma16(kf[2], qf[3], limA); SB();
      ar[1][1][1] = __builtin_amdgcn_exp2f(ar[1][1][1]); SB();
      GM(4, 2, wf1, af1); SB();
      ar[0][0][0] = __builtin_amdgcn_exp2f(ar[0][0][0]); SB();
      GM(4, 3, wf0, af0); SB();
      ar[0][0][1] = __builtin_amdgcn_exp2f(ar[0][0][1]); SB();
      GM(4, 1, wf0, af0); SB();
      ar[0][1][0] = __builtin_amdgcn_exp2f(ar[0][1][0]); SB();
      GM(4, 0, wf0, af0); SB();
      l[3][0] = fmx(l[3][0], sA[3][0]); SB();
      l[3][1] = fmx(l[3][1], sA[3][1]); SB();
      GM(4, 2, wf0, af0); SB();
      l[3][2] = fmx(l[3][2], sA[3][2]); SB();
      l[3][3] = fmx(l[3][3], sA[3][3]); SB();
      GM(4, 3, wf2, af2); SB();
      mx[3] = fmx(fmx(l[3][0], l[3][1]), l[3][2]); SB();
      mx[3] = fmx(mx[3], l[3][3]); SB();
      GM(4, 1, wf2, af2); SB();
      ar[0][1][1] = __builtin_amdgcn_exp2f(ar[0][1][1]); SB();
      GM(4, 0, wf2, af2); SB();
      { const auto r_ = __builtin_amdgcn_permlane32_swap(__float_as_uint(mx[2]), __float_as_uint(mx[3]), false, false); mx[2] = __uint_as_float(r_[0]); mx[3] = __uint_as_float(r_[1]); } SB();
      mx[2] = fmx(mx[2], mx[3]); SB();
      GM(4, 2, wf2, af2); SB();
      qf[4][2] = pk2(acc[4][3][0], acc[4][3][1]); SB();
      qf[4][3] = pk2(acc[4][3][2], acc[4][3][3]); SB();
      mx[3] = cpy(mx[2]); SB();
      { const auto r_ = __builtin_amdgcn_permlane16_swap(qf[4][2], kz[4][0], false, false); qf[4][2] = r_[0]; kf[4][2] = r_[1]; } SB();
      kf[4][0] = pk2(acc[4][1][0], acc[4][1][1]); SB();
      qf[4][0] = pk2(acc[4][0][0], acc[4][0][1]); SB();
      { const auto r_ = __builtin_amdgcn_permlane16_swap(qf[4][3], kz[4][1], false, false); qf[4][3] = r_[0]; kf[4][3] = r_[1]; } SB();
      kf[4][1] = pk2(acc[4][1][2], acc[4][1][3]); SB();
      qf[4][1] = pk2(acc[4][0][2], acc[4][0][3]); SB();
      tx[4] = mfma16h((u32x2){qf[4][2], qf[4][3]}, sel[0], zero4); SB();
      { const auto r_ = __builtin_amdgcn_permlane16_swap(__float_as_uint(mx[2]), __float_as_uint(mx[3]), false, false); mx[2] = __uint_as_float(r_[0]); mx[3] = __uint_as_float(r_[1]); } SB();
      mx[2] = fmx(mx[2], mx[3]); SB();
      l[4] = mfma16(kf[4], qf[4], limB); SB();
      mx[3] = mx[2] * -cs; SB();
      mx[2] = mulo(mx[2], -cs); SB();
      sA[4] = mfma16(kf[3], qf[4], limA); SB();
      pp[1][0] = pk2(ar[1][0][0], ar[1][0][1]); SB();
      { const auto r_ = __builtin_amdgcn_permlane32_swap(__float_as_uint(mx[2]), __float_as_uint(mx[3]), false, false); mx[2] = __uint_as_float(r_[0]); mx[3] = __uint_as_float(r_[1]); } SB();
      ar[2][0] = __builtin_elementwise_fma((f32x2){l[2][0], l[2][1]}, c2, (f32x2){mx[2], mx[2]}); SB();
      ar[2][1] = __builtin_elementwise_fma((f32x2){l[2][2], l[2][3]}, c2, (f32x2){mx[2], mx[2]}); SB();
      ar[2][0][0] = __builtin_amdgcn_exp2f(ar[2][0][0]); SB();
      ar[2][0][1] = __builtin_amdgcn_exp2f(ar[2][0][1]); SB();
      ar[2][1][0] = __builtin_amdgcn_exp2f(ar[2][1][0]); SB();
      ar[2][1][1] = __builtin_amdgcn_exp2f(ar[2][1][1]); SB();
      l[4][0] = fmx(l[4][0], sA[4][0]); SB();
      l[4][1] = fmx(l[4][1], sA[4][1]); SB();
      l[4][2] = fmx(l[4][2], sA[4][2]); SB();
      l[4][3] = fmx(l[4][3], sA[4][3]); SB();
      mx[4] = fmx(fmx(l[4][0], l[4][1]), l[4][2]); SB();
      mx[4] = fmx(mx[4], l[4][3]); SB();
      mx[5] = cpy(mx[4]); SB();
      pp[1][1] = pk2(ar[1][1][0], ar[1][1][1]); SB();
      pp[2][0] = pk2(ar[2][0][0], ar[2][0][1]); SB();
      { const auto r_ = __builtin_amdgcn_permlane32_swap(__float_as_uint(mx[4]), __float_as_uint(mx[5]), false, false); mx[4] = __uint_as_float(r_[0]); mx[5] = __uint_as_float(r_[1]); } SB();
      mx[4] = fmx(mx[4], mx[5]); SB();
      mx[5] = cpy(mx[4]); SB();
      pp[2][1] = pk2(ar[2][1][0], ar[2][1][1]); SB();
      pp[0][0] = pk2(ar[0][0][0], ar[0][0][1]); SB();
      { const auto r_ = __builtin_amdgcn_permlane16_swap(__float_as_uint(mx[4]), __float_as_uint(mx[5]), false, false); mx[4] = __uint_as_float(r_[0]); mx[5] = __uint_as_float(r_[1]); } SB();
      pp[0][1] = pk2(ar[0][1][0], ar[0][1][1]); SB();
      mx[4] = fmx(mx[4], mx[5]); SB();
      pf[1][0] = pp[1][0] & mA[0]; SB();
      sm[0] = mfma16h((u32x2){ones[0], ones[1]}, pp[0], zero4); SB();
      pf[1][2] = pp[1][0] & ~mA[0]; SB();
      pf[1][1] = pp[1][1] & mA[1]; SB();
      oA[0] = mfma16h((u32x2){vb[0], vb[1]}, pp[0], zero4); SB();
      pf[1][3] = pp[1][1] & ~mA[1]; SB();
      pf[2][0] = pp[2][0] & mA[0]; SB();
      oT[2] = mfma16h((u32x2){tb_[0], tb_[1]}, pp[0], zero4); SB();
      pf[2][2] = pp[2][0] & ~mA[0]; SB();
      pf[2][1] = pp[2][1] & mA[1]; SB();
      sm[1] = mfma16(ones, pf[1], zero4); SB();
      pf[2][3] = pp[2][1] & ~mA[1]; SB();
      mx[4] = mx[4] * -cs; SB();
      PIN(tb_); oT[0] = mfma16(__builtin_shufflevector(tb_, tb_, 0, 1, 2, 3), pf[1], zero4); SB();
      ar[3][0] = __builtin_elementwise_fma((f32x2){l[3][0], l[3][1]}, c2, (f32x2){mx[3], mx[3]}); SB();
      ar[3][1] = __builtin_elementwise_fma((f32x2){l[3][2], l[3][3]}, c2, (f32x2){mx[3], mx[3]}); SB();
      sm[2] = mfma16(ones, pf[2], zero4); SB();
      ar[4][0] = __builtin_elementwise_fma((f32x2){l[4][0], l[4][1]}, c2, (f32x2){mx[4], mx[4]}); SB();
      ar[4][1] = __builtin_elementwise_fma((f32x2){l[4][2], l[4][3]}, c2, (f32x2){mx[4], mx[4]}); SB();
      PIN(vb); oA[1] = mfma16(__builtin_shufflevector(vb, vb, 0, 1, 2, 3), pf[1], zero4); SB();
      ar[3][0][0] = __builtin_amdgcn_exp2f(ar[3][0][0]); SB();
      PIN(vb); oA[2] = mfma16(__builtin_shufflevector(vb, vb, 2, 3, 4, 5), pf[2], zero4); SB();
      ar[3][0][1] = __builtin_amdgcn_exp2f(ar[3][0][1]); SB();
      ar[3][1][0] = __builtin_amdgcn_exp2f(ar[3][1][0]); SB();
      ar[3][1][1] = __builtin_amdgcn_exp2f(ar[3][1][1]); SB();
      ar[4][0][0] = __builtin_amdgcn_exp2f(ar[4][0][0]); SB();
      ar[4][0][1] = __builtin_amdgcn_exp2f(ar[4][0][1]); SB();
      ar[4][1][0] = __builtin_amdgcn_exp2f(ar[4][1][0]); SB();
      ar[4][1][1] = __builtin_amdgcn_exp2f(ar[4][1][1]); SB();
      pp[3][0] = pk2(ar[3][0][0], ar[3][0][1]); SB();
      pp[3][1] = pk2(ar[3][1][0], ar[3][1][1]); SB();
      pp[4][0] = pk2(ar[4][0][0], ar[4][0][1]); SB();
      pp[4][1] = pk2(ar[4][1][0], ar[4][1][1]); SB();
      inv[0] = __builtin_amdgcn_rcpf(sm[0][0]); PIN(sm[0]); SB();
      inv[1] = __builtin_amdgcn_rcpf(sm[1][0]); PIN(sm[1]); SB();
      inv[2] = __builtin_amdgcn_rcpf(sm[2][0]); PIN(sm[2]); SB();
      pf[3][0] = pp[3][0] & mA[0]; SB();
      pf[3][2] = pp[3][0] & ~mA[0]; SB();
      pf[3][1] = pp[3][1] & mA[1]; SB();
      pf[3][3] = pp[3][1] & ~mA[1]; SB();
      pf[4][0] = pp[4][0] & mA[0]; SB();
      pf[4][2] = pp[4][0] & ~mA[0]; SB();
      sm[3] = mfma16(ones, pf[3], zero4); SB();
      pf[4][1] = pp[4][1] & mA[1]; SB();
      pf[4][3] = pp[4][1] & ~mA[1]; SB();
      tb_[4] = pk2(tx[2][0], tx[2][1]); SB();
      tb_[5] = pk2(tx[2][2], tx[2][3]); SB();
      sm[4] = mfma16(ones, pf[4], zero4); SB();
      nn0[0] = (f32x2){oA[0][0], oA[0][1]} * (f32x2){inv[0], inv[0]}; SB();
      nn0[1] = (f32x2){oA[0][2], oA[0][3]} * (f32x2){inv[0], inv[0]}; SB();
      PIN(tb_); oT[1] = mfma16(__builtin_shufflevector(tb_, tb_, 2, 3, 4, 5), pf[2], zero4); SB();
      ob[0][0] = pk2(nn0[0][0], nn0[0][1]); SB();
      ob[0][1] = pk2(nn0[1][0], nn0[1][1]); SB();
      nn1[0] = (f32x2){oA[1][0], oA[1][1]} * (f32x2){inv[1], inv[1]}; SB();
      nn1[1] = (f32x2){oA[1][2], oA[1][3]} * (f32x2){inv[1], inv[1]}; SB();
      nn2[0] = (f32x2){oA[2][0], oA[2][1]} * (f32x2){inv[2], inv[2]}; SB();
      nn2[1] = (f32x2){oA[2][2], oA[2][3]} * (f32x2){inv[2], inv[2]}; SB();
      vb[6] = pk2(acc[3][2][0], acc[3][2][1]); SB();
      vb[7] = pk2(acc[3][2][2], acc[3][2][3]); SB();
      vb[8] = pk2(acc[4][2][0], acc[4][2][1]); SB();
      vb[9] = pk2(acc[4][2][2], acc[4][2][3]); SB();
      PIN(vb); oA[3] = mfma16(__builtin_shufflevector(vb, vb, 4, 5, 6, 7), pf[3], zero4); SB();
      tb_[6] = pk2(tx[3][0], tx[3][1]); SB();
      tb_[7] = pk2(tx[3][2], tx[3][3]); SB();
      PIN(vb); oA[4] = mfma16(__builtin_shufflevector(vb, vb, 6, 7, 8, 9), pf[4], zero4); SB();
      tb_[8] = pk2(tx[4][0], tx[4][1]); SB();
      tb_[9] = pk2(tx[4][2], tx[4][3]); SB();
      PIN(tb_); oT[0] = mfma16(__builtin_shufflevector(tb_, tb_, 4, 5, 6, 7), pf[3], oT[0]); SB();
      ob[1][0] = pk2(nn1[0][0], nn1[0][1]); SB();
      ob[1][1] = pk2(nn1[1][0], nn1[1][1]); SB();
      PIN(tb_); oT[1] = mfma16(__builtin_shufflevector(tb_, tb_, 6, 7, 8, 9), pf[4], oT[1]); SB();
      ob[2][0] = pk2(nn2[0][0], nn2[0][1]); SB();
      ob[2][1] = pk2(nn2[1][0], nn2[1][1]); SB();
      ob[5][0] = cpyu(ob[0][0]); SB();
      ob[5][1] = cpyu(ob[0][1]); SB();
      nnU[0] = (f32x2){oT[2][0], oT[2][1]} * (f32x2){inv[0], inv[0]}; SB();
      nnU[1] = (f32x2){oT[2][2], oT[2][3]} * (f32x2){inv[0], inv[0]}; SB();
      obU[0] = pk2(nnU[0][0], nnU[0][1]); SB();
      obU[1] = pk2(nnU[1][0], nnU[1][1]); SB();
      { const auto r_ = __builtin_amdgcn_permlane32_swap(ob[0][0], ob[5][0], false, false); os[0][0] = r_[0]; os[0][2] = r_[1]; } SB();
      { const auto r_ = __builtin_amdgcn_permlane32_swap(ob[0][1], ob[5][1], false, false); os[0][1] = r_[0]; os[0][3] = r_[1]; } SB();
      { const auto r_ = __builtin_amdgcn_permlane32_swap(ob[1][0], ob[2][0], false, false); os[1][0] = r_[0]; os[1][2] = r_[1]; } SB();
      { const auto r_ = __builtin_amdgcn_permlane32_swap(ob[1][1], ob[2][1], false, false); os[1][1] = r_[0]; os[1][3] = r_[1]; } SB();
      inv[3] = __builtin_amdgcn_rcpf(sm[3][0]); PIN(sm[3]); SB();
      inv[4] = __builtin_amdgcn_rcpf(sm[4][0]); PIN(sm[4]); SB();
      it = selm(mo, inv[2], inv[1]); SB();
      iu = selm(mo, inv[4], inv[3]); SB();
      nn3[0] = (f32x2){oA[3][0], oA[3][1]} * (f32x2){inv[3], inv[3]}; SB();
      nn3[1] = (f32x2){oA[3][2], oA[3][3]} * (f32x2){inv[3], inv[3]}; SB();
      nn4[0] = (f32x2){oA[4][0], oA[4][1]} * (f32x2){inv[4], inv[4]}; SB();
      nn4[1] = (f32x2){oA[4][2], oA[4][3]} * (f32x2){inv[4], inv[4]}; SB();
      it = selm(mh, iu, it); SB();
      oT[0][0] = selm(mo, oT[1][0], oT[0][0]); SB();
      oT[0][1] = selm(mo, oT[1][1], oT[0][1]); SB();
      oT[0][2] = selm(mo, oT[1][2], oT[0][2]); SB();
      oT[0][3] = selm(mo, oT[1][3], oT[0][3]); SB();
      ob[3][0] = pk2(nn3[0][0], nn3[0][1]); SB();
      ob[3][1] = pk2(nn3[1][0], nn3[1][1]); SB();
      ob[4][0] = pk2(nn4[0][0], nn4[0][1]); SB();
      ob[4][1] = pk2(nn4[1][0], nn4[1][1]); SB();
      nnT[0] = (f32x2){oT[0][0], oT[0][1]} * (f32x2){it, it}; SB();
      nnT[1] = (f32x2){oT[0][2], oT[0][3]} * (f32x2){it, it}; SB();
      obT[0] = pk2(nnT[0][0], nnT[0][1]); SB();
      obT[1] = pk2(nnT[1][0], nnT[1][1]); SB();
      { const auto r_ = __builtin_amdgcn_permlane32_swap(ob[3][0], ob[4][0], false, false); os[2][0] = r_[0]; os[2][2] = r_[1]; } SB();
      { const auto r_ = __builtin_amdgcn_permlane32_swap(ob[3][1], ob[4][1], false, false); os[2][1] = r_[0]; os[2][3] = r_[1]; } SB();
      XBAR(); SB();
      __builtin_amdgcn_raw_buffer_store_b128(os[1], ro, od[1], 0, 16); SB();
      __builtin_amdgcn_raw_buffer_store_b128(os[2], ro, od[2], 0, 16); SB();
      __builtin_amdgcn_raw_buffer_store_b64(obT, ro, od[3], 0, 16); SB();
      __builtin_amdgcn_raw_buffer_store_b128(os[0], ro, od[0], 0, 16); SB();
      __builtin_amdgcn_raw_buffer_store_b64(obU, ro, od[4], 0, 16); SB();
    }
  } else {
    const int f = b - 224;
    args();
    float* red = (float*)smem;
    u16* hs = (u16*)(smem + 32768);
    const bool do1 = f < 27;
    const bool lead = f == 31;
    if (__builtin_expect(lead, 0)) {
      get_tag();
      const __amdgpu_buffer_rsrc_t rk = __builtin_amdgcn_make_buffer_rsrc((void*)(ws + WS_K), 0, 36864, 0x00020000);
      u32x2 kv[2];
      int ko[2];
      ko[0] = (tid >> 2) * 128 + (tid & 3) * 16;
      ko[1] = tid < 384 ? (128 + (tid >> 2)) * 128 + (tid & 3) * 16 : (224 + ((tid - 384) >> 1)) * 128 + (tid & 1) * 16;
      kv[0] = kv[1] = (u32x2){0u, 0u};
#pragma unroll 1
      for (unsigned spins = 0; spins < SPIN_CAP; ++spins) {
        asm volatile("" ::: "memory");
        u32x4 t_[2];
#pragma unroll
        for (int q = 0; q < 2; ++q) t_[q] = __builtin_amdgcn_raw_buffer_load_b128(rk, ko[q], 0, 16);
        bool ok = true;
#pragma unroll
        for (int q = 0; q < 2; ++q)
          if (ko[q] != 0x7FFFFF00) {
            if ((t_[q][1] == tag) & (t_[q][3] == tag)) {
              kv[q] = (u32x2){t_[q][0], t_[q][2]};
              ko[q] = 0x7FFFFF00;
            } else {
              ok = false;
            }
          }
        if (__all(ok)) break;
        __builtin_amdgcn_s_sleep(4);
      }
      unsigned k1 = max(kv[0][0], kv[0][1]), k2 = min(kv[0][0], kv[0][1]);
      top2_mrg(k1, k2, max(kv[1][0], kv[1][1]), min(kv[1][0], kv[1][1]));
      top2_all(k1, k2);
      unsigned* lk = (unsigned*)(smem + L_VAR + 64);
      if (lane == 0) *(u32x2*)(lk + wave * 2) = (u32x2){k1, k2};
      WAITL0();
      BARRIER();
      k1 = 0u;
      k2 = 0u;
#pragma unroll
      for (int q = 0; q < 4; ++q) {
        const u32x4 a_ = *(const u32x4*)(lk + q * 4);
        top2_mrg(k1, k2, a_[0], a_[1]);
        top2_mrg(k1, k2, a_[2], a_[3]);
      }
      if (tid == 0) {
        const __amdgpu_buffer_rsrc_t rr = __builtin_amdgcn_make_buffer_rsrc((void*)(ws + WS_R), 0, 128, 0x00020000);
        __builtin_amdgcn_raw_buffer_store_b128((u32x4){k1, tag, k2, tag}, rr, 0, 0, 16);
        *(u32x2*)(smem + L_VAR + 32) = (u32x2){k1, k2};
      }
    }
    if (__builtin_expect(f == 30, 0)) {
      get_tag();
      if (wave < 4) {
        const __amdgpu_buffer_rsrc_t rf = __builtin_amdgcn_make_buffer_rsrc((void*)(ws + WS_F2), 0, 32768, 0x00020000);
        int fo = tid < 227 ? tid * 128 : 0x7FFFFF00;
#pragma unroll 1
        for (unsigned spins = 0; spins < SPIN_CAP; ++spins) {
          asm volatile("" ::: "memory");
          if (__builtin_amdgcn_raw_buffer_load_b32(rf, fo, 0, 16) == tag) fo = 0x7FFFFF00;
          if (__all(fo == 0x7FFFFF00)) break;
          __builtin_amdgcn_s_sleep(4);
        }
      }
      BARRIER();
      if (tid == 0) {
        const __amdgpu_buffer_rsrc_t rr = __builtin_amdgcn_make_buffer_rsrc((void*)(ws + WS_R), 0, 256, 0x00020000);
        const unsigned t_ = now();
        __builtin_amdgcn_raw_buffer_store_b128((u32x4){t_, tag, t_, tag}, rr, 128, 0, 16);
        *(u32x4*)(smem + L_CAND2) = (u32x4){t_, tag, t_, tag};
      }
    }
    if (__builtin_expect(f >= 27 && f < 30, 0)) {
      get_tag();
#pragma unroll 1
      for (int c = wave * 3 + f - 27; c < 320; c += 24)
        __builtin_amdgcn_raw_buffer_store_b128((u32x4){SENT, SENT, SENT, SENT}, ro, lane * 16, (c >= 160 ? 112 * 20480 : 56 * 20480) + c * 1024, 16);
      WAITV(0);
      BARRIER();
      if (tid == 0) __hip_atomic_store((gu32*)(ws + WS_F2 + (197 + f) * 128), tag, RLX_AGENT);
    }
    const int g0 = wave < 4 ? wave * 3 : 4 + wave * 2, ng = wave < 4 ? 3 : 2;
    u32x4 r1[3][8], wd[2][8];
    int vo2 = (wave - 3) * 256 + c15 * 16 + g4 * 8 * (DM * 2);
    PIN(vo2);
    if (do1) {
      const char* pa = (const char*)(c15 < 8 ? w_gate : w_up) + f * 128 + (c15 & 7) * 16 + (g0 * 32 + g4 * 8) * (FF * 2);
      const GAS char* pp[12];
#pragma unroll
      for (int j = 0; j < 12; ++j) {
        pp[j] = (const GAS char*)pa + ((j >> 2) * 32 + (j & 3) * 2) * (FF * 2) + FF;
        PIN(pp[j]);
      }
      SB();
#pragma unroll
      for (int j = 0; j < 24; ++j)
        if (j < 16 || wave < 4) r1[j >> 3][j & 7] = __builtin_nontemporal_load((const GAS u32x4*)(pp[j >> 1] + ((j & 1) ? FF : -FF)));
    }
    SB();
    BARRIER();
    if (do1 && wave >= 3) {
      const u32x4 r = make_srd(w_down, DM * FF * 2);
#pragma unroll
      for (int j = 0; j < 16; ++j)
        asm volatile("buffer_load_dwordx4 %0, %1, %2, %3 offen nt" : "=v"(wd[j >> 3][j & 7]) : "v"(vo2), "s"(r), "s"((f * 64 + (j >> 3) * 32 + (j & 7)) * (DM * 2)) : "memory");
    }
    SB();
    if (f < 30) get_tag();
    prepB();
    auto transp = [&](u32x4(&w)[8]) {
      u32x4 t[8];
#pragma unroll
      for (int d = 0; d < 4; ++d)
#pragma unroll
        for (int p = 0; p < 4; ++p) {
          t[2 * d][p] = perm_lo(w[2 * p + 1][d], w[2 * p][d]);
          t[2 * d + 1][p] = perm_hi(w[2 * p + 1][d], w[2 * p][d]);
        }
#pragma unroll
      for (int j = 0; j < 8; ++j) {
        w[j] = t[j];
        PIN(w[j]);
      }
    };
    if (do1) {
      transp(r1[0]);
      transp(r1[1]);
      if (wave < 4) transp(r1[2]);
    }
    __builtin_amdgcn_s_waitcnt(0x0F70);
    if (wave == 0 && !lead) {
      res_wait(ws, smem, lane, tag, 0);
      if (do1) {
        WAITL0();
        const u32x2 kq = *(const u32x2*)(smem + L_VAR + 32);
#pragma unroll
        for (int t = 0; t < 2; ++t) {
          const u16* xr = x + (2047 - (int)(__builtin_amdgcn_readfirstlane(kq[t]) & 2047u)) * DM;
          dma16(lane * 16, xr, lds0 + 36864 + t * 1296);
          if (lane < 16) dma16(lane * 16, xr + 512, lds0 + 36864 + t * 1296 + 1024);
        }
      }
    }
    if (wave == 1) touch_line((const char*)out + tile_off((lane & 31) + (nt2 & 1) * 32) + ((nt2 & 1) ? 0 : 128), lds0 + L_TOUCH + 512);
    if (wave == 1 && f != 30 && !do1) res_wait(ws, smem, lane, tag, 1);
    if (wave < 2) WAITV(0);
    WAITL0();
    BARRIER();
    const unsigned r0_ = now(), c0_ = (unsigned)__builtin_amdgcn_s_memtime();
    if (tid == 128) __hip_atomic_store((gu32*)(ws + WS_A + f * 128), tag, RLX_AGENT);
    const __amdgpu_buffer_rsrc_t ryp = __builtin_amdgcn_make_buffer_rsrc((void*)(ws + WS_YP), 0, 27 * 10240, 0x00020000);
    auto pick = [&](const f32x4(&a)[8]) {
      f32x4 v;
#pragma unroll
      for (int q = 0; q < 4; ++q) {
        const float lo = (g4 & 1) ? a[2 + (q >> 1)][q & 1] : a[q >> 1][q & 1], hi = (g4 & 1) ? a[6 + (q >> 1)][q & 1] : a[4 + (q >> 1)][q & 1];
        v[q] = (g4 & 2) ? hi : lo;
      }
      return v;
    };
    if (do1) {
      u32x4 xf[3];
#pragma unroll
      for (int k = 0; k < 3; ++k) xf[k] = *(const u32x4*)(smem + 36864 + (c15 & 1) * 1296 + ((g0 + (k < ng ? k : 0)) * 32 + g4 * 8) * 2);
      f32x4 acc[8];
#pragma unroll
      for (int ff = 0; ff < 8; ++ff) acc[ff] = (f32x4){0.f, 0.f, 0.f, 0.f};
#pragma unroll
      for (int k = 0; k < 3; ++k)
        if (k < ng) {
#pragma unroll
          for (int j = 0; j < 8; ++j) acc[j] = mfma16(xf[k], r1[k][j], acc[j]);
        }
      *(f32x4*)(red + wave * 256 + c15 * 16 + g4 * 4) = pick(acc);
      WAITL0();
      BARRIER();
      u32x4 wt[2][8];
      if (wave < 2) {
        float gs = 0.f, us = 0.f;
#pragma unroll
        for (int w = 0; w < 8; ++w) {
          gs += red[w * 256 + tid];
          us += red[w * 256 + 128 + tid];
        }
        const float gv = rbf(gs), uv = rbf(us);
        const float hv = rbf(gv * __builtin_amdgcn_rcpf(1.0f + __expf(-gv))) * uv;
        hs[(tid & 1) * 64 + (tid >> 1)] = (u16)pk2(hv, hv);
      } else if (wave >= 3) {
        WAITV(0);
#pragma unroll
        for (int j = 0; j < 16; ++j) {
          wt[j >> 3][j & 7] = wd[j >> 3][j & 7];
          PIN(wt[j >> 3][j & 7]);
        }
        transp(wt[0]);
        transp(wt[1]);
      }
      WAITL0();
      BARRIER();
      if (wave == 2) res_wait(ws, smem, lane, tag, 1);
      if (wave >= 3) {
        const u32x4 h0 = *(const u32x4*)(hs + (c15 & 1) * 64 + g4 * 8), h1 = *(const u32x4*)(hs + (c15 & 1) * 64 + 32 + g4 * 8);
#pragma unroll
        for (int j = 0; j < 8; ++j) acc[j] = mfma16(h0, wt[0][j], (f32x4){0.f, 0.f, 0.f, 0.f});
#pragma unroll
        for (int j = 0; j < 8; ++j) acc[j] = mfma16(h1, wt[1][j], acc[j]);
        *(f32x4*)(red + 2048 + wave * 256 + c15 * 16 + g4 * 4) = pick(acc);
        const f32x4 y = *(const f32x4*)(red + 2048 + wave * 256 + lane * 4);
#pragma unroll
        for (int t = 0; t < 2; ++t)
          __builtin_amdgcn_raw_buffer_store_b128((u32x4){__float_as_uint(y[t]), tag, __float_as_uint(y[2 + t]), tag}, ryp,
                                                 ((f * 2 + t) * 640 + (wave - 3) * 128 + lane * 2) * 8, 0, 16);
      }
      WAITL0();
      BARRIER();
    }
    wo_dma(IC<3>{}, wave, 8, 25);
    if (wave == 0) wo_dma(IC<1>{}, 24, 8, 25);
    if (wave == 1) gate_dma(gate_off(lane));
    if (tid < 8) *(unsigned*)(smem + L_VAR + tid * 4) = 0u;
    if (wave == 2 || (f == 30 && wave == 3)) {
      const unsigned tp = __builtin_amdgcn_readfirstlane(*(const unsigned*)(smem + L_CAND2)), d = wave == 2 ? D1 : DK;
#pragma unroll 1
      for (unsigned spins = 0; spins < 128; ++spins) {
        const unsigned r = now(), c = (unsigned)__builtin_amdgcn_s_memtime();
        if ((r - tp) * (c - c0_) > d * (r - r0_ + 1)) break;
        __builtin_amdgcn_s_sleep(2);
      }
    }
    if (f == 30 && wave == 3) {
      const __amdgpu_buffer_rsrc_t ra = __builtin_amdgcn_make_buffer_rsrc((void*)(ws + WS_A), 0, 4096, 0x00020000);
#pragma unroll 1
      for (unsigned spins = 0; spins < SPIN_CAP; ++spins) {
        asm volatile("" ::: "memory");
        if (__all(__builtin_amdgcn_raw_buffer_load_b32(ra, (lane & 31) * 128, 0, 16) == tag)) break;
        __builtin_amdgcn_s_sleep(8);
      }
      if (lane == 0) __hip_atomic_store((gu32*)(ws + WS_E), tag, RLX_AGENT);
    }
    if (wave == 2) {
      const unsigned t_ = now() + D2;
      const u32x4 v = __builtin_amdgcn_raw_buffer_load_b128(rfl, flag_off(), 0, 16);
      *(u32x4*)(smem + L_FS + lane * 16) = (u32x4){(v[1] == tag ? v[0] : t_) + (ETA_F - ETA_G), tag, 0u, tag};
    }
    if (wave == 0) rows_job();
    WAITV(0);
    WAITL0();
    BARRIER();
  }

  {
    const int m = wave & 3, kh = wave >> 2;
    const bool snd = kh != 0;
    const int oo = (rt2 * 4 + m) * 20480 + kh * 8192, ot = (rt2 * 4 + m) * 20480 + 16384 + kh * 2048;
    const int l16 = lane * 16;
    u32x4 fs = *(const u32x4*)(smem + L_FS + l16);
    const u32x4 kk = *(const u32x4*)(smem + L_VAR + 32);
    u32x2 g0_ = *(const u32x2*)(smem + glo);
    u32x4 yw[2][7];
    int yo, y6, yp, yx;
    auto y_prep = [&]() {
      int ly = lane;
      PIN(ly);
      const int cp = (ly & 15) < 10 ? ly & 15 : 0, col = m * 20 + cp * 2 + (ly >> 5);
      yo = (ly >> 4) * 71680 + (nt2 * 80 + m * 20 + cp * 2) * 8;
      y6 = ly >= 48 ? 0x7FFFFF00 : yo;
      yp = (((col >> 4) + 5 - (nt2 & 1)) % 5) * 1024 + (col & 12) * 64 + (col & 3) * 4;
      yx = L_XT + col * 2;
      PIN(yo); PIN(y6); PIN(yp); PIN(yx);
    };
    auto y_srd = [&](int tok) {
      char* wy = ws;
      asm volatile("" : "+s"(wy));
      return make_srd(wy + WS_YP + tok * 5120, 27 * 10240);
    };
    auto y_issue = [&](u32x4(&yv)[7], u32x4 srd, bool again, int j0 = 0, int j1 = 7) {
#pragma unroll
      for (int j = 0; j < 7; ++j) {
        if (j < j0 || j >= j1) continue;
        if (again) asm volatile("buffer_load_dwordx4 %0, %1, %2, %3 offen sc1" : "+v"(yv[j]) : "v"(j < 6 ? yo : y6), "s"(srd), "s"(j * 10240) : "memory");
        else asm volatile("buffer_load_dwordx4 %0, %1, %2, %3 offen sc1" : "=v"(yv[j]) : "v"(j < 6 ? yo : y6), "s"(srd), "s"(j * 10240) : "memory");
      }
    };
    auto y_done = [&](u32x4(&yv)[7], int tok, int row) {
      volatile LAS unsigned* fl = (volatile LAS unsigned*)(LAS char*)(smem + L_VAR + 16 + (row >> 4) * 4);
      volatile LAS float* pe = (volatile LAS float*)(LAS char*)(smem + yp + (row >> 4) * 5120 + (row & 15) * 16);
      unsigned f_, xv_;
      float omg, pv;
      auto rd = [&]() {
        f_ = *fl;
        omg = *(volatile LAS float*)(LAS char*)(smem + L_JUNK + row * 4);
        xv_ = *(volatile LAS u16*)(LAS char*)(smem + yx + row * 160);
        pv = *pe;
      };
      rd();
      const float rsel = *(const float*)(smem + L_JUNK + 256 + tok * 4);
      SB();
      unsigned lo = tag, hi = tag;
      float y[2] = {0.f, 0.f};
#pragma unroll
      for (int j = 0; j < 7; ++j) {
        PIN(yv[j]);
        if (j == 6 && g4 == 3) yv[j][1] = yv[j][3] = tag;
        lo = min(min(lo, yv[j][1]), yv[j][3]);
        hi = max(max(hi, yv[j][1]), yv[j][3]);
        y[0] += __uint_as_float(yv[j][0]);
        PIN(y[0]);
        y[1] += __uint_as_float(yv[j][2]);
        PIN(y[1]);
      }
      if (!__all(lo == hi)) return false;
      const auto a = __builtin_amdgcn_permlane32_swap(__float_as_uint(y[0]), __float_as_uint(y[1]), false, false);
      const float h = __uint_as_float(a[0]) + __uint_as_float(a[1]);
      const auto c = __builtin_amdgcn_permlane16_swap(__float_as_uint(h), __float_as_uint(h), false, false);
      const float r = rbf(rbf(__uint_as_float(c[0]) + __uint_as_float(c[1])) * rsel);
#pragma unroll 1
      for (unsigned spins = 0; spins < (SPIN_CAP << 6) && !__builtin_amdgcn_readfirstlane(f_); ++spins) rd();
      const float xv = bflo(xv_);
      *pe = pv + (rbf(xv + r) - xv) * omg;
      asm volatile("ds_add_u32 %0, %1" ::"v"(fl), "v"(1u) : "memory");
      return true;
    };
    const int l0 = __builtin_amdgcn_readfirstlane(kk[2]), l1 = __builtin_amdgcn_readfirstlane(kk[3]);
    unsigned ry = min((unsigned)l0, (unsigned)l1);
    unsigned need = 1u + ((l0 >> 4) == m ? 256u : 0u) + ((l1 >> 4) == m ? 256u : 0u);
    asm volatile("" : "+s"(ry), "+s"(need));
#define yjob (ry < 64u)
    if (wave == 7) {
      if (__builtin_expect((~__builtin_amdgcn_ballot_w64((fs[1] == tag) & (fs[3] == tag)) & 0xFFull) != 0, 0)) {
        flag_wait(lane >= 8 ? 0x7FFFFF00 : flag_off());
        WAITL0();
        if (lane == 0) *(volatile LAS unsigned*)(LAS char*)(smem + L_VAR + 8) = 1u;
      }
    }
    if (!snd) {
      dma16(adr[0], x, lds0 + L_XT + m * 2560);
      dma16(adr[1], x, lds0 + L_XT + m * 2560 + 1024);
      if (lane < 32) dma16(adr[2], x, lds0 + L_XT + m * 2560 + 2048);
    }
    SB();
    const char* wb = smem + L_WO + kh * 51200 + l16;
    u32x4 wq[WPD][5];
    auto wrd = [&](int k) {
#pragma unroll
      for (int f = 0; f < 5; ++f) wq[k % WPD][f] = *(const u32x4*)(wb + (k * 5 + f) * 1024);
    };
#pragma unroll
    for (int k = 0; k < NPRE; ++k) wrd(k);
    if (snd) {
#pragma unroll
      for (int k = NPRE; k < WPD; ++k) wrd(k);
    }
    if (__builtin_expect(!__all(g0_[1] == tag), 0)) {
      const __amdgpu_buffer_rsrc_t rg_ = __builtin_amdgcn_make_buffer_rsrc((void*)(ws + WS_K), 0, 36864, 0x00020000);
#pragma unroll 1
      for (unsigned spins = 0; spins < SPIN_CAP; ++spins) {
        asm volatile("" ::: "memory");
        g0_ = __builtin_amdgcn_raw_buffer_load_b64(rg_, gate_off(m * 16 + c15), 0, 16);
        if (__all(g0_[1] == tag)) break;
      }
    }
    const float gt = __uint_as_float(g0_[0]);
    SB();
    {
      const u64 inv = ~__builtin_amdgcn_ballot_w64((fs[1] == tag) & (fs[3] == tag));
      if (__builtin_expect((inv & rel) != 0, 0)) {
#pragma unroll 1
        for (unsigned spins = 0; spins < (SPIN_CAP << 6); ++spins) {
          if (__builtin_amdgcn_readfirstlane(*(volatile LAS unsigned*)(LAS char*)(smem + L_VAR + 8))) break;
          __builtin_amdgcn_s_sleep(1);
        }
        fs[0] = *(volatile LAS unsigned*)(LAS char*)(smem + L_FS + l16);
      }
      const unsigned tg = fs[0] + ETA_G;
      unsigned spins = 128;
#pragma unroll 1
      while ((__builtin_amdgcn_ballot_w64((int)(now() - tg) < 0) & rel) != 0 && --spins) {}
    }
    u32x4 of[8];
    u32x2 ofl[2], ofh[2];
    const u32x4 sro = make_srd(ws + WS_OF, S_LEN * DM * 2);
    int l8 = lane * 8;
    asm volatile("buffer_load_dwordx4 %0, %8, %9, %10 offen sc1\n\tbuffer_load_dwordx4 %1, %8, %9, %10 offen offset:1024 sc1\n\t"
                 "buffer_load_dwordx4 %2, %8, %9, %10 offen offset:2048 sc1\n\tbuffer_load_dwordx4 %3, %8, %9, %10 offen offset:3072 sc1\n\t"
                 "buffer_load_dwordx4 %4, %8, %9, %11 offen sc1\n\tbuffer_load_dwordx4 %5, %8, %9, %11 offen offset:1024 sc1\n\t"
                 "buffer_load_dwordx4 %6, %8, %9, %11 offen offset:2048 sc1\n\tbuffer_load_dwordx4 %7, %8, %9, %11 offen offset:3072 sc1"
                 : "=&v"(of[0]), "=&v"(of[1]), "=&v"(of[2]), "=&v"(of[3]), "=&v"(of[4]), "=&v"(of[5]), "=&v"(of[6]), "=&v"(of[7])
                 : "v"(l16), "s"(sro), "s"(oo), "s"(oo + 4096)
                 : "memory");
    asm volatile("buffer_load_dwordx2 %0, %4, %5, %6 offen sc1\n\tbuffer_load_dwordx2 %1, %4, %5, %6 offen offset:512 sc1\n\t"
                 "buffer_load_dwordx2 %2, %4, %5, %6 offen offset:1024 sc1\n\tbuffer_load_dwordx2 %3, %4, %5, %6 offen offset:1536 sc1"
                 : "=&v"(ofl[0]), "=&v"(ofh[0]), "=&v"(ofl[1]), "=&v"(ofh[1])
                 : "v"(l8), "s"(sro), "s"(ot)
                 : "memory");
    if (!snd) {
#pragma unroll
      for (int k = NPRE; k < WPD; ++k) wrd(k);
    }
    auto o_again = [&](int k) {
      if (k < 8)
        asm volatile("buffer_load_dwordx4 %0, %1, %2, %3 offen sc1" : "+v"(of[k]) : "v"(l16), "s"(sro), "s"(oo + k * 1024) : "memory");
      else
        asm volatile("buffer_load_dwordx2 %0, %2, %3, %4 offen sc1\n\tbuffer_load_dwordx2 %1, %2, %3, %4 offen offset:512 sc1"
                     : "+v"(ofl[k - 8]), "+v"(ofh[k - 8])
                     : "v"(l8), "s"(sro), "s"(ot + (k - 8) * 1024)
                     : "memory");
    };
    auto o_pin = [&](int k) {
      if (k < 8) PIN(of[k]);
      else asm volatile("" : "+v"(ofl[k - 8]), "+v"(ofh[k - 8]));
    };
    auto o_get = [&](int k) { return k < 8 ? of[k] : (u32x4){ofl[k - 8][0], ofl[k - 8][1], ofh[k - 8][0], ofh[k - 8][1]}; };
    auto o_bad = [&](int k) {
      const u32x4 v = o_get(k);
      return __any(max(max(v[0], v[1]), max(v[2], v[3])) == SENT);
    };
    int pa_ = m * 5120 + l16;
    PIN(pa_);
    volatile LAS unsigned* pfl = (volatile LAS unsigned*)(LAS char*)(smem + L_VAR + 16 + m * 4);
    u32x4 rout = make_srd(out, S_LEN * DM * 2);
    asm volatile("" : "+s"(rout));
    f32x4 ac2[5], pp[5];
    unsigned pf_ = 0;
    if (!snd) {
      u32x2 xb[5];
      if (__builtin_expect(yjob, 0)) y_prep();
      WAITV(12);
#pragma unroll
      for (int f = 0; f < 5; ++f) xb[f] = *(const u32x2*)(smem + L_XT + m * 2560 + c15 * 160 + g4 * 8 + ((nt2 & 1) ? (f + 1) % 5 : f) * 32);
      const float omg = 1.0f - gt;
      *(float*)(smem + L_JUNK + (m * 16 + c15) * 4) = omg;
#pragma unroll
      for (int f = 0; f < 5; ++f) {
        pp[f] = (f32x4){bflo(xb[f][0]), bfhi(xb[f][0]), bflo(xb[f][1]), bfhi(xb[f][1])} * omg;
        PIN(pp[f]);
      }
    }
#pragma unroll
    for (int f = 0; f < 5; ++f) ac2[f] = (f32x4){0.f, 0.f, 0.f, 0.f};
    __builtin_amdgcn_sched_barrier(0);
    constexpr int GS[5] = {0, 2, 5, 8, 10};
    auto vleft = [](int k) { return k < 8 ? 11 - k : k == 8 ? 2 : 0; };
    WAITV(10);
    o_pin(0);
    o_pin(1);
    bool bad = o_bad(0) | o_bad(1);
#pragma unroll
    for (int g = 0; g < 4; ++g) {
      if (__builtin_expect(bad, 0)) {
        WAITV(0);
#pragma unroll 1
        for (unsigned spins = 0; spins < SPIN_CAP; ++spins) {
          bool any = false;
#pragma unroll
          for (int k2 = GS[g]; k2 < 10; ++k2) {
            o_pin(k2);
            if (o_bad(k2)) {
              o_again(k2);
              if (k2 < GS[g + 1]) any = true;
            }
          }
          WAITV(0);
          if (!any) break;
        }
      }
      if (g == 3) break;
      unsigned mxv = 0;
      const int nm = (GS[g + 1] - GS[g]) * 5, nn = g < 3 ? GS[g + 2] - GS[g + 1] : 0;
#pragma unroll
      for (int i = 0; i < nm; ++i) {
        const int k = GS[g] + i / 5, f = i % 5;
        const int j = i - (nm - 2 - 2 * nn);
        const int kn = GS[g + 1] + j / 2;
        if (j >= 0 && j < 2 * nn && !(j & 1)) {
          WAITV_(vleft(kn));
          o_pin(kn);
          SB();
        }
        ac2[f] = mfma16(wq[k % WPD][f], o_get(k), ac2[f]);
        SB();
        if (k + WPD < 10) wq[k % WPD][f] = *(const u32x4*)(wb + ((k + WPD) * 5 + f) * 1024);
        if (j >= 0 && j < 2 * nn) {
          const u32x4 nv = o_get(kn);
          mxv = max(max(mxv, nv[(j & 1) * 2]), nv[(j & 1) * 2 + 1]);
        }
        if (nn && j == 2 * nn) bad = __any(mxv == SENT);
        if (g == 2 && i == PRD - 1) __builtin_amdgcn_s_waitcnt(0xC07F);
        if (g == 2 && i == PRD && snd) {
          pf_ = *pfl;
          asm volatile("" ::: "memory");
#pragma unroll
          for (int f = 0; f < 5; ++f) pp[f] = *(const f32x4*)(smem + pa_ + f * 1024);
        }
        SB();
      }
    }
    u32x4 o8 = o_get(8), o9 = o_get(9);
    PIN(o8); PIN(o9);
    auto mm = [&](int f) {
      ac2[f] = mfma16(wq[8 % WPD][f], o8, ac2[f]);
      SB();
      ac2[f] = mfma16(wq[9 % WPD][f], o9, ac2[f]);
      SB();
    };
    auto fin = [&](int f, int q) {
      pp[f][q] = __builtin_fmaf(ac2[f][q], gt, pp[f][q]);
      PIN(pp[f][q]);
    };
    if (!snd) {
      SB();
      auto wr = [&](int f) {
#pragma unroll
        for (int q = 0; q < 4; ++q) fin(f, q);
        *(f32x4*)(smem + pa_ + f * 1024) = pp[f];
        SB();
      };
      auto done = [&]() {
        asm volatile("" ::: "memory");
        *pfl = 1u;
      };
      if (__builtin_expect(yjob, 0)) {
        int tok = (unsigned)l0 >= 64u;
        const u32x4 sy = y_srd(tok);
        mm(0); y_issue(yw[0], sy, false, 0, 2); SB(); mm(1); y_issue(yw[0], sy, false, 2, 4); SB(); mm(2); y_issue(yw[0], sy, false, 4, 7); SB();
        if (max((unsigned)l0, (unsigned)l1) < 64u) y_issue(yw[1], y_srd(1), false);
        wr(0); mm(3); wr(1); mm(4); wr(2); wr(3); wr(4);
        done();
        WAITV(0);
#pragma unroll
        for (int j = 0; j < 7; ++j) PIN(yw[1][j]);
#pragma unroll 1
        for (unsigned spins = 0; spins < SPIN_CAP; ++spins) {
          if (y_done(yw[0], tok, tok ? l1 : l0)) {
            if (tok || (unsigned)l1 >= 64u) break;
            tok = 1;
#pragma unroll
            for (int j = 0; j < 7; ++j) yw[0][j] = yw[1][j];
          } else {
            y_issue(yw[0], y_srd(tok), true);
            WAITV(0);
          }
        }
      } else {
        mm(0); mm(1); mm(2);
        wr(0); mm(3); wr(1); mm(4);
        wr(2); wr(3); wr(4);
        done();
      }
    } else {
      SB();
      u32x2 r[5];
      auto cv = [&](int f) {
#pragma unroll
        for (int q = 0; q < 4; ++q) fin(f, q);
        r[f] = (u32x2){pk2(pp[f][0], pp[f][1]), pk2(pp[f][2], pp[f][3])};
        SB();
      };
      auto st = [&](int f) {
        const auto a = __builtin_amdgcn_permlane16_swap(r[f][0], r[f + 1][0], false, false);
        const auto c = __builtin_amdgcn_permlane16_swap(r[f][1], r[f + 1][1], false, false);
        asm volatile("buffer_store_dwordx4 %0, %1, %2, 0 offen offset:%3 sc1\n\ts_nop 1" ::"v"((u32x4){a[0], c[0], a[1], c[1]}), "v"(adr[0]), "s"(rout), "i"(f * 32) : "memory");
        SB();
      };
      if (__builtin_expect(__builtin_amdgcn_readfirstlane(pf_) != need, 0)) {
        mm(0); mm(1); mm(2); mm(3); mm(4);
#pragma unroll 1
        for (unsigned spins = 0; spins < (SPIN_CAP << 6); ++spins) {
          pf_ = *pfl;
          asm volatile("" ::: "memory");
#pragma unroll
          for (int f = 0; f < 5; ++f) pp[f] = *(const f32x4*)(smem + pa_ + f * 1024);
          asm volatile("" ::: "memory");
          if (__builtin_amdgcn_readfirstlane(pf_) == need) break;
        }
        cv(0); cv(1);
      } else {
        mm(0); mm(1); mm(2);
        cv(0); mm(3); cv(1); mm(4);
      }
      st(0); cv(2); cv(3); st(2); cv(4);
      asm volatile("buffer_store_dwordx2 %0, %1, %2, 0 offen sc1" ::"v"(r[4]), "v"(adr[1]), "s"(rout) : "memory");
    }
  }
}

extern "C" void kernel_launch(void* const* d_in, const int* in_sizes, int n_in, void* d_out, int out_size, void* d_ws,
                              size_t ws_size, hipStream_t stream) {
  kf<<<256, 512, 0, stream>>>((const u16*)d_in[0], (const u16*)d_in[1], (const u16*)d_in[2], (const u16*)d_in[4],
                              (const u16*)d_in[8], (const u16*)d_in[5], (const u16*)d_in[6], (const u16*)d_in[7],
                              (char*)d_ws, (u16*)d_out);
}
```

```cpp
#include <hip/hip_runtime.h>
#include <stdint.h>

typedef unsigned short u16;
typedef unsigned long long u64;
typedef __attribute__((ext_vector_type(8))) __bf16 bf16x8;
typedef __attribute__((ext_vector_type(2))) __bf16 bf16x2;
typedef __attribute__((ext_vector_type(4))) short s16x4;
typedef __attribute__((ext_vector_type(4))) float f32x4;
typedef __attribute__((ext_vector_type(2))) float f32x2;
typedef __attribute__((ext_vector_type(4))) unsigned u32x4;
typedef __attribute__((ext_vector_type(2))) unsigned u32x2;
typedef __attribute__((ext_vector_type(10))) unsigned u32x10;
#define GAS __attribute__((address_space(1)))
#define LAS __attribute__((address_space(3)))
typedef GAS unsigned gu32;

#define S_LEN 2048
#define DM 640
#define NQKV 1920
#define FF 1728

#define WS_OF 0
#define WS_WOF 2621440
#define WS_E (WS_WOF + 819200)
#define WS_F2 (WS_E + 256)
#define WS_F (WS_F2 + 32768)
#define WS_K (WS_F + 28672)
#define WS_R (WS_K + 36864)
#define WS_A (WS_R + 256)
#define WS_YP (WS_A + 4096)
#define TAG_STEP 0x9E3779B9u
#define SENT 0xFFFFFFFFu
#define SPIN_CAP (1u << 18)
#define NPRE 3
#define WPD 7
#define PRD 10
#define WB8 2
#define TCY 2750
#define ETA_G 10
#define ETA_F 15
#define D1 6700
#define D2 75
#define DK 4200
#define RLX_AGENT __ATOMIC_RELAXED, __HIP_MEMORY_SCOPE_AGENT

__device__ __forceinline__ float bflo(unsigned u) { return __uint_as_float(u << 16); }
__device__ __forceinline__ float bfhi(unsigned u) { return __uint_as_float(u & 0xFFFF0000u); }
__device__ __forceinline__ unsigned pk2(float a, float b) {
  f32x2 v = {a, b};
  return __builtin_bit_cast(unsigned, __builtin_convertvector(v, bf16x2));
}
__device__ __forceinline__ float rbf(float f) { return bflo(pk2(f, f)); }
__device__ __forceinline__ unsigned perm_lo(unsigned hi_src, unsigned lo_src) {
  return __builtin_amdgcn_perm(hi_src, lo_src, 0x05040100u);
}
__device__ __forceinline__ unsigned perm_hi(unsigned hi_src, unsigned lo_src) {
  return __builtin_amdgcn_perm(hi_src, lo_src, 0x07060302u);
}
__device__ __forceinline__ f32x4 mfma16(u32x4 a, u32x4 b, f32x4 c) {
  return __builtin_amdgcn_mfma_f32_16x16x32_bf16(__builtin_bit_cast(bf16x8, a), __builtin_bit_cast(bf16x8, b), c, 0, 0, 0);
}
__device__ __forceinline__ u32x2 tr_read(const char* p) {
  return __builtin_bit_cast(u32x2, __builtin_amdgcn_ds_read_tr16_b64_v4i16((LAS s16x4*)(p)));
}
#define BARRIER() asm volatile("s_barrier" ::: "memory")
#define WAITV(n) asm volatile("s_waitcnt vmcnt(" #n ")" ::: "memory")
#define WAITV_(n) asm volatile("s_waitcnt vmcnt(%0)" ::"i"(n) : "memory")
#define WAITL0() asm volatile("s_waitcnt lgkmcnt(0)" ::: "memory")

__device__ __forceinline__ void top2_mrg(unsigned& k1, unsigned& k2, unsigned b1, unsigned b2) {
  k2 = max(max(k2, b2), min(k1, b1));
  k1 = max(k1, b1);
}
template <int CTRL>
__device__ __forceinline__ void top2_dpp(unsigned& k1, unsigned& k2) {
  const unsigned b1 = (unsigned)__builtin_amdgcn_update_dpp(0, (int)k1, CTRL, 0xF, 0xF, true);
  const unsigned b2 = (unsigned)__builtin_amdgcn_update_dpp(0, (int)k2, CTRL, 0xF, 0xF, true);
  top2_mrg(k1, k2, b1, b2);
}
__device__ __forceinline__ void top2_row(unsigned& k1, unsigned& k2) {
  top2_dpp<0xB1>(k1, k2);
  top2_dpp<0x4E>(k1, k2);
  top2_dpp<0x141>(k1, k2);
  top2_dpp<0x140>(k1, k2);
}
__device__ __forceinline__ void top2_all(unsigned& k1, unsigned& k2) {
  top2_row(k1, k2);
  {
    auto a = __builtin_amdgcn_permlane16_swap(k1, k1, false, false);
    auto c = __builtin_amdgcn_permlane16_swap(k2, k2, false, false);
    k1 = a[0];
    k2 = c[0];
    top2_mrg(k1, k2, a[1], c[1]);
  }
  {
    auto a = __builtin_amdgcn_permlane32_swap(k1, k1, false, false);
    auto c = __builtin_amdgcn_permlane32_swap(k2, k2, false, false);
    k1 = a[0];
    k2 = c[0];
    top2_mrg(k1, k2, a[1], c[1]);
  }
}
__device__ __forceinline__ void touch_line(const char* p, unsigned lds_off) {
  asm volatile("s_mov_b32 m0, %1\n\ts_nop 0\n\tglobal_load_lds_dword %0, off" ::"v"(p), "s"(lds_off) : "memory");
}
__device__ __forceinline__ void dma16(unsigned voff, const void* sbase, unsigned lds_off) {
  asm volatile("s_mov_b32 m0, %2\n\ts_nop 0\n\tglobal_load_lds_dwordx4 %0, %1" ::"v"(voff), "s"(sbase), "s"(lds_off) : "memory");
}
__device__ __forceinline__ void dma16_sc1(unsigned voff, const void* sbase, unsigned lds_off) {
  asm volatile("s_mov_b32 m0, %2\n\ts_nop 0\n\tglobal_load_lds_dwordx4 %0, %1 sc1" ::"v"(voff), "s"(sbase), "s"(lds_off) : "memory");
}
__device__ __forceinline__ void bdma16(unsigned voff, u32x4 srd, unsigned soff, unsigned lds_off) {
  asm volatile("s_mov_b32 m0, %3\n\ts_nop 0\n\tbuffer_load_dwordx4 %0, %1, %2 offen lds" ::"v"(voff), "s"(srd), "s"(soff), "s"(lds_off) : "memory");
}
__device__ __forceinline__ u32x4 make_srd(const void* p, unsigned bytes) {
  return (u32x4){(unsigned)(uintptr_t)p, (unsigned)((uintptr_t)p >> 32) & 0xFFFFu, bytes, 0x00020000u};
}
__device__ __forceinline__ float key_val(unsigned key) {
  unsigned u = key >> 16;
  u ^= (u & 0x8000u) ? 0x8000u : 0xFFFFu;
  return bflo(u);
}
__device__ __forceinline__ float fmx(float a, float b) { return __builtin_elementwise_maximum(a, b); }
#define SB() __builtin_amdgcn_sched_barrier(0)
__device__ __forceinline__ f32x4 mfma16h(u32x2 a, u32x2 b, f32x4 c) {
  return __builtin_amdgcn_mfma_f32_16x16x16bf16_1k(__builtin_bit_cast(s16x4, a), __builtin_bit_cast(s16x4, b), c, 0, 0, 0);
}

#define SLOT_B 43008
#define L_WO SLOT_B
#define L_EARLY (3 * SLOT_B)
#define L_PIECE L_EARLY
#define L_CAND (L_VAR + 128)
#define L_TOUCH (L_EARLY + 12288)
#define L_JUNK (L_WO + 102400)
#define L_VAR (L_JUNK + 384)
#define L_CAND2 (L_VAR + 48)
#define L_GT (L_VAR + 256)
#define L_FS (L_GT + 1024)
#define L_XT (L_FS + 1024)
#define L_TOTAL (L_XT + 10240)
#define CODE_LINES 256

__device__ __forceinline__ void res_wait(char* ws, char* smem, int lane, unsigned tag, int which) {
  const __amdgpu_buffer_rsrc_t r = __builtin_amdgcn_make_buffer_rsrc((void*)(ws + WS_R), 0, 256, 0x00020000);
  u32x4 v;
#pragma unroll 1
  for (unsigned spins = 0; spins < SPIN_CAP; ++spins) {
    asm volatile("" ::: "memory");
    v = __builtin_amdgcn_raw_buffer_load_b128(r, which * 128, 0, 16);
    if (__all((v[1] == tag) & (v[3] == tag))) break;
    __builtin_amdgcn_s_sleep(2);
  }
  if (lane == 0 && which == 0) *(u32x2*)(smem + L_VAR + 32) = (u32x2){v[0], v[2]};
  if (lane == 0 && which == 1) *(u32x4*)(smem + L_CAND2) = v;
}
__device__ __forceinline__ float dot2(unsigned a, unsigned b, float c) {
  return __builtin_amdgcn_fdot2_f32_bf16(__builtin_bit_cast(bf16x2, a), __builtin_bit_cast(bf16x2, b), c, false);
}
template <int CTRL>
__device__ __forceinline__ float dpp_add(float v) {
  return v + __int_as_float(__builtin_amdgcn_update_dpp(0, __float_as_int(v), CTRL, 0xF, 0xF, true));
}
__device__ __forceinline__ float row_sum(float v) {
  v = dpp_add<0xB1>(v);
  v = dpp_add<0x4E>(v);
  v = dpp_add<0x141>(v);
  return dpp_add<0x140>(v);
}

template <int N> struct IC { static constexpr int value = N; };
#define PIN(v) asm volatile("" : "+v"(v))
__global__ __launch_bounds__(512, 2) void kf(const u16*, const u16*, const u16*, const u16*, const u16*, const u16*, const u16*, const u16*, char*, u16*) {
  __shared__ __attribute__((aligned(16))) char smem[L_TOTAL];
  typedef const __attribute__((address_space(4))) u64* karg_t;
  const karg_t kp0 = (karg_t)__builtin_amdgcn_kernarg_segment_ptr();
  u64 pc;
  asm volatile("s_load_dword s100, %1, 0x0\n\ts_load_dword s101, %1, 0x40\n\ts_getpc_b64 %0" : "=s"(pc) : "s"(kp0) : "s100", "s101");
  const u16 *x, *wqkv, *wo, *w_r, *gate_w, *w_gate, *w_up, *w_down;
  char* ws;
  u16* out;
  auto args = [&]() {
    SB();
    karg_t kp = kp0;
    asm volatile("" : "+s"(kp));
    x = (const u16*)kp[0]; wqkv = (const u16*)kp[1]; wo = (const u16*)kp[2]; w_r = (const u16*)kp[3]; gate_w = (const u16*)kp[4];
    w_gate = (const u16*)kp[5]; w_up = (const u16*)kp[6]; w_down = (const u16*)kp[7]; ws = (char*)kp[8]; out = (u16*)kp[9];
  };
  const int tid = threadIdx.x, b = blockIdx.x;
  {
    const int ln = (b >> 3) + 32 * tid;
    if (ln < CODE_LINES)
      touch_line((const char*)((pc & ~255ull) + ln * 128), (unsigned)(uintptr_t)(LAS char*)smem + L_TOUCH);
  }
  unsigned tag;
  auto get_tag = [&]() {
    asm volatile("s_load_dword %0, %1, 0x0\n\ts_waitcnt lgkmcnt(0)" : "=s"(tag) : "s"(ws + WS_E) : "memory");
    tag += TAG_STEP;
  };
  const int lane = tid & 63;
  const int wave = __builtin_amdgcn_readfirstlane(tid >> 6);
  const int xcd = b & 7, jj = b >> 3;
  const int c15 = lane & 15, g4 = lane >> 4;
  const bool gemm_blk = jj < 28;
  const int q_ = (xcd & 3) * 28 + jj;
  const int rg = q_ >> 2, hg = (xcd >> 2) * 4 + (q_ & 3);
  const int par = rg >= 14, gi = rg - par * 14;
  const int tb = min(73 * gi, 944);
  const int tlo = gi == 0 ? 0 : 73 * gi + 7;
  const int lg = jj >> 2;
  const int rt2 = gemm_blk ? par * 16 + gi : (xcd & 2) * 8 + 14 + (xcd & 1), nt2 = hg;
  const unsigned lds0 = (unsigned)(uintptr_t)(LAS char*)smem;
  auto wo_dma = [&](auto n_, int i0, int st, int lo) {
    const u32x4 srd = make_srd(ws + WS_WOF + nt2 * 102400, 102400);
    int g = __builtin_amdgcn_readfirstlane((i0 + ((lg * (25 - lo)) >> 3)) * 4096);
#pragma unroll
    for (int i = 0; i < decltype(n_)::value; ++i) {
      if (g >= 102400) g -= (25 - lo) * 4096;
      asm volatile("s_add_u32 m0, %3, %2\n\ts_nop 0\n\tbuffer_load_dwordx4 %0, %1, %2 offen sc1 lds\n\t"
                   "buffer_load_dwordx4 %0, %1, %2 offen offset:1024 sc1 lds\n\tbuffer_load_dwordx4 %0, %1, %2 offen offset:2048 sc1 lds\n\t"
                   "buffer_load_dwordx4 %0, %1, %2 offen offset:3072 sc1 lds" ::"v"(lane * 16), "s"(srd), "s"(g), "s"(lds0 + L_WO)
                   : "memory", "scc");
      g += st * 4096;
    }
  };

  int od[5];
  const int pa = tb + c15 - 64 * gi;
  const bool pv = tb + c15 >= tlo;
  auto prepO = [&]() {
    const int h = hg * 4 + (wave & 3);
    const int b0 = (par * 64 + 56 + (pa >> 4)) * 20480, b1 = (par * 64 + gi * 4) * 20480;
    od[0] = (pv && lane < 32) ? b0 + (2 * h + g4) * 256 + (pa & 15) * 16 : 0x7FFFFF00;
    od[1] = b1 + (lane >> 5) * 20480 + (2 * h + (g4 & 1)) * 256 + c15 * 16;
    od[2] = od[1] + 2 * 20480;
    od[3] = b1 + g4 * 20480 + 16384 + h * 128 + c15 * 8;
    od[4] = (pv && g4 == 3) ? b0 + 16384 + h * 128 + (pa & 15) * 8 : 0x7FFFFF00;
#pragma unroll
    for (int e = 0; e < 5; ++e) PIN(od[e]);
  };
  auto row_t = [&](int row) {
    const int a = (xcd & 1) * 64 + row;
    return gemm_blk ? tb + 16 + row : a + (a < 16 ? 0 : (a - 7) / 9) * 64;
  };
  int adr[3], glo;
  u64 rel;
  auto tile_off = [&](int row) { return ((2 * row_t(row) + (rt2 >> 4)) * DM + nt2 * 80) * 2; };
  auto prepB = [&]() {
    const int row = (wave & 3) * 16 + c15;
    if (wave < 4) {
#pragma unroll
      for (int e = 0; e < 3; ++e) {
        const int c = min(e * 64 + lane, 159), r = c / 10;
        adr[e] = tile_off((wave & 3) * 16 + r) + (c - r * 10) * 16;
      }
    } else {
      adr[2] = tile_off(row);
      adr[0] = adr[2] + (nt2 & 1) * 32 + (g4 & 1) * 32 + (g4 >> 1) * 16;
      adr[1] = adr[2] + ((nt2 & 1) ? 0 : 128) + g4 * 8;
    }
    PIN(adr[0]); PIN(adr[1]); PIN(adr[2]);
    glo = L_GT + row * 16 + (((gemm_blk ? tb : 0) + row) & 1) * 8;
    PIN(glo);
    {
      const int a0 = (xcd & 1) * 64 + (wave & 3) * 16, gl = (xcd & 1) ? 6 + (lane >> 3) : min(lane >> 3, 6);
      rel = __builtin_amdgcn_ballot_w64(((lane >> 2) & 1) == (wave >> 2) && (gemm_blk ? lane < 8 : gl >= (a0 < 16 ? 0 : (a0 - 7) / 9) && gl <= (a0 + 8) / 9));
      asm volatile("" : "+s"(rel));
    }
  };
  auto lrow_of = [&](unsigned key) {
    const int i = 2047 - (int)(key & 0xFFFFu);
    const int t = i >> 1, g = max(t - 7, 0) / 73, rho = t - min(73 * g, 944);
    return (i & 1) == (rt2 >> 4) ? (rho >= 16 ? g * 64 + rho - 16 : 896 + t - 64 * g) - (rt2 & 15) * 64 : -1;
  };
  auto rows_job = [&]() {
    if (lane < 2) {
      const unsigned key = *(const unsigned*)(smem + L_VAR + 32 + lane * 4);
      *(int*)(smem + L_VAR + 40 + lane * 4) = lrow_of(key);
      *(float*)(smem + L_JUNK + 256 + lane * 4) = rbf(__builtin_amdgcn_rcpf(1.0f + __expf(-key_val(key))));
    }
  };
  auto gate_off = [&](int row) {
    const int p2 = rt2 >> 4, tg = row_t(row);
    const int q2 = p2 * 2 + (tg >> 9), u = tg & 511;
    return (u < 448 ? (q2 * 56 + (u >> 3)) * 128 + (u & 7) * 8 : (224 + q2 * 16 + ((u - 448) >> 2)) * 128 + (u & 3) * 8) + 64;
  };
  auto flag_off = [&]() {
    const int g = gemm_blk ? gi : (xcd & 1) ? 6 + (lane >> 3) : min(lane >> 3, 6);
    return (((rt2 >> 4) * 14 + g) * 8 + (lane & 7)) * 128 + 56;
  };
#define rfl __builtin_amdgcn_make_buffer_rsrc((void*)(ws + WS_F), 0, 28672, 0x00020000)
  auto gate_dma = [&](int go) { dma16_sc1(go & ~15, ws + WS_K, lds0 + L_GT); };
  auto now = []() { return (unsigned)__builtin_amdgcn_s_memrealtime(); };
  auto flag_wait = [&](int foff) {
#pragma unroll 1
    for (unsigned spins = 0; spins < SPIN_CAP; ++spins) {
      asm volatile("" ::: "memory");
      const u32x4 v = __builtin_amdgcn_raw_buffer_load_b128(rfl, foff, 0, 16);
      if ((v[1] == tag) & (v[3] == tag) & (foff != 0x7FFFFF00)) {
        *(u32x4*)(smem + L_FS + lane * 16) = v;
        foff = 0x7FFFFF00;
      }
      if (__all(foff == 0x7FFFFF00)) break;
      __builtin_amdgcn_s_sleep(8);
    }
  };
#define ro __builtin_amdgcn_make_buffer_rsrc((void*)(ws + WS_OF), 0, S_LEN * DM * 2, 0x00020000)

  if (__builtin_expect(gemm_blk, 1)) {
    const bool wjob = wave < (b >= 160 ? 3 : 2);
    auto job_t = [&]() {
      int b_ = b;
      asm volatile("" : "+s"(b_));
      return (b_ & 1) * 512 + (wave < 2 ? (b_ >> 5) * 64 + (b_ & 4) * 8 + (b_ & 24) + wave * 4 : 448 - 160 + (b_ & ~7) + (b_ & 4));
    };
    u32x4 xs[5], wv[4];
    if (wjob) {
      const int jo0 = lane * 16, jo1 = g4 * 2560 + c15 * 16, jo2 = c15 * 16, jr = (2 * job_t() + ((b >> 1) & 1)) * DM;
      args();
      const u16* xr = x + jr;
      asm volatile("global_load_dwordx4 %0, %9, %12\n\tglobal_load_dwordx4 %1, %9, %12 offset:2560\n\t"
                   "global_load_dwordx4 %2, %9, %13\n\tglobal_load_dwordx4 %3, %9, %13 offset:2560\n\t"
                   "global_load_dwordx4 %4, %10, %12 offset:1024\n\tglobal_load_dwordx4 %5, %9, %14\n\t"
                   "global_load_dwordx4 %6, %11, %14 offset:1024\n\tglobal_load_dwordx4 %7, %9, %15\n\tglobal_load_dwordx4 %8, %11, %15 offset:1024"
                   : "=&v"(xs[0]), "=&v"(xs[1]), "=&v"(xs[2]), "=&v"(xs[3]), "=&v"(xs[4]), "=&v"(wv[0]), "=&v"(wv[1]), "=&v"(wv[2]), "=&v"(wv[3])
                   : "v"(jo0), "v"(jo1), "v"(jo2), "s"(xr), "s"(xr + 4 * DM), "s"(w_r), "s"(gate_w)
                   : "memory");
    }
    if (__builtin_expect(wave >= 4, 1)) {
      const int hw = wave - 4;
      const int rot = (lg % 5) * 2 + (xcd & 1);
      int voffA = (lane >> 3) * (DM * 4) + (((lane & 7) ^ (((hw & 1) * 4 + g4) & 7)) * 16);
      int sA[3];
#pragma unroll
      for (int v = 0; v < 3; ++v) {
        sA[v] = ((tb + 8 * (hw + 4 * v)) * 2 + par) * (DM * 2);
      }
      const int hi = lane >> 5;
      int pair = ((lane & 31) >> 1) ^ (hi | ((hw & 1) << 1) | ((hw >> 1) << 2));
      const int sec = (pair >= 5) + (pair >= 10);
      int voffW = pair == 15 ? 0x7FFFFF00 : hi * (NQKV * 2) + sec * (1280 - 160) + hg * 160 + pair * 32 + ((lane & 1) ^ (sec == 2)) * 16;
      const int n0 = (hw & 1) + 4 * (hw >> 1);
      PIN(voffA); PIN(voffW);
      args();
      const u32x4 rsx = make_srd(x, S_LEN * DM * 2), rsw = make_srd(wqkv, DM * NQKV * 2);
      auto issue = [&](int i, int buf) {
        int kc = rot + (i < 5 ? 2 * i : 2 * i - 9);
        if (kc >= 10) kc -= 10;
        const unsigned dst = lds0 + buf * SLOT_B;
#pragma unroll
        for (int v = 0; v < 3; ++v)
          if (v < 2 || hw < 2) bdma16(voffA, rsx, sA[v] + kc * 128, dst + (hw + 4 * v) * 1024);
        const int so = (kc * 64 + 2 * n0) * (NQKV * 2);
#pragma unroll
        for (int u = 0; u < 8; ++u) {
          const int dn = 2 * (u & 1) + 8 * (u >> 1);
          bdma16(voffW, rsw, so + dn * 2 * (NQKV * 2), dst + 10240 + (n0 + dn) * 1024);
        }
      };
      const int qp = rg * 8 + hg;
      issue(0, 0);
      SB();
      int md = (par * 64 + gi * 4 + 2 * (hw & 1) + (lane >> 5)) * 20480 + 16384 + hg * 512 + (lane & 31) * 16;
      PIN(md);
      int so_ = hw == 0 ? flag_off() : gate_off(lane);
      PIN(so_);
      prepB();
      __builtin_amdgcn_sched_barrier(0);
      issue(1, 1);
      get_tag();
      WAITV(10);
      BARRIER();
      issue(2, 2);
      WAITV(10);
      BARRIER();
      const unsigned r0_ = now(), c0_ = (unsigned)__builtin_amdgcn_s_memtime();
      {
        int buf = 0;
#pragma unroll 1
        for (int i = 3; i < 9; ++i) {
          const bool ex = i == 4 && hw == 2;
          if (ex && lane == 0) __hip_atomic_store((gu32*)(ws + WS_F2 + b * 128), tag, RLX_AGENT);
          issue(i, buf);
          if (i == 3)
            asm volatile("s_load_dword s100, %0, 0x0\n\ts_load_dword s100, %0, 0xa00\n\ts_load_dword s100, %0, 0x1400\n\ts_load_dword s100, %0, 0x1e00\n\t"
                         "s_load_dword s100, %0, 0x2800\n\ts_load_dword s100, %0, 0x3200\n\ts_load_dword s100, %0, 0x3c00\n\ts_load_dword s100, %0, 0x4600" ::"s"(
                             (const char*)out + tile_off(hw * 8 + (nt2 & 1) * 32) + ((nt2 & 1) ? 0 : 128))
                         : "s100");
          if (ex) WAITV(11); else WAITV(10);
          buf = buf == 2 ? 0 : buf + 1;
          BARRIER();
        }
      }
      if (hw == 3) {
        if (lane < 2) dma16_sc1(lane * 128, ws + WS_R, lds0 + L_CAND);
        if (lane < 8) *(unsigned*)(smem + L_VAR + lane * 4) = 0u;
      }
      issue(9, 0);
      auto mark = [&]() {
        const u32x4 sent4 = {SENT, SENT, SENT, SENT};
        const int mo_ = (par * 64 + gi * 4 + (hw & 1) * 2) * 20480 + hg * 2048;
#pragma unroll
        for (int e = 0; e < 5; ++e) {
          if (e == 0) __builtin_amdgcn_raw_buffer_store_b128(sent4, ro, md, 0, 16);
          else __builtin_amdgcn_raw_buffer_store_b128(sent4, ro, lane * 16 + ((e - 1) & 1) * 1024, mo_ + ((e - 1) >> 1) * 20480, 16);
        }
        SB();
      };
      auto words = [&]() {
        const u32x4 rv = *(const u32x4*)(smem + L_CAND + (lane & 1) * 16);
        if (__all((rv[1] == tag) & (rv[3] == tag))) {
          if (tid == 256) *(u32x2*)(smem + L_VAR + 32) = (u32x2){rv[0], rv[2]};
        } else {
          res_wait(ws, smem, lane, tag, 0);
          res_wait(ws, smem, lane, tag, 1);
        }
      };
      if (hw >= 2) {
        mark();
        WAITV(15);
        BARRIER();
        WAITV(5);
        BARRIER();
        unsigned t0_ = now();
        t0_ += (unsigned)((float)(t0_ - r0_) * (float)TCY * __builtin_amdgcn_rcpf((float)((unsigned)__builtin_amdgcn_s_memtime() - c0_)));
        WAITV(0);
        __builtin_amdgcn_raw_buffer_store_b128((u32x4){t0_, tag, t0_, tag}, rfl, lane < 4 ? qp * 128 + (hw & 1) * 64 + lane * 16 : 0x7FFFFF00, 0, 16);
        if (hw == 2) rows_job();
      } else {
        WAITV(10);
        BARRIER();
        words();
        wo_dma(IC<WB8>{}, hw, 2, 25);
        WAITV_(4 * WB8);
        WAITL0();
        BARRIER();
        if (hw == 1) gate_dma(so_);
        wo_dma(IC<12 - WB8>{}, 2 * WB8 + hw, 2, 2 * WB8);
        if (hw == 0 && lane < 8) dma16_sc1(so_, ws + WS_F, lds0 + L_FS);
        if (hw == 0) wo_dma(IC<1>{}, 24, 2, 2 * WB8);
        WAITV(0);
      }
      WAITL0();
      BARRIER();
    } else {
      args();
      const int tj0 = job_t(), pj = par, xi = ((tj0 & 511) - 448) >> 2;
      f32x4 acc[5][4];
#pragma unroll
      for (int m = 0; m < 5; ++m)
#pragma unroll
        for (int f = 0; f < 4; ++f) acc[m][f] = (f32x4){0.f, 0.f, 0.f, 0.f};
      const int wks = b % 20;
      if (b < 200) {
        const int kh = wks >= 10, s = wks - kh * 10, pr = lane >> 3, g = wave, hi = pr >> 2;
        const int row = (s < 8 ? (2 * (kh * 8 + s) + (g >> 1)) * 20 + 4 * (g & 1) + 8 * hi : (kh * 16 + (s - 8) * 8 + hi * 4 + g) * 20 + 16) + (pr & 3);
        asm volatile("s_mov_b32 m0, %2\n\ts_nop 0\n\tglobal_load_lds_dwordx4 %0, %1 nt" ::"v"((row * DM + (b / 20) * 64 + (lane & 7) * 8) * 2), "s"(wo), "s"(lds0 + L_PIECE + wave * 1024) : "memory");
      }
      const __amdgpu_buffer_rsrc_t rk = __builtin_amdgcn_make_buffer_rsrc((void*)(ws + WS_K), 0, 36864, 0x00020000);
      const int ko = c15 != 0 ? 0x7FFFFF00 : wave < 2 ? (rg * 8 + hg) * 128 + (wave * 4 + g4) * 8 : (224 + (xcd & 3) * 16 + xi) * 128 + g4 * 8;
      auto rows4 = [&](float (&p)[5]) {
        const auto a = __builtin_amdgcn_permlane32_swap(__float_as_uint(p[0]), __float_as_uint(p[2]), false, false);
        const auto c = __builtin_amdgcn_permlane32_swap(__float_as_uint(p[1]), __float_as_uint(p[3]), false, false);
        const auto e = __builtin_amdgcn_permlane16_swap(__float_as_uint(__uint_as_float(a[0]) + __uint_as_float(a[1])),
                                                        __float_as_uint(__uint_as_float(c[0]) + __uint_as_float(c[1])), false, false);
        return row_sum(__uint_as_float(e[0]) + __uint_as_float(e[1]) + p[4]);
      };
      const int p4 = lane & 3;
      f32x4 limA, limB;
      unsigned mA[2];
      u32x4 ones;
      u32x2 sel[4], kz[5];
      auto prepM = [&]() {
        int ln = lane;
        PIN(ln);
        const int e0 = (ln & 15) - 4 * (ln >> 4);
#pragma unroll
        for (int i = 0; i < 4; ++i) {
          limA[i] = e0 - i <= -9 ? 0.f : -__builtin_inff();
          limB[i] = (e0 - i >= 0 && e0 - i <= 7) ? 0.f : -__builtin_inff();
        }
        mA[0] = (e0 <= -9 ? 0xFFFFu : 0u) | (e0 - 1 <= -9 ? 0xFFFF0000u : 0u);
        mA[1] = (e0 - 2 <= -9 ? 0xFFFFu : 0u) | (e0 - 3 <= -9 ? 0xFFFF0000u : 0u);
        PIN(limA); PIN(limB); PIN(mA[0]); PIN(mA[1]);
      };
      auto prepS = [&]() {
        int ln = lane;
        PIN(ln);
        ones = (u32x4){0x3F803F80u, 0x3F803F80u, 0x3F803F80u, 0x3F803F80u};
        PIN(ones);
#pragma unroll
        for (int t = 0; t < 4; ++t) {
          const bool on = (ln >> 4) == 2 && ((((ln & 15) >> 2) - t + 1) & 3) < 2;
          sel[t] = (u32x2){on && (ln & 3) < 2 ? 0x3F80u << (16 * (ln & 3)) : 0u, on && (ln & 3) >= 2 ? 0x3F80u << (16 * ((ln & 3) - 2)) : 0u};
          PIN(sel[t]);
        }
#pragma unroll
        for (int t = 0; t < 5; ++t) {
          kz[t] = (u32x2){0u, 0u};
          PIN(kz[t]);
        }
      };
      const int swz = (c15 >> 1) & 7;
      const int aoff0 = c15 * 128 + ((g4 ^ swz) * 16);
      const int aoff1 = c15 * 128 + (((4 + g4) ^ swz) * 16);
      int woff[4];
      {
        const int q4 = c15 >> 2, sw = q4 | ((g4 & 1) << 2);
#pragma unroll
        for (int t = 0; t < 4; ++t) {
          const int sc = t < 3 ? t : p4;
          const int by = wave * 40 + (t < 3 ? p4 : 4) * 8;
          woff[t] = 10240 + (8 * g4 + q4) * 512 + (((sc == 3 ? 15 : sc * 5 + (by >> 5)) ^ sw) * 32) + ((by & 31) ^ (sc == 2 ? 16 : sc == 3 ? 8 : 0));
          PIN(woff[t]);
        }
      }
      int wo3[2][4], ao3[2][2];
#pragma unroll
      for (int q = 0; q < 2; ++q) {
#pragma unroll
        for (int j = 0; j < 4; ++j) {
          wo3[q][j] = woff[j] + (q + 1) * SLOT_B;
          PIN(wo3[q][j]);
        }
        ao3[q][0] = aoff0 + (q + 1) * SLOT_B;
        ao3[q][1] = aoff1 + (q + 1) * SLOT_B;
        PIN(ao3[q][0]); PIN(ao3[q][1]);
      }
#define GM(m, f, wf, af) acc[m][f] = (f) == 2 ? mfma16(af[m], wf[f], acc[m][f]) : mfma16(wf[f], af[m], acc[m][f])
      u32x4 af0[5], wf0[4], af1[5], wf1[4];
      auto rd1 = [&](u32x4(&an)[5], u32x4(&wn)[4], int q, int h, int r) {
        const int ao = q ? ao3[q - 1][h] : h ? aoff1 : aoff0;
        if (r == 2) {
          an[0] = *(const u32x4*)(smem + ao);
        } else if (r >= 9) {
          an[r - 8] = *(const u32x4*)(smem + ao + (r - 8) * 2048);
        } else {
          const int k = r < 2 ? r : r - 1;
          const u32x2 t = tr_read(smem + (q ? wo3[q - 1][k >> 1] : woff[k >> 1]) + h * 16384 + (k & 1) * 2048);
          wn[k >> 1][(k & 1) * 2] = t[0];
          wn[k >> 1][(k & 1) * 2 + 1] = t[1];
        }
      };
      auto half = [&](const u32x4(&a)[5], const u32x4(&w)[4], u32x4(&an)[5], u32x4(&wn)[4], int q, int h) {
#pragma unroll
        for (int i = 0; i < 20; ++i) {
          const int m_ = i < 15 ? i / 3 : 19 - i, j_ = ((i / 3) & 1) ? 2 - i % 3 : i % 3, f_ = i < 15 ? j_ + (j_ == 2) : 2;
          GM(m_, f_, w, a);
          SB();
          if (i < 13) rd1(an, wn, q, h, i);
          SB();
        }
      };
      SB();
      get_tag();
      if (wjob) {
        if (b < 200) WAITV(3); else WAITV(2);
#pragma unroll
        for (int i = 0; i < 5; ++i) PIN(xs[i]);
        PIN(wv[0]); PIN(wv[1]);
        float p[5];
#pragma unroll
        for (int i = 0; i < 5; ++i) {
          p[i] = 0.f;
#pragma unroll
          for (int q = 0; q < 4; ++q) p[i] = dot2(xs[i][q], wv[i == 4][q], p[i]);
        }
        const float r_ = rows4(p);
        unsigned u = pk2(r_, r_) & 0xFFFFu;
        u ^= (u & 0x8000u) ? 0xFFFFu : 0x8000u;
        const unsigned key = (u << 16) | (unsigned)(2047 - (2 * (tj0 + g4) + pj));
        __builtin_amdgcn_raw_buffer_store_b64((u32x2){key, tag}, rk, ko, 0, 16);
        SB();
      }
      u32x2 gv = {0u, 0u};
      int ga = 0x7FFFFF00;
      if (wjob) {
        if (b < 200) WAITV(2); else WAITV(1);
        PIN(wv[2]); PIN(wv[3]);
        float p[5];
#pragma unroll
        for (int i = 0; i < 5; ++i) {
          p[i] = 0.f;
#pragma unroll
          for (int q = 0; q < 4; ++q) {
            const unsigned gw = wv[2 + (i == 4)][q];
            p[i] = dot2(pk2(bflo(xs[i][q]) * bflo(gw), bfhi(xs[i][q]) * bfhi(gw)), 0x3F803F80u, p[i]);
          }
        }
        const float g_ = rbf(rows4(p));
        gv = (u32x2){__float_as_uint(rbf(1.0f / (1.0f + __expf(-g_)))), tag};
        ga = c15 != 0 ? ko : ko + 64;
        SB();
      }
      PIN(gv); PIN(ga);
      u32x4 wfr;
      int wfa;
      {
        if (wjob) WAITV(1); else WAITV(0);
        const char* tp = smem + L_PIECE + wave * 1024 + (c15 >> 2) * 128 + g4 * 32 + (lane & 3) * 8;
        const u32x2 lo = tr_read(tp), hi = tr_read(tp + 512);
        const int T = (b / 20) * 4 + g4;
        wfr = (u32x4){lo[0], lo[1], hi[0], hi[1]};
        wfa = b < 200 ? (((T / 5) * 20 + wks) * 5 + (T + 5 - ((T / 5) & 1)) % 5) * 1024 + (wave * 16 + c15) * 16 : 0x7FFFFF00;
        PIN(wfr); PIN(wfa);
      }
      WAITL0();
      BARRIER();
#pragma unroll
      for (int r = 0; r < 13; ++r) rd1(af0, wf0, 0, 0, r);
      SB();
#pragma unroll
      for (int i = 0; i < 9; ++i) {
        half(af0, wf0, af1, wf1, i % 3, 1);
        if (i == 0) {
          __builtin_amdgcn_raw_buffer_store_b128(wfr, __builtin_amdgcn_make_buffer_rsrc((void*)(ws + WS_WOF), 0, 819200, 0x00020000), wfa, 0, 16);
          __builtin_amdgcn_raw_buffer_store_b64(gv, rk, ga, 0, 16);
        }
        if (i == 1) WAITV(0);
        if (i == 3) prepO();
        if (i == 4) prepM();
        if (i == 5) prepS();
        if (i == 6) prepB();
        SB();
        WAITL0();
        BARRIER();
        if (i < 8) half(af1, wf1, af0, wf0, (i + 1) % 3, 0);
      }
      u32x4 wf2[4], af2[5];
      u32x4 qf[5], kf[5];
      u32x10 vb, tb_;
      u32x4 pf[5], os[3];
      u32x2 pp[5], ob[6], obT, obU;
      f32x4 l[5], sA[5], oA[5], sm[5], tx[5], oT[3];
      f32x2 ar[5][2], nn0[2], nn1[2], nn2[2], nn3[2], nn4[2], nnT[2], nnU[2];
      float mx[6], inv[5], it, iu;
      constexpr u64 mo = 0xFFFF0000FFFF0000ull, mh = 0xFFFFFFFF00000000ull;
      auto selm = [](u64 m_, float a, float c) { return __builtin_amdgcn_inverse_ballot_w64(m_) ? a : c; };
      const float cs = 0.22360679774997896f * 1.4426950408889634f;
      const f32x2 c2 = {cs, cs};
      const f32x4 zero4 = {0.f, 0.f, 0.f, 0.f};
      auto cpy = [](float v) { PIN(v); return v; };
      auto cpyu = [](unsigned v) { PIN(v); return v; };
      auto mulo = [](float v, float c) { PIN(v); return v * c; };
#define XBAR() BARRIER()
      GM(0, 3, wf1, af1); SB();
      af0[0] = *(const u32x4*)(smem + aoff0 + 0); SB();
      { const u32x2 t_ = tr_read(smem + woff[3] + 0); wf0[3][0] = t_[0]; wf0[3][1] = t_[1]; } SB();
      GM(0, 1, wf1, af1); SB();
      { const u32x2 t_ = tr_read(smem + woff[3] + 2048); wf0[3][2] = t_[0]; wf0[3][3] = t_[1]; } SB();
      { const u32x2 t_ = tr_read(smem + woff[1] + 0); wf0[1][0] = t_[0]; wf0[1][1] = t_[1]; } SB();
      GM(0, 0, wf1, af1); SB();
      { const u32x2 t_ = tr_read(smem + woff[1] + 2048); wf0[1][2] = t_[0]; wf0[1][3] = t_[1]; } SB();
      { const u32x2 t_ = tr_read(smem + woff[0] + 0); wf0[0][0] = t_[0]; wf0[0][1] = t_[1]; } SB();
      GM(0, 2, wf1, af1); SB();
      { const u32x2 t_ = tr_read(smem + woff[0] + 2048); wf0[0][2] = t_[0]; wf0[0][3] = t_[1]; } SB();
      af2[0] = *(const u32x4*)(smem + aoff1 + 0); SB();
      GM(1, 3, wf1, af1); SB();
      { const u32x2 t_ = tr_read(smem + woff[2] + 0); wf0[2][0] = t_[0]; wf0[2][1] = t_[1]; } SB();
      { const u32x2 t_ = tr_read(smem + woff[3] + 16384); wf2[3][0] = t_[0]; wf2[3][1] = t_[1]; } SB();
      GM(1, 1, wf1, af1); SB();
      { const u32x2 t_ = tr_read(smem + woff[2] + 2048); wf0[2][2] = t_[0]; wf0[2][3] = t_[1]; } SB();
      { const u32x2 t_ = tr_read(smem + woff[3] + 18432); wf2[3][2] = t_[0]; wf2[3][3] = t_[1]; } SB();
      GM(1, 0, wf1, af1); SB();
      { const u32x2 t_ = tr_read(smem + woff[1] + 16384); wf2[1][0] = t_[0]; wf2[1][1] = t_[1]; } SB();
      { const u32x2 t_ = tr_read(smem + woff[1] + 18432); wf2[1][2] = t_[0]; wf2[1][3] = t_[1]; } SB();
      GM(1, 2, wf1, af1); SB();
      { const u32x2 t_ = tr_read(smem + woff[0] + 16384); wf2[0][0] = t_[0]; wf2[0][1] = t_[1]; } SB();
      { const u32x2 t_ = tr_read(smem + woff[0] + 18432); wf2[0][2] = t_[0]; wf2[0][3] = t_[1]; } SB();
      GM(2, 3, wf1, af1); SB();
      af0[1] = *(const u32x4*)(smem + aoff0 + 2048); SB();
      { const u32x2 t_ = tr_read(smem + woff[2] + 16384); wf2[2][0] = t_[0]; wf2[2][1] = t_[1]; } SB();
      GM(2, 1, wf1, af1); SB();
      af2[1] = *(const u32x4*)(smem + aoff1 + 2048); SB();
      { const u32x2 t_ = tr_read(smem + woff[2] + 18432); wf2[2][2] = t_[0]; wf2[2][3] = t_[1]; } SB();
      GM(2, 0, wf1, af1); SB();
      af0[2] = *(const u32x4*)(smem + aoff0 + 4096); SB();
      af2[2] = *(const u32x4*)(smem + aoff1 + 4096); SB();
      GM(2, 2, wf1, af1); SB();
      af0[3] = *(const u32x4*)(smem + aoff0 + 6144); SB();
      af2[3] = *(const u32x4*)(smem + aoff1 + 6144); SB();
      GM(0, 3, wf0, af0); SB();
      af0[4] = *(const u32x4*)(smem + aoff0 + 8192); SB();
      af2[4] = *(const u32x4*)(smem + aoff1 + 8192); SB();
      GM(0, 1, wf0, af0); SB();
      GM(0, 0, wf0, af0); SB();
      GM(0, 2, wf0, af0); SB();
      GM(0, 3, wf2, af2); SB();
      GM(0, 1, wf2, af2); SB();
      GM(0, 0, wf2, af2); SB();
      GM(0, 2, wf2, af2); SB();
      GM(1, 3, wf0, af0); SB();
      GM(1, 1, wf0, af0); SB();
      GM(1, 0, wf0, af0); SB();
      GM(1, 2, wf0, af0); SB();
      qf[0][2] = pk2(acc[0][3][0], acc[0][3][1]); SB();
      GM(1, 3, wf2, af2); SB();
      qf[0][3] = pk2(acc[0][3][2], acc[0][3][3]); SB();
      { const auto r_ = __builtin_amdgcn_permlane16_swap(qf[0][2], kz[0][0], false, false); qf[0][2] = r_[0]; kf[0][2] = r_[1]; } SB();
      GM(1, 1, wf2, af2); SB();
      kf[0][0] = pk2(acc[0][1][0], acc[0][1][1]); SB();
      { const auto r_ = __builtin_amdgcn_permlane16_swap(qf[0][3], kz[0][1], false, false); qf[0][3] = r_[0]; kf[0][3] = r_[1]; } SB();
      GM(1, 0, wf2, af2); SB();
      kf[0][1] = pk2(acc[0][1][2], acc[0][1][3]); SB();
      qf[0][0] = pk2(acc[0][0][0], acc[0][0][1]); SB();
      tx[0] = mfma16h((u32x2){qf[0][2], qf[0][3]}, sel[0], zero4); SB();
      qf[1][2] = pk2(acc[1][3][0], acc[1][3][1]); SB();
      qf[1][3] = pk2(acc[1][3][2], acc[1][3][3]); SB();
      GM(1, 2, wf2, af2); SB();
      { const auto r_ = __builtin_amdgcn_permlane16_swap(qf[1][2], kz[1][0], false, false); qf[1][2] = r_[0]; kf[1][2] = r_[1]; } SB();
      kf[1][0] = pk2(acc[1][1][0], acc[1][1][1]); SB();
      GM(2, 3, wf0, af0); SB();
      qf[1][0] = pk2(acc[1][0][0], acc[1][0][1]); SB();
      { const auto r_ = __builtin_amdgcn_permlane16_swap(qf[1][3], kz[1][1], false, false); qf[1][3] = r_[0]; kf[1][3] = r_[1]; } SB();
      GM(2, 1, wf0, af0); SB();
      kf[1][1] = pk2(acc[1][1][2], acc[1][1][3]); SB();
      qf[1][1] = pk2(acc[1][0][2], acc[1][0][3]); SB();
      tx[1] = mfma16h((u32x2){qf[1][2], qf[1][3]}, sel[1], zero4); SB();
      qf[0][1] = pk2(acc[0][0][2], acc[0][0][3]); SB();
      tb_[0] = pk2(tx[0][0], tx[0][1]); SB();
      l[1] = mfma16(kf[1], qf[1], limB); SB();
      tb_[1] = pk2(tx[0][2], tx[0][3]); SB();
      vb[0] = pk2(acc[0][2][0], acc[0][2][1]); SB();
      sA[1] = mfma16(kf[0], qf[1], limA); SB();
      vb[1] = pk2(acc[0][2][2], acc[0][2][3]); SB();
      vb[2] = pk2(acc[1][2][0], acc[1][2][1]); SB();
      l[0] = mfma16(kf[0], qf[0], limB); SB();
      vb[3] = pk2(acc[1][2][2], acc[1][2][3]); SB();
      tb_[2] = pk2(tx[1][0], tx[1][1]); SB();
      GM(2, 0, wf0, af0); SB();
      tb_[3] = pk2(tx[1][2], tx[1][3]); SB();
      GM(2, 2, wf0, af0); SB();
      l[1][0] = fmx(l[1][0], sA[1][0]); SB();
      l[1][1] = fmx(l[1][1], sA[1][1]); SB();
      GM(2, 3, wf2, af2); SB();
      l[1][2] = fmx(l[1][2], sA[1][2]); SB();
      mx[0] = fmx(fmx(l[0][0], l[0][1]), l[0][2]); SB();
      GM(2, 1, wf2, af2); SB();
      l[1][3] = fmx(l[1][3], sA[1][3]); SB();
      mx[1] = fmx(fmx(l[1][0], l[1][1]), l[1][2]); SB();
      GM(2, 0, wf2, af2); SB();
      mx[0] = fmx(mx[0], l[0][3]); SB();
      mx[1] = fmx(mx[1], l[1][3]); SB();
      GM(2, 2, wf2, af2); SB();
      qf[2][2] = pk2(acc[2][3][0], acc[2][3][1]); SB();
      qf[2][3] = pk2(acc[2][3][2], acc[2][3][3]); SB();
      GM(3, 3, wf1, af1); SB();
      { const auto r_ = __builtin_amdgcn_permlane16_swap(qf[2][2], kz[2][0], false, false); qf[2][2] = r_[0]; kf[2][2] = r_[1]; } SB();
      kf[2][0] = pk2(acc[2][1][0], acc[2][1][1]); SB();
      GM(3, 1, wf1, af1); SB();
      qf[2][0] = pk2(acc[2][0][0], acc[2][0][1]); SB();
      { const auto r_ = __builtin_amdgcn_permlane16_swap(qf[2][3], kz[2][1], false, false); qf[2][3] = r_[0]; kf[2][3] = r_[1]; } SB();
      GM(3, 0, wf1, af1); SB();
      kf[2][1] = pk2(acc[2][1][2], acc[2][1][3]); SB();
      qf[2][1] = pk2(acc[2][0][2], acc[2][0][3]); SB();
      tx[2] = mfma16h((u32x2){qf[2][2], qf[2][3]}, sel[2], zero4); SB();
      { const auto r_ = __builtin_amdgcn_permlane32_swap(__float_as_uint(mx[0]), __float_as_uint(mx[1]), false, false); mx[0] = __uint_as_float(r_[0]); mx[1] = __uint_as_float(r_[1]); } SB();
      mx[0] = fmx(mx[0], mx[1]); SB();
      l[2] = mfma16(kf[2], qf[2], limB); SB();
      mx[1] = cpy(mx[0]); SB();
      vb[4] = pk2(acc[2][2][0], acc[2][2][1]); SB();
      sA[2] = mfma16(kf[1], qf[2], limA); SB();
      { const auto r_ = __builtin_amdgcn_permlane16_swap(__float_as_uint(mx[0]), __float_as_uint(mx[1]), false, false); mx[0] = __uint_as_float(r_[0]); mx[1] = __uint_as_float(r_[1]); } SB();
      mx[0] = fmx(mx[0], mx[1]); SB();
      GM(3, 2, wf1, af1); SB();
      mx[1] = mx[0] * -cs; SB();
      mx[0] = mulo(mx[0], -cs); SB();
      GM(3, 3, wf0, af0); SB();
      vb[5] = pk2(acc[2][2][2], acc[2][2][3]); SB();
      { const auto r_ = __builtin_amdgcn_permlane32_swap(__float_as_uint(mx[0]), __float_as_uint(mx[1]), false, false); mx[0] = __uint_as_float(r_[0]); mx[1] = __uint_as_float(r_[1]); } SB();
      GM(3, 1, wf0, af0); SB();
      l[2][0] = fmx(l[2][0], sA[2][0]); SB();
      l[2][1] = fmx(l[2][1], sA[2][1]); SB();
      GM(3, 0, wf0, af0); SB();
      l[2][2] = fmx(l[2][2], sA[2][2]); SB();
      l[2][3] = fmx(l[2][3], sA[2][3]); SB();
      GM(3, 2, wf0, af0); SB();
      mx[2] = fmx(fmx(l[2][0], l[2][1]), l[2][2]); SB();
      mx[2] = fmx(mx[2], l[2][3]); SB();
      GM(3, 3, wf2, af2); SB();
      ar[1][0] = __builtin_elementwise_fma((f32x2){l[1][0], l[1][1]}, c2, (f32x2){mx[1], mx[1]}); SB();
      ar[1][1] = __builtin_elementwise_fma((f32x2){l[1][2], l[1][3]}, c2, (f32x2){mx[1], mx[1]}); SB();
      GM(3, 1, wf2, af2); SB();
      ar[0][0] = __builtin_elementwise_fma((f32x2){l[0][0], l[0][1]}, c2, (f32x2){mx[0], mx[0]}); SB();
      ar[0][1] = __builtin_elementwise_fma((f32x2){l[0][2], l[0][3]}, c2, (f32x2){mx[0], mx[0]}); SB();
      GM(3, 0, wf2, af2); SB();
      ar[1][0][0] = __builtin_amdgcn_exp2f(ar[1][0][0]); SB();
      GM(3, 2, wf2, af2); SB();
      qf[3][2] = pk2(acc[3][3][0], acc[3][3][1]); SB();
      qf[3][3] = pk2(acc[3][3][2], acc[3][3][3]); SB();
      GM(4, 3, wf1, af1); SB();
      { const auto r_ = __builtin_amdgcn_permlane16_swap(qf[3][2], kz[3][0], false, false); qf[3][2] = r_[0]; kf[3][2] = r_[1]; } SB();
      kf[3][0] = pk2(acc[3][1][0], acc[3][1][1]); SB();
      GM(4, 1, wf1, af1); SB();
      qf[3][0] = pk2(acc[3][0][0], acc[3][0][1]); SB();
      { const auto r_ = __builtin_amdgcn_permlane16_swap(qf[3][3], kz[3][1], false, false); qf[3][3] = r_[0]; kf[3][3] = r_[1]; } SB();
      GM(4, 0, wf1, af1); SB();
      kf[3][1] = pk2(acc[3][1][2], acc[3][1][3]); SB();
      qf[3][1] = pk2(acc[3][0][2], acc[3][0][3]); SB();
      tx[3] = mfma16h((u32x2){qf[3][2], qf[3][3]}, sel[3], zero4); SB();
      ar[1][0][1] = __builtin_amdgcn_exp2f(ar[1][0][1]); SB();
      l[3] = mfma16(kf[3], qf[3], limB); SB();
      ar[1][1][0] = __builtin_amdgcn_exp2f(ar[1][1][0]); SB();
      sA[3] = mfma16(kf[2], qf[3], limA); SB();
      ar[1][1][1] = __builtin_amdgcn_exp2f(ar[1][1][1]); SB();
      GM(4, 2, wf1, af1); SB();
      ar[0][0][0] = __builtin_amdgcn_exp2f(ar[0][0][0]); SB();
      GM(4, 3, wf0, af0); SB();
      ar[0][0][1] = __builtin_amdgcn_exp2f(ar[0][0][1]); SB();
      GM(4, 1, wf0, af0); SB();
      ar[0][1][0] = __builtin_amdgcn_exp2f(ar[0][1][0]); SB();
      GM(4, 0, wf0, af0); SB();
      l[3][0] = fmx(l[3][0], sA[3][0]); SB();
      l[3][1] = fmx(l[3][1], sA[3][1]); SB();
      GM(4, 2, wf0, af0); SB();
      l[3][2] = fmx(l[3][2], sA[3][2]); SB();
      l[3][3] = fmx(l[3][3], sA[3][3]); SB();
      GM(4, 3, wf2, af2); SB();
      mx[3] = fmx(fmx(l[3][0], l[3][1]), l[3][2]); SB();
      mx[3] = fmx(mx[3], l[3][3]); SB();
      GM(4, 1, wf2, af2); SB();
      ar[0][1][1] = __builtin_amdgcn_exp2f(ar[0][1][1]); SB();
      GM(4, 0, wf2, af2); SB();
      { const auto r_ = __builtin_amdgcn_permlane32_swap(__float_as_uint(mx[2]), __float_as_uint(mx[3]), false, false); mx[2] = __uint_as_float(r_[0]); mx[3] = __uint_as_float(r_[1]); } SB();
      mx[2] = fmx(mx[2], mx[3]); SB();
      GM(4, 2, wf2, af2); SB();
      qf[4][2] = pk2(acc[4][3][0], acc[4][3][1]); SB();
      qf[4][3] = pk2(acc[4][3][2], acc[4][3][3]); SB();
      mx[3] = cpy(mx[2]); SB();
      { const auto r_ = __builtin_amdgcn_permlane16_swap(qf[4][2], kz[4][0], false, false); qf[4][2] = r_[0]; kf[4][2] = r_[1]; } SB();
      kf[4][0] = pk2(acc[4][1][0], acc[4][1][1]); SB();
      qf[4][0] = pk2(acc[4][0][0], acc[4][0][1]); SB();
      { const auto r_ = __builtin_amdgcn_permlane16_swap(qf[4][3], kz[4][1], false, false); qf[4][3] = r_[0]; kf[4][3] = r_[1]; } SB();
      kf[4][1] = pk2(acc[4][1][2], acc[4][1][3]); SB();
      qf[4][1] = pk2(acc[4][0][2], acc[4][0][3]); SB();
      tx[4] = mfma16h((u32x2){qf[4][2], qf[4][3]}, sel[0], zero4); SB();
      { const auto r_ = __builtin_amdgcn_permlane16_swap(__float_as_uint(mx[2]), __float_as_uint(mx[3]), false, false); mx[2] = __uint_as_float(r_[0]); mx[3] = __uint_as_float(r_[1]); } SB();
      mx[2] = fmx(mx[2], mx[3]); SB();
      l[4] = mfma16(kf[4], qf[4], limB); SB();
      mx[3] = mx[2] * -cs; SB();
      mx[2] = mulo(mx[2], -cs); SB();
      sA[4] = mfma16(kf[3], qf[4], limA); SB();
      pp[1][0] = pk2(ar[1][0][0], ar[1][0][1]); SB();
      { const auto r_ = __builtin_amdgcn_permlane32_swap(__float_as_uint(mx[2]), __float_as_uint(mx[3]), false, false); mx[2] = __uint_as_float(r_[0]); mx[3] = __uint_as_float(r_[1]); } SB();
      ar[2][0] = __builtin_elementwise_fma((f32x2){l[2][0], l[2][1]}, c2, (f32x2){mx[2], mx[2]}); SB();
      ar[2][1] = __builtin_elementwise_fma((f32x2){l[2][2], l[2][3]}, c2, (f32x2){mx[2], mx[2]}); SB();
      ar[2][0][0] = __builtin_amdgcn_exp2f(ar[2][0][0]); SB();
      ar[2][0][1] = __builtin_amdgcn_exp2f(ar[2][0][1]); SB();
      ar[2][1][0] = __builtin_amdgcn_exp2f(ar[2][1][0]); SB();
      ar[2][1][1] = __builtin_amdgcn_exp2f(ar[2][1][1]); SB();
      l[4][0] = fmx(l[4][0], sA[4][0]); SB();
      l[4][1] = fmx(l[4][1], sA[4][1]); SB();
      l[4][2] = fmx(l[4][2], sA[4][2]); SB();
      l[4][3] = fmx(l[4][3], sA[4][3]); SB();
      mx[4] = fmx(fmx(l[4][0], l[4][1]), l[4][2]); SB();
      mx[4] = fmx(mx[4], l[4][3]); SB();
      mx[5] = cpy(mx[4]); SB();
      pp[1][1] = pk2(ar[1][1][0], ar[1][1][1]); SB();
      pp[2][0] = pk2(ar[2][0][0], ar[2][0][1]); SB();
      { const auto r_ = __builtin_amdgcn_permlane32_swap(__float_as_uint(mx[4]), __float_as_uint(mx[5]), false, false); mx[4] = __uint_as_float(r_[0]); mx[5] = __uint_as_float(r_[1]); } SB();
      mx[4] = fmx(mx[4], mx[5]); SB();
      mx[5] = cpy(mx[4]); SB();
      pp[2][1] = pk2(ar[2][1][0], ar[2][1][1]); SB();
      pp[0][0] = pk2(ar[0][0][0], ar[0][0][1]); SB();
      { const auto r_ = __builtin_amdgcn_permlane16_swap(__float_as_uint(mx[4]), __float_as_uint(mx[5]), false, false); mx[4] = __uint_as_float(r_[0]); mx[5] = __uint_as_float(r_[1]); } SB();
      pp[0][1] = pk2(ar[0][1][0], ar[0][1][1]); SB();
      mx[4] = fmx(mx[4], mx[5]); SB();
      pf[1][0] = pp[1][0] & mA[0]; SB();
      sm[0] = mfma16h((u32x2){ones[0], ones[1]}, pp[0], zero4); SB();
      pf[1][2] = pp[1][0] & ~mA[0]; SB();
      pf[1][1] = pp[1][1] & mA[1]; SB();
      oA[0] = mfma16h((u32x2){vb[0], vb[1]}, pp[0], zero4); SB();
      pf[1][3] = pp[1][1] & ~mA[1]; SB();
      pf[2][0] = pp[2][0] & mA[0]; SB();
      oT[2] = mfma16h((u32x2){tb_[0], tb_[1]}, pp[0], zero4); SB();
      pf[2][2] = pp[2][0] & ~mA[0]; SB();
      pf[2][1] = pp[2][1] & mA[1]; SB();
      sm[1] = mfma16(ones, pf[1], zero4); SB();
      pf[2][3] = pp[2][1] & ~mA[1]; SB();
      mx[4] = mx[4] * -cs; SB();
      PIN(tb_); oT[0] = mfma16(__builtin_shufflevector(tb_, tb_, 0, 1, 2, 3), pf[1], zero4); SB();
      ar[3][0] = __builtin_elementwise_fma((f32x2){l[3][0], l[3][1]}, c2, (f32x2){mx[3], mx[3]}); SB();
      ar[3][1] = __builtin_elementwise_fma((f32x2){l[3][2], l[3][3]}, c2, (f32x2){mx[3], mx[3]}); SB();
      sm[2] = mfma16(ones, pf[2], zero4); SB();
      ar[4][0] = __builtin_elementwise_fma((f32x2){l[4][0], l[4][1]}, c2, (f32x2){mx[4], mx[4]}); SB();
      ar[4][1] = __builtin_elementwise_fma((f32x2){l[4][2], l[4][3]}, c2, (f32x2){mx[4], mx[4]}); SB();
      PIN(vb); oA[1] = mfma16(__builtin_shufflevector(vb, vb, 0, 1, 2, 3), pf[1], zero4); SB();
      ar[3][0][0] = __builtin_amdgcn_exp2f(ar[3][0][0]); SB();
      PIN(vb); oA[2] = mfma16(__builtin_shufflevector(vb, vb, 2, 3, 4, 5), pf[2], zero4); SB();
      ar[3][0][1] = __builtin_amdgcn_exp2f(ar[3][0][1]); SB();
      ar[3][1][0] = __builtin_amdgcn_exp2f(ar[3][1][0]); SB();
      ar[3][1][1] = __builtin_amdgcn_exp2f(ar[3][1][1]); SB();
      ar[4][0][0] = __builtin_amdgcn_exp2f(ar[4][0][0]); SB();
      ar[4][0][1] = __builtin_amdgcn_exp2f(ar[4][0][1]); SB();
      ar[4][1][0] = __builtin_amdgcn_exp2f(ar[4][1][0]); SB();
      ar[4][1][1] = __builtin_amdgcn_exp2f(ar[4][1][1]); SB();
      pp[3][0] = pk2(ar[3][0][0], ar[3][0][1]); SB();
      pp[3][1] = pk2(ar[3][1][0], ar[3][1][1]); SB();
      pp[4][0] = pk2(ar[4][0][0], ar[4][0][1]); SB();
      pp[4][1] = pk2(ar[4][1][0], ar[4][1][1]); SB();
      inv[0] = __builtin_amdgcn_rcpf(sm[0][0]); PIN(sm[0]); SB();
      inv[1] = __builtin_amdgcn_rcpf(sm[1][0]); PIN(sm[1]); SB();
      inv[2] = __builtin_amdgcn_rcpf(sm[2][0]); PIN(sm[2]); SB();
      pf[3][0] = pp[3][0] & mA[0]; SB();
      pf[3][2] = pp[3][0] & ~mA[0]; SB();
      pf[3][1] = pp[3][1] & mA[1]; SB();
      pf[3][3] = pp[3][1] & ~mA[1]; SB();
      pf[4][0] = pp[4][0] & mA[0]; SB();
      pf[4][2] = pp[4][0] & ~mA[0]; SB();
      sm[3] = mfma16(ones, pf[3], zero4); SB();
      pf[4][1] = pp[4][1] & mA[1]; SB();
      pf[4][3] = pp[4][1] & ~mA[1]; SB();
      tb_[4] = pk2(tx[2][0], tx[2][1]); SB();
      tb_[5] = pk2(tx[2][2], tx[2][3]); SB();
      sm[4] = mfma16(ones, pf[4], zero4); SB();
      nn0[0] = (f32x2){oA[0][0], oA[0][1]} * (f32x2){inv[0], inv[0]}; SB();
      nn0[1] = (f32x2){oA[0][2], oA[0][3]} * (f32x2){inv[0], inv[0]}; SB();
      PIN(tb_); oT[1] = mfma16(__builtin_shufflevector(tb_, tb_, 2, 3, 4, 5), pf[2], zero4); SB();
      ob[0][0] = pk2(nn0[0][0], nn0[0][1]); SB();
      ob[0][1] = pk2(nn0[1][0], nn0[1][1]); SB();
      nn1[0] = (f32x2){oA[1][0], oA[1][1]} * (f32x2){inv[1], inv[1]}; SB();
      nn1[1] = (f32x2){oA[1][2], oA[1][3]} * (f32x2){inv[1], inv[1]}; SB();
      nn2[0] = (f32x2){oA[2][0], oA[2][1]} * (f32x2){inv[2], inv[2]}; SB();
      nn2[1] = (f32x2){oA[2][2], oA[2][3]} * (f32x2){inv[2], inv[2]}; SB();
      vb[6] = pk2(acc[3][2][0], acc[3][2][1]); SB();
      vb[7] = pk2(acc[3][2][2], acc[3][2][3]); SB();
      vb[8] = pk2(acc[4][2][0], acc[4][2][1]); SB();
      vb[9] = pk2(acc[4][2][2], acc[4][2][3]); SB();
      PIN(vb); oA[3] = mfma16(__builtin_shufflevector(vb, vb, 4, 5, 6, 7), pf[3], zero4); SB();
      tb_[6] = pk2(tx[3][0], tx[3][1]); SB();
      tb_[7] = pk2(tx[3][2], tx[3][3]); SB();
      PIN(vb); oA[4] = mfma16(__builtin_shufflevector(vb, vb, 6, 7, 8, 9), pf[4], zero4); SB();
      tb_[8] = pk2(tx[4][0], tx[4][1]); SB();
      tb_[9] = pk2(tx[4][2], tx[4][3]); SB();
      PIN(tb_); oT[0] = mfma16(__builtin_shufflevector(tb_, tb_, 4, 5, 6, 7), pf[3], oT[0]); SB();
      ob[1][0] = pk2(nn1[0][0], nn1[0][1]); SB();
      ob[1][1] = pk2(nn1[1][0], nn1[1][1]); SB();
      PIN(tb_); oT[1] = mfma16(__builtin_shufflevector(tb_, tb_, 6, 7, 8, 9), pf[4], oT[1]); SB();
      ob[2][0] = pk2(nn2[0][0], nn2[0][1]); SB();
      ob[2][1] = pk2(nn2[1][0], nn2[1][1]); SB();
      ob[5][0] = cpyu(ob[0][0]); SB();
      ob[5][1] = cpyu(ob[0][1]); SB();
      nnU[0] = (f32x2){oT[2][0], oT[2][1]} * (f32x2){inv[0], inv[0]}; SB();
      nnU[1] = (f32x2){oT[2][2], oT[2][3]} * (f32x2){inv[0], inv[0]}; SB();
      obU[0] = pk2(nnU[0][0], nnU[0][1]); SB();
      obU[1] = pk2(nnU[1][0], nnU[1][1]); SB();
      { const auto r_ = __builtin_amdgcn_permlane32_swap(ob[0][0], ob[5][0], false, false); os[0][0] = r_[0]; os[0][2] = r_[1]; } SB();
      { const auto r_ = __builtin_amdgcn_permlane32_swap(ob[0][1], ob[5][1], false, false); os[0][1] = r_[0]; os[0][3] = r_[1]; } SB();
      { const auto r_ = __builtin_amdgcn_permlane32_swap(ob[1][0], ob[2][0], false, false); os[1][0] = r_[0]; os[1][2] = r_[1]; } SB();
      { const auto r_ = __builtin_amdgcn_permlane32_swap(ob[1][1], ob[2][1], false, false); os[1][1] = r_[0]; os[1][3] = r_[1]; } SB();
      inv[3] = __builtin_amdgcn_rcpf(sm[3][0]); PIN(sm[3]); SB();
      inv[4] = __builtin_amdgcn_rcpf(sm[4][0]); PIN(sm[4]); SB();
      it = selm(mo, inv[2], inv[1]); SB();
      iu = selm(mo, inv[4], inv[3]); SB();
      nn3[0] = (f32x2){oA[3][0], oA[3][1]} * (f32x2){inv[3], inv[3]}; SB();
      nn3[1] = (f32x2){oA[3][2], oA[3][3]} * (f32x2){inv[3], inv[3]}; SB();
      nn4[0] = (f32x2){oA[4][0], oA[4][1]} * (f32x2){inv[4], inv[4]}; SB();
      nn4[1] = (f32x2){oA[4][2], oA[4][3]} * (f32x2){inv[4], inv[4]}; SB();
      it = selm(mh, iu, it); SB();
      oT[0][0] = selm(mo, oT[1][0], oT[0][0]); SB();
      oT[0][1] = selm(mo, oT[1][1], oT[0][1]); SB();
      oT[0][2] = selm(mo, oT[1][2], oT[0][2]); SB();
      oT[0][3] = selm(mo, oT[1][3], oT[0][3]); SB();
      ob[3][0] = pk2(nn3[0][0], nn3[0][1]); SB();
      ob[3][1] = pk2(nn3[1][0], nn3[1][1]); SB();
      ob[4][0] = pk2(nn4[0][0], nn4[0][1]); SB();
      ob[4][1] = pk2(nn4[1][0], nn4[1][1]); SB();
      nnT[0] = (f32x2){oT[0][0], oT[0][1]} * (f32x2){it, it}; SB();
      nnT[1] = (f32x2){oT[0][2], oT[0][3]} * (f32x2){it, it}; SB();
      obT[0] = pk2(nnT[0][0], nnT[0][1]); SB();
      obT[1] = pk2(nnT[1][0], nnT[1][1]); SB();
      { const auto r_ = __builtin_amdgcn_permlane32_swap(ob[3][0], ob[4][0], false, false); os[2][0] = r_[0]; os[2][2] = r_[1]; } SB();
      { const auto r_ = __builtin_amdgcn_permlane32_swap(ob[3][1], ob[4][1], false, false); os[2][1] = r_[0]; os[2][3] = r_[1]; } SB();
      XBAR(); SB();
      __builtin_amdgcn_raw_buffer_store_b128(os[1], ro, od[1], 0, 16); SB();
      __builtin_amdgcn_raw_buffer_store_b128(os[2], ro, od[2], 0, 16); SB();
      __builtin_amdgcn_raw_buffer_store_b64(obT, ro, od[3], 0, 16); SB();
      __builtin_amdgcn_raw_buffer_store_b128(os[0], ro, od[0], 0, 16); SB();
      __builtin_amdgcn_raw_buffer_store_b64(obU, ro, od[4], 0, 16); SB();
    }
  } else {
    const int f = b - 224;
    args();
    float* red = (float*)smem;
    u16* hs = (u16*)(smem + 32768);
    const bool do1 = f < 27;
    const bool lead = f == 31;
    if (__builtin_expect(lead, 0)) {
      get_tag();
      const __amdgpu_buffer_rsrc_t rk = __builtin_amdgcn_make_buffer_rsrc((void*)(ws + WS_K), 0, 36864, 0x00020000);
      u32x2 kv[2];
      int ko[2];
      ko[0] = (tid >> 2) * 128 + (tid & 3) * 16;
      ko[1] = tid < 384 ? (128 + (tid >> 2)) * 128 + (tid & 3) * 16 : (224 + ((tid - 384) >> 1)) * 128 + (tid & 1) * 16;
      kv[0] = kv[1] = (u32x2){0u, 0u};
#pragma unroll 1
      for (unsigned spins = 0; spins < SPIN_CAP; ++spins) {
        asm volatile("" ::: "memory");
        u32x4 t_[2];
#pragma unroll
        for (int q = 0; q < 2; ++q) t_[q] = __builtin_amdgcn_raw_buffer_load_b128(rk, ko[q], 0, 16);
        bool ok = true;
#pragma unroll
        for (int q = 0; q < 2; ++q)
          if (ko[q] != 0x7FFFFF00) {
            if ((t_[q][1] == tag) & (t_[q][3] == tag)) {
              kv[q] = (u32x2){t_[q][0], t_[q][2]};
              ko[q] = 0x7FFFFF00;
            } else {
              ok = false;
            }
          }
        if (__all(ok)) break;
        __builtin_amdgcn_s_sleep(4);
      }
      unsigned k1 = max(kv[0][0], kv[0][1]), k2 = min(kv[0][0], kv[0][1]);
      top2_mrg(k1, k2, max(kv[1][0], kv[1][1]), min(kv[1][0], kv[1][1]));
      top2_all(k1, k2);
      unsigned* lk = (unsigned*)(smem + L_VAR + 64);
      if (lane == 0) *(u32x2*)(lk + wave * 2) = (u32x2){k1, k2};
      WAITL0();
      BARRIER();
      k1 = 0u;
      k2 = 0u;
#pragma unroll
      for (int q = 0; q < 4; ++q) {
        const u32x4 a_ = *(const u32x4*)(lk + q * 4);
        top2_mrg(k1, k2, a_[0], a_[1]);
        top2_mrg(k1, k2, a_[2], a_[3]);
      }
      if (tid == 0) {
        const __amdgpu_buffer_rsrc_t rr = __builtin_amdgcn_make_buffer_rsrc((void*)(ws + WS_R), 0, 128, 0x00020000);
        __builtin_amdgcn_raw_buffer_store_b128((u32x4){k1, tag, k2, tag}, rr, 0, 0, 16);
        *(u32x2*)(smem + L_VAR + 32) = (u32x2){k1, k2};
      }
    }
    if (__builtin_expect(f == 30, 0)) {
      get_tag();
      if (wave < 4) {
        const __amdgpu_buffer_rsrc_t rf = __builtin_amdgcn_make_buffer_rsrc((void*)(ws + WS_F2), 0, 32768, 0x00020000);
        int fo = tid < 227 ? tid * 128 : 0x7FFFFF00;
#pragma unroll 1
        for (unsigned spins = 0; spins < SPIN_CAP; ++spins) {
          asm volatile("" ::: "memory");
          if (__builtin_amdgcn_raw_buffer_load_b32(rf, fo, 0, 16) == tag) fo = 0x7FFFFF00;
          if (__all(fo == 0x7FFFFF00)) break;
          __builtin_amdgcn_s_sleep(4);
        }
      }
      BARRIER();
      if (tid == 0) {
        const __amdgpu_buffer_rsrc_t rr = __builtin_amdgcn_make_buffer_rsrc((void*)(ws + WS_R), 0, 256, 0x00020000);
        const unsigned t_ = now();
        __builtin_amdgcn_raw_buffer_store_b128((u32x4){t_, tag, t_, tag}, rr, 128, 0, 16);
        *(u32x4*)(smem + L_CAND2) = (u32x4){t_, tag, t_, tag};
      }
    }
    if (__builtin_expect(f >= 27 && f < 30, 0)) {
      get_tag();
#pragma unroll 1
      for (int c = wave * 3 + f - 27; c < 320; c += 24)
        __builtin_amdgcn_raw_buffer_store_b128((u32x4){SENT, SENT, SENT, SENT}, ro, lane * 16, (c >= 160 ? 112 * 20480 : 56 * 20480) + c * 1024, 16);
      WAITV(0);
      BARRIER();
      if (tid == 0) __hip_atomic_store((gu32*)(ws + WS_F2 + (197 + f) * 128), tag, RLX_AGENT);
    }
    const int g0 = wave < 4 ? wave * 3 : 4 + wave * 2, ng = wave < 4 ? 3 : 2;
    u32x4 r1[3][8], wd[2][8];
    int vo2 = (wave - 3) * 256 + c15 * 16 + g4 * 8 * (DM * 2);
    PIN(vo2);
    if (do1) {
      const char* pa = (const char*)(c15 < 8 ? w_gate : w_up) + f * 128 + (c15 & 7) * 16 + (g0 * 32 + g4 * 8) * (FF * 2);
      const GAS char* pp[12];
#pragma unroll
      for (int j = 0; j < 12; ++j) {
        pp[j] = (const GAS char*)pa + ((j >> 2) * 32 + (j & 3) * 2) * (FF * 2) + FF;
        PIN(pp[j]);
      }
      SB();
#pragma unroll
      for (int j = 0; j < 24; ++j)
        if (j < 16 || wave < 4) r1[j >> 3][j & 7] = __builtin_nontemporal_load((const GAS u32x4*)(pp[j >> 1] + ((j & 1) ? FF : -FF)));
    }
    SB();
    BARRIER();
    if (do1 && wave >= 3) {
      const u32x4 r = make_srd(w_down, DM * FF * 2);
#pragma unroll
      for (int j = 0; j < 16; ++j)
        asm volatile("buffer_load_dwordx4 %0, %1, %2, %3 offen nt" : "=v"(wd[j >> 3][j & 7]) : "v"(vo2), "s"(r), "s"((f * 64 + (j >> 3) * 32 + (j & 7)) * (DM * 2)) : "memory");
    }
    SB();
    if (f < 30) get_tag();
    prepB();
    auto transp = [&](u32x4(&w)[8]) {
      u32x4 t[8];
#pragma unroll
      for (int d = 0; d < 4; ++d)
#pragma unroll
        for (int p = 0; p < 4; ++p) {
          t[2 * d][p] = perm_lo(w[2 * p + 1][d], w[2 * p][d]);
          t[2 * d + 1][p] = perm_hi(w[2 * p + 1][d], w[2 * p][d]);
        }
#pragma unroll
      for (int j = 0; j < 8; ++j) {
        w[j] = t[j];
        PIN(w[j]);
      }
    };
    if (do1) {
      transp(r1[0]);
      transp(r1[1]);
      if (wave < 4) transp(r1[2]);
    }
    __builtin_amdgcn_s_waitcnt(0x0F70);
    if (wave == 0 && !lead) {
      res_wait(ws, smem, lane, tag, 0);
      if (do1) {
        WAITL0();
        const u32x2 kq = *(const u32x2*)(smem + L_VAR + 32);
#pragma unroll
        for (int t = 0; t < 2; ++t) {
          const u16* xr = x + (2047 - (int)(__builtin_amdgcn_readfirstlane(kq[t]) & 2047u)) * DM;
          dma16(lane * 16, xr, lds0 + 36864 + t * 1296);
          if (lane < 16) dma16(lane * 16, xr + 512, lds0 + 36864 + t * 1296 + 1024);
        }
      }
    }
    if (wave == 1) touch_line((const char*)out + tile_off((lane & 31) + (nt2 & 1) * 32) + ((nt2 & 1) ? 0 : 128), lds0 + L_TOUCH + 512);
    if (wave == 1 && f != 30 && !do1) res_wait(ws, smem, lane, tag, 1);
    if (wave < 2) WAITV(0);
    WAITL0();
    BARRIER();
    const unsigned r0_ = now(), c0_ = (unsigned)__builtin_amdgcn_s_memtime();
    if (tid == 128) __hip_atomic_store((gu32*)(ws + WS_A + f * 128), tag, RLX_AGENT);
    const __amdgpu_buffer_rsrc_t ryp = __builtin_amdgcn_make_buffer_rsrc((void*)(ws + WS_YP), 0, 27 * 10240, 0x00020000);
    auto pick = [&](const f32x4(&a)[8]) {
      f32x4 v;
#pragma unroll
      for (int q = 0; q < 4; ++q) {
        const float lo = (g4 & 1) ? a[2 + (q >> 1)][q & 1] : a[q >> 1][q & 1], hi = (g4 & 1) ? a[6 + (q >> 1)][q & 1] : a[4 + (q >> 1)][q & 1];
        v[q] = (g4 & 2) ? hi : lo;
      }
      return v;
    };
    if (do1) {
      u32x4 xf[3];
#pragma unroll
      for (int k = 0; k < 3; ++k) xf[k] = *(const u32x4*)(smem + 36864 + (c15 & 1) * 1296 + ((g0 + (k < ng ? k : 0)) * 32 + g4 * 8) * 2);
      f32x4 acc[8];
#pragma unroll
      for (int ff = 0; ff < 8; ++ff) acc[ff] = (f32x4){0.f, 0.f, 0.f, 0.f};
#pragma unroll
      for (int k = 0; k < 3; ++k)
        if (k < ng) {
#pragma unroll
          for (int j = 0; j < 8; ++j) acc[j] = mfma16(xf[k], r1[k][j], acc[j]);
        }
      *(f32x4*)(red + wave * 256 + c15 * 16 + g4 * 4) = pick(acc);
      WAITL0();
      BARRIER();
      u32x4 wt[2][8];
      if (wave < 2) {
        float gs = 0.f, us = 0.f;
#pragma unroll
        for (int w = 0; w < 8; ++w) {
          gs += red[w * 256 + tid];
          us += red[w * 256 + 128 + tid];
        }
        const float gv = rbf(gs), uv = rbf(us);
        const float hv = rbf(gv * __builtin_amdgcn_rcpf(1.0f + __expf(-gv))) * uv;
        hs[(tid & 1) * 64 + (tid >> 1)] = (u16)pk2(hv, hv);
      } else if (wave >= 3) {
        WAITV(0);
#pragma unroll
        for (int j = 0; j < 16; ++j) {
          wt[j >> 3][j & 7] = wd[j >> 3][j & 7];
          PIN(wt[j >> 3][j & 7]);
        }
        transp(wt[0]);
        transp(wt[1]);
      }
      WAITL0();
      BARRIER();
      if (wave == 2) res_wait(ws, smem, lane, tag, 1);
      if (wave >= 3) {
        const u32x4 h0 = *(const u32x4*)(hs + (c15 & 1) * 64 + g4 * 8), h1 = *(const u32x4*)(hs + (c15 & 1) * 64 + 32 + g4 * 8);
#pragma unroll
        for (int j = 0; j < 8; ++j) acc[j] = mfma16(h0, wt[0][j], (f32x4){0.f, 0.f, 0.f, 0.f});
#pragma unroll
        for (int j = 0; j < 8; ++j) acc[j] = mfma16(h1, wt[1][j], acc[j]);
        *(f32x4*)(red + 2048 + wave * 256 + c15 * 16 + g4 * 4) = pick(acc);
        const f32x4 y = *(const f32x4*)(red + 2048 + wave * 256 + lane * 4);
#pragma unroll
        for (int t = 0; t < 2; ++t)
          __builtin_amdgcn_raw_buffer_store_b128((u32x4){__float_as_uint(y[t]), tag, __float_as_uint(y[2 + t]), tag}, ryp,
                                                 ((f * 2 + t) * 640 + (wave - 3) * 128 + lane * 2) * 8, 0, 16);
      }
      WAITL0();
      BARRIER();
    }
    wo_dma(IC<3>{}, wave, 8, 25);
    if (wave == 0) wo_dma(IC<1>{}, 24, 8, 25);
    if (wave == 1) gate_dma(gate_off(lane));
    if (tid < 8) *(unsigned*)(smem + L_VAR + tid * 4) = 0u;
    if (wave == 2 || (f == 30 && wave == 3)) {
      const unsigned tp = __builtin_amdgcn_readfirstlane(*(const unsigned*)(smem + L_CAND2)), d = wave == 2 ? D1 : DK;
#pragma unroll 1
      for (unsigned spins = 0; spins < 128; ++spins) {
        const unsigned r = now(), c = (unsigned)__builtin_amdgcn_s_memtime();
        if ((r - tp) * (c - c0_) > d * (r - r0_ + 1)) break;
        __builtin_amdgcn_s_sleep(2);
      }
    }
    if (f == 30 && wave == 3) {
      const __amdgpu_buffer_rsrc_t ra = __builtin_amdgcn_make_buffer_rsrc((void*)(ws + WS_A), 0, 4096, 0x00020000);
#pragma unroll 1
      for (unsigned spins = 0; spins < SPIN_CAP; ++spins) {
        asm volatile("" ::: "memory");
        if (__all(__builtin_amdgcn_raw_buffer_load_b32(ra, (lane & 31) * 128, 0, 16) == tag)) break;
        __builtin_amdgcn_s_sleep(8);
      }
      if (lane == 0) __hip_atomic_store((gu32*)(ws + WS_E), tag, RLX_AGENT);
    }
    if (wave == 2) {
      const unsigned t_ = now() + D2;
      const u32x4 v = __builtin_amdgcn_raw_buffer_load_b128(rfl, flag_off(), 0, 16);
      *(u32x4*)(smem + L_FS + lane * 16) = (u32x4){(v[1] == tag ? v[0] : t_) + (ETA_F - ETA_G), tag, 0u, tag};
    }
    if (wave == 0) rows_job();
    WAITV(0);
    __builtin_amdgcn_s_waitcnt(0xC07F);
    BARRIER();
  }

  {
    const int m = wave & 3, kh = wave >> 2;
    const bool snd = kh != 0;
    const int oo = (rt2 * 4 + m) * 20480 + kh * 8192, ot = (rt2 * 4 + m) * 20480 + 16384 + kh * 2048;
    const int l16 = lane * 16;
    u32x4 fs = *(const u32x4*)(smem + L_FS + l16);
    const u32x4 kk = *(const u32x4*)(smem + L_VAR + 32);
    u32x2 g0_ = *(const u32x2*)(smem + glo);
    u32x4 yw[2][7];
    int yo, y6, yp, yx;
    auto y_prep = [&]() {
      int ly = lane;
      PIN(ly);
      const int cp = (ly & 15) < 10 ? ly & 15 : 0, col = m * 20 + cp * 2 + (ly >> 5);
      yo = (ly >> 4) * 71680 + (nt2 * 80 + m * 20 + cp * 2) * 8;
      y6 = ly >= 48 ? 0x7FFFFF00 : yo;
      yp = (((col >> 4) + 5 - (nt2 & 1)) % 5) * 1024 + (col & 12) * 64 + (col & 3) * 4;
      yx = L_XT + col * 2;
      PIN(yo); PIN(y6); PIN(yp); PIN(yx);
    };
    auto y_srd = [&](int tok) {
      char* wy = ws;
      asm volatile("" : "+s"(wy));
      return make_srd(wy + WS_YP + tok * 5120, 27 * 10240);
    };
    auto y_issue = [&](u32x4(&yv)[7], u32x4 srd, bool again, int j0 = 0, int j1 = 7) {
#pragma unroll
      for (int j = 0; j < 7; ++j) {
        if (j < j0 || j >= j1) continue;
        if (again) asm volatile("buffer_load_dwordx4 %0, %1, %2, %3 offen sc1" : "+v"(yv[j]) : "v"(j < 6 ? yo : y6), "s"(srd), "s"(j * 10240) : "memory");
        else asm volatile("buffer_load_dwordx4 %0, %1, %2, %3 offen sc1" : "=v"(yv[j]) : "v"(j < 6 ? yo : y6), "s"(srd), "s"(j * 10240) : "memory");
      }
    };
    auto y_done = [&](u32x4(&yv)[7], int tok, int row) {
      volatile LAS unsigned* fl = (volatile LAS unsigned*)(LAS char*)(smem + L_VAR + 16 + (row >> 4) * 4);
      volatile LAS float* pe = (volatile LAS float*)(LAS char*)(smem + yp + (row >> 4) * 5120 + (row & 15) * 16);
      unsigned f_, xv_;
      float omg, pv;
      auto rd = [&]() {
        f_ = *fl;
        omg = *(volatile LAS float*)(LAS char*)(smem + L_JUNK + row * 4);
        xv_ = *(volatile LAS u16*)(LAS char*)(smem + yx + row * 160);
        pv = *pe;
      };
      rd();
      const float rsel = *(const float*)(smem + L_JUNK + 256 + tok * 4);
      SB();
      unsigned lo = tag, hi = tag;
      float y[2] = {0.f, 0.f};
#pragma unroll
      for (int j = 0; j < 7; ++j) {
        PIN(yv[j]);
        if (j == 6 && g4 == 3) yv[j][1] = yv[j][3] = tag;
        lo = min(min(lo, yv[j][1]), yv[j][3]);
        hi = max(max(hi, yv[j][1]), yv[j][3]);
        y[0] += __uint_as_float(yv[j][0]);
        PIN(y[0]);
        y[1] += __uint_as_float(yv[j][2]);
        PIN(y[1]);
      }
      if (!__all(lo == hi)) return false;
      const auto a = __builtin_amdgcn_permlane32_swap(__float_as_uint(y[0]), __float_as_uint(y[1]), false, false);
      const float h = __uint_as_float(a[0]) + __uint_as_float(a[1]);
      const auto c = __builtin_amdgcn_permlane16_swap(__float_as_uint(h), __float_as_uint(h), false, false);
      const float r = rbf(rbf(__uint_as_float(c[0]) + __uint_as_float(c[1])) * rsel);
#pragma unroll 1
      for (unsigned spins = 0; spins < (SPIN_CAP << 6) && !__builtin_amdgcn_readfirstlane(f_); ++spins) rd();
      const float xv = bflo(xv_);
      *pe = pv + (rbf(xv + r) - xv) * omg;
      asm volatile("ds_add_u32 %0, %1" ::"v"(fl), "v"(1u) : "memory");
      return true;
    };
    const int l0 = __builtin_amdgcn_readfirstlane(kk[2]), l1 = __builtin_amdgcn_readfirstlane(kk[3]);
    unsigned ry = min((unsigned)l0, (unsigned)l1);
    unsigned need = 1u + ((l0 >> 4) == m ? 256u : 0u) + ((l1 >> 4) == m ? 256u : 0u);
    asm volatile("" : "+s"(ry), "+s"(need));
#define yjob (ry < 64u)
    if (wave == 7) {
      if (__builtin_expect((~__builtin_amdgcn_ballot_w64((fs[1] == tag) & (fs[3] == tag)) & 0xFFull) != 0, 0)) {
        flag_wait(lane >= 8 ? 0x7FFFFF00 : flag_off());
        WAITL0();
        if (lane == 0) *(volatile LAS unsigned*)(LAS char*)(smem + L_VAR + 8) = 1u;
      }
    }
    if (!snd) {
      dma16(adr[0], x, lds0 + L_XT + m * 2560);
      dma16(adr[1], x, lds0 + L_XT + m * 2560 + 1024);
      if (lane < 32) dma16(adr[2], x, lds0 + L_XT + m * 2560 + 2048);
    }
    SB();
    const char* wb = smem + L_WO + kh * 51200 + l16;
    u32x4 wq[WPD][5];
    auto wrd = [&](int k) {
#pragma unroll
      for (int f = 0; f < 5; ++f) wq[k % WPD][f] = *(const u32x4*)(wb + (k * 5 + f) * 1024);
    };
#pragma unroll
    for (int k = 0; k < NPRE; ++k) wrd(k);
    if (snd) {
#pragma unroll
      for (int k = NPRE; k < WPD; ++k) wrd(k);
    }
    if (__builtin_expect(!__all(g0_[1] == tag), 0)) {
      const __amdgpu_buffer_rsrc_t rg_ = __builtin_amdgcn_make_buffer_rsrc((void*)(ws + WS_K), 0, 36864, 0x00020000);
#pragma unroll 1
      for (unsigned spins = 0; spins < SPIN_CAP; ++spins) {
        asm volatile("" ::: "memory");
        g0_ = __builtin_amdgcn_raw_buffer_load_b64(rg_, gate_off(m * 16 + c15), 0, 16);
        if (__all(g0_[1] == tag)) break;
      }
    }
    const float gt = __uint_as_float(g0_[0]);
    SB();
    {
      const u64 inv = ~__builtin_amdgcn_ballot_w64((fs[1] == tag) & (fs[3] == tag));
      if (__builtin_expect((inv & rel) != 0, 0)) {
#pragma unroll 1
        for (unsigned spins = 0; spins < (SPIN_CAP << 6); ++spins) {
          if (__builtin_amdgcn_readfirstlane(*(volatile LAS unsigned*)(LAS char*)(smem + L_VAR + 8))) break;
          __builtin_amdgcn_s_sleep(1);
        }
        fs[0] = *(volatile LAS unsigned*)(LAS char*)(smem + L_FS + l16);
      }
      const unsigned tg = fs[0] + ETA_G;
      unsigned spins = 128;
#pragma unroll 1
      while ((__builtin_amdgcn_ballot_w64((int)(now() - tg) < 0) & rel) != 0 && --spins) {}
    }
    u32x4 of[8];
    u32x2 ofl[2], ofh[2];
    const u32x4 sro = make_srd(ws + WS_OF, S_LEN * DM * 2);
    int l8 = lane * 8;
    asm volatile("buffer_load_dwordx4 %0, %8, %9, %10 offen sc1\n\tbuffer_load_dwordx4 %1, %8, %9, %10 offen offset:1024 sc1\n\t"
                 "buffer_load_dwordx4 %2, %8, %9, %10 offen offset:2048 sc1\n\tbuffer_load_dwordx4 %3, %8, %9, %10 offen offset:3072 sc1\n\t"
                 "buffer_load_dwordx4 %4, %8, %9, %11 offen sc1\n\tbuffer_load_dwordx4 %5, %8, %9, %11 offen offset:1024 sc1\n\t"
                 "buffer_load_dwordx4 %6, %8, %9, %11 offen offset:2048 sc1\n\tbuffer_load_dwordx4 %7, %8, %9, %11 offen offset:3072 sc1"
                 : "=&v"(of[0]), "=&v"(of[1]), "=&v"(of[2]), "=&v"(of[3]), "=&v"(of[4]), "=&v"(of[5]), "=&v"(of[6]), "=&v"(of[7])
                 : "v"(l16), "s"(sro), "s"(oo), "s"(oo + 4096)
                 : "memory");
    asm volatile("buffer_load_dwordx2 %0, %4, %5, %6 offen sc1\n\tbuffer_load_dwordx2 %1, %4, %5, %6 offen offset:512 sc1\n\t"
                 "buffer_load_dwordx2 %2, %4, %5, %6 offen offset:1024 sc1\n\tbuffer_load_dwordx2 %3, %4, %5, %6 offen offset:1536 sc1"
                 : "=&v"(ofl[0]), "=&v"(ofh[0]), "=&v"(ofl[1]), "=&v"(ofh[1])
                 : "v"(l8), "s"(sro), "s"(ot)
                 : "memory");
    if (!snd) {
#pragma unroll
      for (int k = NPRE; k < WPD; ++k) wrd(k);
    }
    auto o_again = [&](int k) {
      if (k < 8)
        asm volatile("buffer_load_dwordx4 %0, %1, %2, %3 offen sc1" : "+v"(of[k]) : "v"(l16), "s"(sro), "s"(oo + k * 1024) : "memory");
      else
        asm volatile("buffer_load_dwordx2 %0, %2, %3, %4 offen sc1\n\tbuffer_load_dwordx2 %1, %2, %3, %4 offen offset:512 sc1"
                     : "+v"(ofl[k - 8]), "+v"(ofh[k - 8])
                     : "v"(l8), "s"(sro), "s"(ot + (k - 8) * 1024)
                     : "memory");
    };
    auto o_pin = [&](int k) {
      if (k < 8) PIN(of[k]);
      else asm volatile("" : "+v"(ofl[k - 8]), "+v"(ofh[k - 8]));
    };
    auto o_get = [&](int k) { return k < 8 ? of[k] : (u32x4){ofl[k - 8][0], ofl[k - 8][1], ofh[k - 8][0], ofh[k - 8][1]}; };
    auto o_bad = [&](int k) {
      const u32x4 v = o_get(k);
      return __any(max(max(v[0], v[1]), max(v[2], v[3])) == SENT);
    };
    int pa_ = m * 5120 + l16;
    PIN(pa_);
    volatile LAS unsigned* pfl = (volatile LAS unsigned*)(LAS char*)(smem + L_VAR + 16 + m * 4);
    u32x4 rout = make_srd(out, S_LEN * DM * 2);
    asm volatile("" : "+s"(rout));
    f32x4 ac2[5], pp[5];
    unsigned pf_ = 0;
    if (!snd) {
      u32x2 xb[5];
      if (__builtin_expect(yjob, 0)) y_prep();
      WAITV(12);
#pragma unroll
      for (int f = 0; f < 5; ++f) xb[f] = *(const u32x2*)(smem + L_XT + m * 2560 + c15 * 160 + g4 * 8 + ((nt2 & 1) ? (f + 1) % 5 : f) * 32);
      const float omg = 1.0f - gt;
      *(float*)(smem + L_JUNK + (m * 16 + c15) * 4) = omg;
#pragma unroll
      for (int f = 0; f < 5; ++f) {
        pp[f] = (f32x4){bflo(xb[f][0]), bfhi(xb[f][0]), bflo(xb[f][1]), bfhi(xb[f][1])} * omg;
        PIN(pp[f]);
      }
    }
#pragma unroll
    for (int f = 0; f < 5; ++f) ac2[f] = (f32x4){0.f, 0.f, 0.f, 0.f};
    __builtin_amdgcn_sched_barrier(0);
    constexpr int GS[5] = {0, 2, 5, 8, 10};
    auto vleft = [](int k) { return k < 8 ? 11 - k : k == 8 ? 2 : 0; };
    WAITV(10);
    o_pin(0);
    o_pin(1);
    bool bad = o_bad(0) | o_bad(1);
#pragma unroll
    for (int g = 0; g < 4; ++g) {
      if (__builtin_expect(bad, 0)) {
        WAITV(0);
#pragma unroll 1
        for (unsigned spins = 0; spins < SPIN_CAP; ++spins) {
          bool any = false;
#pragma unroll
          for (int k2 = GS[g]; k2 < 10; ++k2) {
            o_pin(k2);
            if (o_bad(k2)) {
              o_again(k2);
              if (k2 < GS[g + 1]) any = true;
            }
          }
          WAITV(0);
          if (!any) break;
        }
      }
      if (g == 3) break;
      unsigned mxv = 0;
      const int nm = (GS[g + 1] - GS[g]) * 5, nn = g < 3 ? GS[g + 2] - GS[g + 1] : 0;
#pragma unroll
      for (int i = 0; i < nm; ++i) {
        const int k = GS[g] + i / 5, f = i % 5;
        const int j = i - (nm - 2 - 2 * nn);
        const int kn = GS[g + 1] + j / 2;
        if (j >= 0 && j < 2 * nn && !(j & 1)) {
          WAITV_(vleft(kn));
          o_pin(kn);
          SB();
        }
        ac2[f] = mfma16(wq[k % WPD][f], o_get(k), ac2[f]);
        SB();
        if (k + WPD < 10) wq[k % WPD][f] = *(const u32x4*)(wb + ((k + WPD) * 5 + f) * 1024);
        if (j >= 0 && j < 2 * nn) {
          const u32x4 nv = o_get(kn);
          mxv = max(max(mxv, nv[(j & 1) * 2]), nv[(j & 1) * 2 + 1]);
        }
        if (nn && j == 2 * nn) bad = __any(mxv == SENT);
        if (g == 2 && i == PRD - 1) __builtin_amdgcn_s_waitcnt(0xC07F);
        if (g == 2 && i == PRD && snd) {
          pf_ = *pfl;
          asm volatile("" ::: "memory");
#pragma unroll
          for (int f = 0; f < 5; ++f) pp[f] = *(const f32x4*)(smem + pa_ + f * 1024);
        }
        SB();
      }
    }
    u32x4 o8 = o_get(8), o9 = o_get(9);
    PIN(o8); PIN(o9);
    auto mm = [&](int f) {
      ac2[f] = mfma16(wq[8 % WPD][f], o8, ac2[f]);
      SB();
      ac2[f] = mfma16(wq[9 % WPD][f], o9, ac2[f]);
      SB();
    };
    auto fin = [&](int f, int q) {
      pp[f][q] = __builtin_fmaf(ac2[f][q], gt, pp[f][q]);
      PIN(pp[f][q]);
    };
    if (!snd) {
      SB();
      auto wr = [&](int f) {
#pragma unroll
        for (int q = 0; q < 4; ++q) fin(f, q);
        *(f32x4*)(smem + pa_ + f * 1024) = pp[f];
        SB();
      };
      auto done = [&]() {
        asm volatile("" ::: "memory");
        *pfl = 1u;
      };
      if (__builtin_expect(yjob, 0)) {
        int tok = (unsigned)l0 >= 64u;
        const u32x4 sy = y_srd(tok);
        mm(0); y_issue(yw[0], sy, false, 0, 2); SB(); mm(1); y_issue(yw[0], sy, false, 2, 4); SB(); mm(2); y_issue(yw[0], sy, false, 4, 7); SB();
        if (max((unsigned)l0, (unsigned)l1) < 64u) y_issue(yw[1], y_srd(1), false);
        wr(0); mm(3); wr(1); mm(4); wr(2); wr(3); wr(4);
        done();
        WAITV(0);
#pragma unroll
        for (int j = 0; j < 7; ++j) PIN(yw[1][j]);
#pragma unroll 1
        for (unsigned spins = 0; spins < SPIN_CAP; ++spins) {
          if (y_done(yw[0], tok, tok ? l1 : l0)) {
            if (tok || (unsigned)l1 >= 64u) break;
            tok = 1;
#pragma unroll
            for (int j = 0; j < 7; ++j) yw[0][j] = yw[1][j];
          } else {
            y_issue(yw[0], y_srd(tok), true);
            WAITV(0);
          }
        }
      } else {
        mm(0); mm(1); mm(2);
        wr(0); mm(3); wr(1); mm(4);
        wr(2); wr(3); wr(4);
        done();
      }
    } else {
      SB();
      u32x2 r[5];
      auto cv = [&](int f) {
#pragma unroll
        for (int q = 0; q < 4; ++q) fin(f, q);
        r[f] = (u32x2){pk2(pp[f][0], pp[f][1]), pk2(pp[f][2], pp[f][3])};
        SB();
      };
      auto st = [&](int f) {
        const auto a = __builtin_amdgcn_permlane16_swap(r[f][0], r[f + 1][0], false, false);
        const auto c = __builtin_amdgcn_permlane16_swap(r[f][1], r[f + 1][1], false, false);
        asm volatile("buffer_store_dwordx4 %0, %1, %2, 0 offen offset:%3 sc1\n\ts_nop 1" ::"v"((u32x4){a[0], c[0], a[1], c[1]}), "v"(adr[0]), "s"(rout), "i"(f * 32) : "memory");
        SB();
      };
      if (__builtin_expect(__builtin_amdgcn_readfirstlane(pf_) != need, 0)) {
        mm(0); mm(1); mm(2); mm(3); mm(4);
#pragma unroll 1
        for (unsigned spins = 0; spins < (SPIN_CAP << 6); ++spins) {
          pf_ = *pfl;
          asm volatile("" ::: "memory");
#pragma unroll
          for (int f = 0; f < 5; ++f) pp[f] = *(const f32x4*)(smem + pa_ + f * 1024);
          asm volatile("" ::: "memory");
          if (__builtin_amdgcn_readfirstlane(pf_) == need) break;
        }
        cv(0); cv(1);
      } else {
        mm(0); mm(1); mm(2);
        cv(0); mm(3); cv(1); mm(4);
      }
      st(0); cv(2); cv(3); st(2); cv(4);
      asm volatile("buffer_store_dwordx2 %0, %1, %2, 0 offen sc1" ::"v"(r[4]), "v"(adr[1]), "s"(rout) : "memory");
    }
  }
}

extern "C" void kernel_launch(void* const* d_in, const int* in_sizes, int n_in, void* d_out, int out_size, void* d_ws,
                              size_t ws_size, hipStream_t stream) {
  kf<<<256, 512, 0, stream>>>((const u16*)d_in[0], (const u16*)d_in[1], (const u16*)d_in[2], (const u16*)d_in[4],
                              (const u16*)d_in[8], (const u16*)d_in[5], (const u16*)d_in[6], (const u16*)d_in[7],
                              (char*)d_ws, (u16*)d_out);
}
```
